```python
import torch

def _softmax(x, axis=-1):
    return torch.softmax(x, dim=axis)
def _gelu(x):
    return torch.nn.functional.gelu(x, approximate="tanh")
def _silu(x):
    return torch.nn.functional.silu(x)
def _sigmoid(x):
    return torch.sigmoid(x)
def _layernorm(x, axis=-1, eps=1e-5):
    m = x.mean(dim=axis, keepdim=True)
    v = x.var(dim=axis, keepdim=True, unbiased=False)
    return (x - m) / torch.sqrt(v + eps)
def _scatter_add(tgt, idx, src, axis=0):
    return tgt.clone().index_add_(axis, idx, src)
def _segment_sum(src, seg, n):
    out = torch.zeros((n,) + src.shape[1:], dtype=src.dtype)
    return out.index_add_(0, seg, src)
def _pad(x, pw):
    flat = tuple(v for pair in reversed(pw) for v in pair)
    return torch.nn.functional.pad(x, flat)

kernel_name = 'recurrent_scan__gqa_block_cached__glu_gated_bilinear__prompt'

def setup_inputs():

    _w = (lambda a: (lambda t: t.to(torch.bfloat16) if t.is_floating_point() else t)(torch.as_tensor(a)))
    def _A_setup():
        B, Sq, D = 1, 2048, 512
        Hq, Hkv, Sk, Dh = 32, 32, 2048, 128
        assert Hq % Hkv == 0, (Hq, Hkv)
        return {
            "x": _w((lambda *s: torch.randn(*s, dtype=torch.bfloat16) * 0.05)(B, Sq, D)),
            "k_cache": _w((lambda *s: torch.randn(*s, dtype=torch.bfloat16) * 1.6)(B, Hkv, Sk, Dh)),
            "v_cache": _w((lambda *s: torch.randn(*s, dtype=torch.bfloat16) * 1.6)(B, Hkv, Sk, Dh)),
            "wq": _w((lambda *s: torch.randn(*s, dtype=torch.bfloat16) * 1.6)(D, Hq * Dh) * (D ** -0.5)),
            "wo": _w((lambda *s: torch.randn(*s, dtype=torch.bfloat16) * 1.6)(Hq * Dh, D) * ((Hq * Dh) ** -0.5)),
        }
    _A_in = _A_setup()

    def _B_setup():
        B, S, D, F = 1, 2048, 512, 1376
        return {
            "x": _w((lambda *s: torch.randn(*s, dtype=torch.bfloat16) * 0.05)(B, S, D)),
            "w_gate": _w((lambda *s: torch.randn(*s, dtype=torch.bfloat16) * 0.2)(D, F) * (D ** -0.5)),
            "w_up": _w((lambda *s: torch.randn(*s, dtype=torch.bfloat16) * 0.2)(D, F) * (D ** -0.5)),
            "w_down": _w((lambda *s: torch.randn(*s, dtype=torch.bfloat16) * 0.2)(F, D) * (F ** -0.5)),
        }
    _B_in = _B_setup()

    return {
        "A_x": _A_in["x"],
        "A_k_cache": _A_in["k_cache"],
        "A_v_cache": _A_in["v_cache"],
        "A_wq": _A_in["wq"],
        "A_wo": _A_in["wo"],
        "B_x": _B_in["x"],
        "B_w_gate": _B_in["w_gate"],
        "B_w_up": _B_in["w_up"],
        "B_w_down": _B_in["w_down"],
    }

def reference(A_x, A_k_cache, A_v_cache, A_wq, A_wo, B_x, B_w_gate, B_w_up, B_w_down):
    def _A_ref(x, k_cache, v_cache, wq, wo):
        B, Sq, D = x.shape
        Hq, Dh = 32, 128
        Hkv = k_cache.shape[1]
        rep = Hq // Hkv
        q = (lambda x, *d: x.permute(*d))((x @ wq).reshape(B, Sq, Hq, Dh), 0, 2, 1, 3)
        k = (lambda x, n, axis: torch.repeat_interleave(x, n, dim=axis))(k_cache, rep, axis=1)
        v = (lambda x, n, axis: torch.repeat_interleave(x, n, dim=axis))(v_cache, rep, axis=1)
        scores = torch.einsum("bhqd,bhkd->bhqk", q, k) * (Dh ** -0.5)
        p = _softmax(scores, axis=-1)
        o = torch.einsum("bhqk,bhkd->bhqd", p, v)
        return (lambda x, *d: x.permute(*d))(o, 0, 2, 1, 3).reshape(B, Sq, Hq * Dh) @ wo

    def _B_ref(x, w_gate, w_up, w_down):
        g = torch.einsum("bsd,df->bsf", x, w_gate)
        u = torch.einsum("bsd,df->bsf", x, w_up)
        h = g * u
        return torch.einsum("bsf,fd->bsd", h, w_down)

    x = A_x
    xn = _layernorm(x, axis=-1)
    s = _A_ref(xn, A_k_cache, A_v_cache, A_wq, A_wo)
    g = _silu(_B_ref(xn, B_w_gate, B_w_up, B_w_down))
    return x + s * g
```

```cpp
#include <hip/hip_runtime.h>
#include <hip/hip_bf16.h>

#define FUSED_CLOBBERS "v1","v2","v3","v4","v5","v6","v7","v8","v9","v10","v11","v12","v13","v14","v15","v16","v17","v18","v19","v20","v21","v22","v23","v24","v25","v26","v27","v28","v29","v30","v31","v32","v33","v34","v35","v36","v37","v38","v39","v40","v41","v42","v43","v44","v45","v46","v47","v48","v49","v50","v51","v52","v53","v54","v55","v56","v57","v58","v59","v60","v61","v62","v63","v64","v65","v66","v67","v68","v69","v70","v71","v72","v73","v74","v75","v76","v77","v78","v79","v80","v81","v82","v83","v84","v85","v86","v87","v88","v89","v90","v91","v92","v93","v94","v95","v96","v97","v98","v99","v100","v101","v102","v103","v104","v105","v106","v107","v108","v109","v110","v111","v112","v113","v114","v115","v116","v117","v118","v119","v120","v121","v122","v123","v124","v125","v126","v127","v128","v129","v130","v131","v132","v133","v134","v135","v136","v137","v138","v139","v140","v141","v142","v143","v144","v145","v146","v147","v148","v149","v150","v151","v152","v153","v154","v155","v156","v157","v158","v159","v160","v161","v162","v163","v164","v165","v166","v167","v168","v169","v170","v171","v172","v173","v174","v175","v176","v177","v178","v179","v180","v181","v182","v183","v184","v185","v186","v187","v188","v189","v190","v191","v192","v193","v194","v195","v196","v197","v198","v199","v200","v201","v202","v203","v204","v205","v206","v207","v208","v209","v210","v211","v212","v213","v214","v215","v216","v217","v218","v219","v220","v221","v222","v223","v224","v225","v226","v227","v228","v229","v230","v231","v232","v233","v234","v235","v236","v237","v238","v239","v240","v241","v242","v243","v244","v245","v246","v247","v248","v249","v250","v251","v252","v253","v254","v255","a0","a1","a2","a3","a4","a5","a6","a7","a8","a9","a10","a11","a12","a13","a14","a15","a16","a17","a18","a19","a20","a21","a22","a23","a24","a25","a26","a27","a28","a29","a30","a31","a32","a33","a34","a35","a36","a37","a38","a39","a40","a41","a42","a43","a44","a45","a46","a47","a48","a49","a50","a51","a52","a53","a54","a55","a56","a57","a58","a59","a60","a61","a62","a63","a64","a65","a66","a67","a68","a69","a70","a71","a72","a73","a74","a75","a76","a77","a78","a79","a80","a81","a82","a83","a84","a85","a86","a87","a88","a89","a90","a91","a92","a93","a94","a95","a96","a97","a98","a99","a100","a101","a102","a103","a104","a105","a106","a107","a108","a109","a110","a111","a112","a113","a114","a115","a116","a117","a118","a119","a120","a121","a122","a123","a124","a125","a126","a127","a128","a129","a130","a131","a132","a133","a134","a135","a136","a137","a138","a139","a140","a141","a142","a143","a144","a145","a146","a147","a148","a149","a150","a151","a152","a153","a154","a155","a156","a157","a158","a159","a160","a161","a162","a163","a164","a165","a166","a167","a168","a169","a170","a171","a172","a173","a174","a175","a176","a177","a178","a179","a180","a181","a182","a183","a184","a185","a186","a187","a188","a189","a190","a191","a192","a193","a194","a195","a196","a197","a198","a199","a200","a201","a202","a203","a204","a205","a206","a207","a208","a209","a210","a211","a212","a213","a214","a215","a216","a217","a218","a219","a220","a221","a222","a223","a224","a225","a226","a227","a228","a229","a230","a231","a232","a233","a234","a235","a236","a237","a238","a239","a240","a241","a242","a243","a244","a245","a246","a247","a248","a249","a250","a251","a252","a253","a254","a255","s12","s13","s14","s15","s16","s17","s18","s19","s20","s21","s22","s23","s24","s25","s26","s27","s28","s29","s30","s31","s34","s35","s36","s37","s38","s39","s40","s41","s42","s43","s44","s45","s46","s47","s48","s49","s50","s51","s52","s53","s54","s55","s56","s57","s58","s59","s60","s61","s62","s63","s64","s65","s66","s67","s68","s69","s70","s71","s72","s73","s74","s75","s76","s77","s78","s79","s80","s81","s82","s83","s84","s85","s86","s87","s88","s89","s90","s91","s92","s93","s94","s95","s96","s97","s98","s99","vcc","scc","memory"
__global__ __launch_bounds__(256, 1) void fused_block(const void* x, const void* kc, const void* vc, const void* wq, const void* wg, const void* wu,
                                                      const void* wo, const void* wd, void* ws, void* out) {
  __shared__ __attribute__((aligned(16))) char lds[163840];
  asm volatile(
R"ASM(
.macro Ba d,a,o
ds_read_b128 a[\d:\d+3], v\a offset:\o
.endm
.macro Bv d,a,o
ds_read_b128 v[\d:\d+3], v\a offset:\o
.endm
.macro CV d,a,b
v_cvt_pk_bf16_f32 v\d, v\a, v\b
.endm
.macro Cvav d,a,b,c
v_mfma_f32_16x16x32_bf16 v[\d:\d+3], a[\a:\a+3], v[\b:\b+3], v[\c:\c+3]
.endm
.macro EX d
v_exp_f32 v\d, v\d
.endm
.macro Maaa d,a,b
v_mfma_f32_16x16x32_bf16 a[\d:\d+3], a[\a:\a+3], a[\b:\b+3], a[\d:\d+3]
.endm
.macro Maav d,a,b
v_mfma_f32_16x16x32_bf16 a[\d:\d+3], a[\a:\a+3], v[\b:\b+3], a[\d:\d+3]
.endm
.macro Mvaa d,a,b
v_mfma_f32_16x16x32_bf16 v[\d:\d+3], a[\a:\a+3], a[\b:\b+3], v[\d:\d+3]
.endm
.macro Mvav d,a,b
v_mfma_f32_16x16x32_bf16 v[\d:\d+3], a[\a:\a+3], v[\b:\b+3], v[\d:\d+3]
.endm
.macro Mvva d,a,b
v_mfma_f32_16x16x32_bf16 v[\d:\d+3], v[\a:\a+3], a[\b:\b+3], v[\d:\d+3]
.endm
.macro Mvvv d,a,b
v_mfma_f32_16x16x32_bf16 v[\d:\d+3], v[\a:\a+3], v[\b:\b+3], v[\d:\d+3]
.endm
.macro Ta d,a,o
ds_read_b64_tr_b16 a[\d:\d+1], v\a offset:\o
.endm
.macro Tv d,a,o
ds_read_b64_tr_b16 v[\d:\d+1], v\a offset:\o
.endm
.macro Zaaa d,a,b
v_mfma_f32_16x16x32_bf16 a[\d:\d+3], a[\a:\a+3], a[\b:\b+3], 0
.endm
.macro Zaav d,a,b
v_mfma_f32_16x16x32_bf16 a[\d:\d+3], a[\a:\a+3], v[\b:\b+3], 0
.endm
.macro Zvaa d,a,b
v_mfma_f32_16x16x32_bf16 v[\d:\d+3], a[\a:\a+3], a[\b:\b+3], 0
.endm
.macro Zvav d,a,b
v_mfma_f32_16x16x32_bf16 v[\d:\d+3], a[\a:\a+3], v[\b:\b+3], 0
.endm
.macro Zvva d,a,b
v_mfma_f32_16x16x32_bf16 v[\d:\d+3], v[\a:\a+3], a[\b:\b+3], 0
.endm
s_load_dwordx16 s[12:27], %0, 0
s_load_dwordx4 s[28:31], %0, 64
s_getpc_b64 s[54:55]
Lpc_%=:
v_and_b32 v1, 63, v0
v_lshlrev_b32 v54, 6, v1
global_load_dword a255, v54, s[54:55]
s_lshr_b32 s51, %1, 3
s_lshl_b32 s56, s51, 11
s_add_u32 s56, s56, 4096
v_and_b32 v54, 0x7ff, v54
v_add_u32 v54, s56, v54
v_min_u32 v54, Ldone_%=-Lpc_%=-4, v54
global_load_dword a255, v54, s[54:55]
v_lshrrev_b32 v247, 6, v0
s_and_b32 s50, %1, 7
v_readfirstlane_b32 s35, v247
s_lshl_b32 s50, s50, 2
s_and_b32 s40, s51, 3
s_add_u32 s40, s40, s50
s_lshr_b32 s41, s51, 2
v_and_b32 v240, 15, v1
v_lshrrev_b32 v241, 4, v1
s_lshl_b32 s72, s35, 10
s_lshl_b32 s73, s35, 14
s_add_u32 s73, s73, 16384
s_cmp_eq_u32 s35, 3
s_cselect_b32 s73, 131072, s73
v_lshrrev_b32 v244, 1, v247
v_lshl_or_b32 v244, v244, 2, v241
v_lshl_add_u32 v245, v247, 2, v241
v_lshlrev_b32 v246, 1, v244
v_xor_b32 v246, v246, v240
v_lshlrev_b32 v26, 13, v245
v_lshl_add_u32 v26, v246, 4, v26
v_and_b32 v249, 7, v1
v_xor_b32 v248, v241, v249
v_lshrrev_b32 v249, 3, v1
v_lshlrev_b32 v249, 10, v249
v_lshl_add_u32 v30, v248, 4, v249
v_xor_b32 v31, 0x2040, v30
v_lshrrev_b32 v248, 1, v246
v_mul_u32_u24 v249, 11, v248
v_lshrrev_b32 v249, 5, v249
s_min_u32 s57, s40, 28
s_mul_i32 s52, s57, 3
s_and_b32 s57, s57, 1
v_add_u32 v248, s57, v248
v_mul_u32_u24 v250, 11, v248
v_lshrrev_b32 v250, 5, v250
v_mul_u32_u24 v250, 3, v250
v_sub_u32 v250, v248, v250
v_add_u32 v250, s52, v250
v_min_u32 v250, 85, v250
s_lshr_b32 s91, s40, 2
s_lshl_b32 s90, s41, 1
s_add_u32 s91, s91, s90
s_and_b32 s91, s91, 7
s_lshl_b32 s90, s91, 1
s_lshl_b32 s56, s91, 6
v_add_u32 v251, s56, v245
v_mul_u32_u24 v251, 2752, v251
v_lshl_add_u32 v251, v250, 5, v251
v_cmp_eq_u32 vcc, 1, v249
v_and_b32 v249, 1, v1
v_lshl_add_u32 v251, v249, 4, v251
s_mov_b32 s70, 44032
s_mov_b32 s71, 0
s_lshl_b32 s50, s40, 8
s_lshl_b32 s51, s91, 19
s_add_u32 s50, s50, s51
s_lshl_b32 s53, s41, 18
s_lshl_b32 s51, s35, 16
s_add_u32 s51, s53, s51
s_lshl_b32 s53, s91, 7
s_add_u32 s51, s51, s53
s_sub_u32 s90, 15, s90
s_sub_u32 s91, 7, s91
s_waitcnt lgkmcnt(0)
s_add_u32 s18, s18, s50
s_addc_u32 s19, s19, 0
s_add_u32 s60, s18, 131072
s_addc_u32 s61, s19, 0
s_sub_u32 s90, s90, 1
s_cselect_b64 s[92:93], -15, 1
s_add_u32 m0, s72, 114688
s_nop 0
global_load_lds_dwordx4 v26, s[18:19]
s_lshl_b64 s[92:93], s[92:93], 18
s_add_u32 s18, s18, s92
s_addc_u32 s19, s19, s93
s_add_u32 m0, s72, 118784
s_nop 0
global_load_lds_dwordx4 v26, s[60:61]
s_add_u32 s60, s60, s92
s_addc_u32 s61, s61, s93
v_mov_b32 v248, s20
v_mov_b32 v249, s22
v_cndmask_b32 v252, v248, v249, vcc
v_mov_b32 v56, s21
v_mov_b32 v57, s23
v_cndmask_b32 v253, v56, v57, vcc
v_add_co_u32 v28, vcc, v252, v251
s_nop 1
v_addc_co_u32 v29, vcc, 0, v253, vcc
s_add_u32 s62, s12, s51
s_addc_u32 s63, s13, 0
s_add_u32 s64, s62, 16384
s_addc_u32 s65, s63, 0
s_add_u32 s66, s62, 32768
s_addc_u32 s67, s63, 0
s_add_u32 s68, s62, 49152
s_addc_u32 s69, s63, 0
s_sub_u32 s91, s91, 1
s_cselect_b64 s[20:21], -7, 1
s_add_u32 m0, s73, 0
s_nop 0
global_load_lds_dwordx4 v30, s[62:63]
s_lshl_b64 s[20:21], s[20:21], 7
s_add_u32 m0, s73, 1024
s_nop 0
global_load_lds_dwordx4 v31, s[62:63]
s_add_u32 s62, s62, s20
s_addc_u32 s63, s63, s21
s_add_u32 m0, s73, 2048
s_nop 0
global_load_lds_dwordx4 v30, s[64:65]
s_add_u32 m0, s73, 3072
s_nop 0
global_load_lds_dwordx4 v31, s[64:65]
s_add_u32 s64, s64, s20
s_addc_u32 s65, s65, s21
s_add_u32 m0, s73, 4096
s_nop 0
global_load_lds_dwordx4 v30, s[66:67]
s_add_u32 m0, s73, 5120
s_nop 0
global_load_lds_dwordx4 v31, s[66:67]
s_add_u32 s66, s66, s20
s_addc_u32 s67, s67, s21
s_add_u32 m0, s73, 6144
s_nop 0
global_load_lds_dwordx4 v30, s[68:69]
s_add_u32 m0, s73, 7168
s_nop 0
global_load_lds_dwordx4 v31, s[68:69]
)ASM"
R"ASM(
s_add_u32 s68, s68, s20
s_addc_u32 s69, s69, s21
s_mov_b32 s95, s93
s_add_u32 m0, s72, 122880
s_nop 0
global_load_lds_dwordx4 v[28:29], off
v_lshl_add_u64 v[28:29], v[28:29], 0, s[70:71]
s_cmp_eq_u32 s95, 0
s_cselect_b32 s94, s70, 4293602304
s_add_u32 m0, s72, 126976
s_nop 0
global_load_lds_dwordx4 v[28:29], off
v_lshl_add_u64 v[28:29], v[28:29], 0, s[94:95]
s_sub_u32 s90, s90, 1
s_cselect_b64 s[92:93], -15, 1
s_add_u32 m0, s72, 0
s_nop 0
global_load_lds_dwordx4 v26, s[18:19]
s_lshl_b64 s[92:93], s[92:93], 18
s_add_u32 s18, s18, s92
s_addc_u32 s19, s19, s93
s_add_u32 m0, s72, 4096
s_nop 0
global_load_lds_dwordx4 v26, s[60:61]
s_add_u32 s60, s60, s92
s_addc_u32 s61, s61, s93
s_mov_b32 s95, s93
s_add_u32 m0, s72, 8192
s_nop 0
global_load_lds_dwordx4 v[28:29], off
v_lshl_add_u64 v[28:29], v[28:29], 0, s[70:71]
s_cmp_eq_u32 s95, 0
s_cselect_b32 s94, s70, 4293602304
s_add_u32 m0, s72, 12288
s_nop 0
global_load_lds_dwordx4 v[28:29], off
v_lshl_add_u64 v[28:29], v[28:29], 0, s[94:95]
v_and_b32 v243, 3, v1
v_bfe_u32 v242, v1, 2, 2
v_mov_b32 v249, 41943040
global_load_dword a254, v249, s[28:29] offset:128 sc1
s_lshl_b32 s34, s35, 12
s_add_u32 s42, s34, 65536
s_lshl_b32 s50, s40, 19
s_add_u32 s14, s14, s50
s_addc_u32 s15, s15, 0
s_add_u32 s16, s16, s50
s_addc_u32 s17, s17, 0
v_lshl_add_u32 v254, v247, 4, v241
v_lshlrev_b32 v248, 8, v254
v_or_b32 v249, 0, v241
v_xor_b32 v249, v249, v240
v_lshl_add_u32 v6, v249, 4, v248
v_or_b32 v249, 4, v241
v_xor_b32 v249, v249, v240
v_lshl_add_u32 v7, v249, 4, v248
v_or_b32 v249, 8, v241
v_xor_b32 v249, v249, v240
v_lshl_add_u32 v8, v249, 4, v248
v_or_b32 v249, 12, v241
v_xor_b32 v249, v249, v240
v_lshl_add_u32 v9, v249, 4, v248
v_or_b32 v249, 0, v241
v_lshlrev_b32 v249, 1, v249
v_xor_b32 v249, v249, v240
v_lshl_add_u32 v10, v249, 4, v248
v_or_b32 v249, 4, v241
v_lshlrev_b32 v249, 1, v249
v_xor_b32 v249, v249, v240
v_lshl_add_u32 v11, v249, 4, v248
s_mov_b64 s[36:37], s[14:15]
s_mov_b64 s[38:39], s[16:17]
s_lshl_b32 s50, s41, 12
s_add_u32 s50, s50, 41943040
v_mov_b32 v248, s50
v_lshrrev_b32 v250, 2, v1
v_lshl_add_u32 v250, v250, 8, v248
v_lshl_add_u32 v250, v243, 2, v250
v_accvgpr_write_b32 a142, v250
v_and_b32 v248, 1, v241
v_lshl_or_b32 v248, v248, 2, v242
v_lshl_add_u32 v249, v241, 3, v242
v_lshlrev_b32 v249, 8, v249
v_lshl_add_u32 v249, v243, 3, v249
v_add_u32 v249, 65536, v249
v_xor_b32 v250, 0, v248
v_lshl_add_u32 v12, v250, 5, v249
v_xor_b32 v250, 1, v248
v_lshl_add_u32 v13, v250, 5, v249
v_xor_b32 v250, 2, v248
v_lshl_add_u32 v14, v250, 5, v249
v_xor_b32 v250, 3, v248
v_lshl_add_u32 v15, v250, 5, v249
v_xor_b32 v250, 4, v248
v_lshl_add_u32 v16, v250, 5, v249
v_xor_b32 v250, 5, v248
v_lshl_add_u32 v17, v250, 5, v249
v_xor_b32 v250, 6, v248
v_lshl_add_u32 v18, v250, 5, v249
v_xor_b32 v250, 7, v248
v_lshl_add_u32 v19, v250, 5, v249
v_or_b32 v250, 0, v247
v_xor_b32 v250, v250, v248
v_lshl_add_u32 v20, v250, 5, v249
v_or_b32 v250, 4, v247
v_xor_b32 v250, v250, v248
v_lshl_add_u32 v21, v250, 5, v249
v_or_b32 v250, 0, v247
v_xor_b32 v250, v250, v248
v_lshl_add_u32 v22, v250, 5, v249
v_add_u32 v22, 8192, v22
v_or_b32 v250, 4, v247
v_xor_b32 v250, v250, v248
v_lshl_add_u32 v23, v250, 5, v249
v_add_u32 v23, 8192, v23
v_or_b32 v250, 0, v241
v_lshrrev_b32 v251, 1, v240
v_xor_b32 v250, v250, v251
v_lshlrev_b32 v250, 4, v250
v_lshl_add_u32 v250, v240, 7, v250
v_add_u32 v24, s73, v250
v_or_b32 v250, 4, v241
v_lshrrev_b32 v251, 1, v240
v_xor_b32 v250, v250, v251
v_lshlrev_b32 v250, 4, v250
v_lshl_add_u32 v250, v240, 7, v250
v_add_u32 v25, s73, v250
v_lshlrev_b32 v2, 2, v0
v_add_u32 v2, 147456, v2
v_lshlrev_b32 v3, 2, v1
v_lshl_add_u32 v3, v243, 8, v3
v_add_u32 v3, 147456, v3
v_mov_b32 v4, 0
ds_write_b32 v2, v4 offset:0
ds_write_b32 v2, v4 offset:3072
v_mov_b32 v4, 1
s_mov_b32 s44, 0
s_mov_b32 s46, 0
v_mov_b32 v248, 0x3f803f80
v_accvgpr_write_b32 a208, v248
v_accvgpr_write_b32 a209, v248
v_accvgpr_write_b32 a210, v248
v_accvgpr_write_b32 a211, v248
v_accvgpr_write_b32 a160, -1
v_accvgpr_write_b32 a161, -1
v_accvgpr_write_b32 a162, -1
v_accvgpr_write_b32 a163, -1
v_lshlrev_b32 v27, 4, v1
v_lshrrev_b32 v248, 1, v241
v_and_b32 v249, 1, v241
v_lshl_or_b32 v248, v249, 1, v248
v_or_b32 v249, 0, v248
v_xor_b32 v249, v249, v240
v_lshlrev_b32 v249, 4, v249
v_lshl_add_u32 v250, v240, 8, v249
v_accvgpr_write_b32 a112, v250
v_or_b32 v249, 4, v248
v_xor_b32 v249, v249, v240
v_lshlrev_b32 v249, 4, v249
v_lshl_add_u32 v250, v240, 8, v249
v_accvgpr_write_b32 a113, v250
v_or_b32 v249, 8, v248
v_xor_b32 v249, v249, v240
v_lshlrev_b32 v249, 4, v249
v_lshl_add_u32 v250, v240, 8, v249
v_accvgpr_write_b32 a114, v250
v_or_b32 v249, 12, v248
v_xor_b32 v249, v249, v240
v_lshlrev_b32 v249, 4, v249
v_lshl_add_u32 v250, v240, 8, v249
v_accvgpr_write_b32 a115, v250
v_lshl_add_u32 v248, v241, 2, v242
v_and_b32 v249, 7, v248
v_lshlrev_b32 v250, 8, v248
v_lshl_add_u32 v250, v243, 3, v250
v_add_u32 v252, 65536, v250
v_xor_b32 v251, 0, v249
v_lshl_add_u32 v251, v251, 5, v252
v_accvgpr_write_b32 a116, v251
v_xor_b32 v251, 1, v249
v_lshl_add_u32 v251, v251, 5, v252
v_accvgpr_write_b32 a117, v251
v_xor_b32 v251, 2, v249
v_lshl_add_u32 v251, v251, 5, v252
v_accvgpr_write_b32 a118, v251
v_xor_b32 v251, 3, v249
v_lshl_add_u32 v251, v251, 5, v252
v_accvgpr_write_b32 a119, v251
v_xor_b32 v251, 4, v249
v_lshl_add_u32 v251, v251, 5, v252
v_accvgpr_write_b32 a120, v251
v_xor_b32 v251, 5, v249
v_lshl_add_u32 v251, v251, 5, v252
v_accvgpr_write_b32 a121, v251
v_xor_b32 v251, 6, v249
v_lshl_add_u32 v251, v251, 5, v252
v_accvgpr_write_b32 a122, v251
v_xor_b32 v251, 7, v249
v_lshl_add_u32 v251, v251, 5, v252
v_accvgpr_write_b32 a123, v251
)ASM"
R"ASM(
v_add_u32 v251, 0, v27
v_accvgpr_write_b32 a124, v251
v_add_u32 v251, 131072, v27
v_accvgpr_write_b32 a125, v251
v_add_u32 v251, 262144, v27
v_accvgpr_write_b32 a126, v251
v_add_u32 v251, 393216, v27
v_accvgpr_write_b32 a127, v251
s_bfe_u32 s86, s40, 0x20002
s_and_b32 s89, s40, 28
s_bitcmp1_b32 s40, 2
s_cselect_b32 s50, 3, 0
s_or_b32 s89, s89, s50
s_and_b32 s58, s40, 3
s_lshr_b32 s59, s40, 4
s_lshl_b32 s50, s41, 1
s_add_u32 s59, s59, s50
v_lshlrev_b32 v250, 7, v248
v_lshl_add_u32 v250, v243, 3, v250
v_bfe_u32 v249, v248, 1, 2
s_and_b32 s50, s86, 1
s_lshl_b32 s50, s50, 1
v_xor_b32 v249, s50, v249
v_xor_b32 v251, 0, v249
v_lshl_add_u32 v251, v251, 5, v250
v_accvgpr_write_b32 a128, v251
v_xor_b32 v251, 1, v249
v_lshl_add_u32 v251, v251, 5, v250
v_accvgpr_write_b32 a129, v251
v_xor_b32 v251, 2, v249
v_lshl_add_u32 v251, v251, 5, v250
v_accvgpr_write_b32 a130, v251
v_xor_b32 v251, 3, v249
v_lshl_add_u32 v251, v251, 5, v250
v_accvgpr_write_b32 a131, v251
v_lshrrev_b32 v250, 3, v1
v_lshl_add_u32 v250, v247, 5, v250
v_lshlrev_b32 v250, 10, v250
v_and_b32 v249, 7, v1
v_lshlrev_b32 v251, 1, v241
v_xor_b32 v249, v249, v251
v_lshl_add_u32 v250, v249, 4, v250
v_add_u32 v251, 0, v250
v_accvgpr_write_b32 a132, v251
v_add_u32 v251, 7168, v250
v_accvgpr_write_b32 a133, v251
v_add_u32 v251, 14336, v250
v_accvgpr_write_b32 a134, v251
v_add_u32 v251, 21504, v250
v_accvgpr_write_b32 a135, v251
v_add_u32 v251, 45056, v27
v_accvgpr_write_b32 a136, v251
s_lshl_b32 s56, s59, 3
s_lshl_b32 s50, s35, 1
s_add_u32 s56, s56, s50
s_lshl_b32 s53, s58, 8
s_lshl_b32 s51, s86, 6
s_add_u32 s51, s51, s53
s_add_u32 s26, s26, s53
s_addc_u32 s27, s27, 0
s_lshl_b32 s50, s56, 14
s_add_u32 s50, s50, s51
v_lshlrev_b32 v251, 10, v240
v_lshl_add_u32 v251, v241, 3, v251
v_add_u32 v251, s50, v251
v_accvgpr_write_b32 a137, v251
v_add_u32 v251, 16384, v251
v_accvgpr_write_b32 a138, v251
s_add_u32 s24, s24, s53
s_addc_u32 s25, s25, 0
s_lshl_b32 s87, s35, 5
s_sub_u32 s87, 1344, s87
s_lshl_b32 s50, s56, 17
s_add_u32 s80, s28, s50
s_addc_u32 s81, s29, 0
s_mul_i32 s50, s56, 45056
s_mul_i32 s51, s86, 11264
s_add_u32 s50, s50, s51
s_add_u32 s50, s50, 16777216
s_add_u32 s84, s28, s50
s_addc_u32 s85, s29, 0
s_lshl_b32 s50, s59, 2
s_add_u32 s50, s50, s58
s_lshl_b32 s50, s50, 18
s_lshl_b32 s51, s35, 14
s_add_u32 s50, s50, s51
s_add_u32 s50, s50, 25165824
s_add_u32 s82, s28, s50
s_addc_u32 s83, s29, 0
s_lshl_b32 s50, s41, 21
s_lshl_b32 s51, s35, 19
s_add_u32 s50, s50, s51
s_lshl_b32 s51, s40, 12
s_add_u32 s50, s50, s51
s_add_u32 s48, s28, s50
s_addc_u32 s49, s29, 0
s_lshl_b32 s50, s41, 2
s_add_u32 s50, s50, s35
s_mul_i32 s50, s50, 180224
s_add_u32 s50, s50, 16777216
s_add_u32 s51, s52, s57
s_lshl_b32 s51, s51, 9
s_add_u32 s50, s50, s51
s_add_u32 s74, s28, s50
s_addc_u32 s75, s29, 0
s_mul_i32 s51, s57, -2040
s_add_u32 s50, s50, s51
s_add_u32 s50, s50, 1024
s_add_u32 s76, s28, s50
s_addc_u32 s77, s29, 0
s_lshl_b32 s88, s35, 10
s_lshl_b32 s50, s41, 5
s_add_u32 s50, s50, s40
s_lshl_b32 s50, s50, 2
s_add_u32 s50, s50, s35
s_lshl_b32 s51, s50, 11
v_lshlrev_b32 v248, 5, v1
v_add_u32 v248, s51, v248
global_load_dwordx4 v[240:243], v248, s[12:13] offset:0
global_load_dwordx4 v[244:247], v248, s[12:13] offset:16
s_sub_u32 s91, s91, 1
s_cselect_b64 s[20:21], -7, 1
s_add_u32 m0, s73, 8192
s_nop 0
global_load_lds_dwordx4 v30, s[62:63]
s_lshl_b64 s[20:21], s[20:21], 7
s_add_u32 m0, s73, 9216
s_nop 0
global_load_lds_dwordx4 v31, s[62:63]
s_add_u32 s62, s62, s20
s_addc_u32 s63, s63, s21
s_add_u32 m0, s73, 10240
s_nop 0
global_load_lds_dwordx4 v30, s[64:65]
s_add_u32 m0, s73, 11264
s_nop 0
global_load_lds_dwordx4 v31, s[64:65]
s_add_u32 s64, s64, s20
s_addc_u32 s65, s65, s21
s_add_u32 m0, s73, 12288
s_nop 0
global_load_lds_dwordx4 v30, s[66:67]
s_add_u32 m0, s73, 13312
s_nop 0
global_load_lds_dwordx4 v31, s[66:67]
s_add_u32 s66, s66, s20
s_addc_u32 s67, s67, s21
s_add_u32 m0, s73, 14336
s_nop 0
global_load_lds_dwordx4 v30, s[68:69]
s_add_u32 m0, s73, 15360
s_nop 0
global_load_lds_dwordx4 v31, s[68:69]
s_add_u32 s68, s68, s20
s_addc_u32 s69, s69, s21
s_waitcnt vmcnt(15)
s_waitcnt lgkmcnt(0)
s_barrier
Ba 32,24,0
Ba 36,24,2048
Ba 40,24,4096
Ba 44,24,6144
Ta 0,12,57344
Ta 2,12,58368
Ta 4,15,57344
Ta 6,15,58368
Ta 8,13,57344
Ta 10,13,58368
Ta 12,16,57344
Ta 14,16,58368
Ba 48,25,0
Ba 52,25,2048
Ba 56,25,4096
Ba 60,25,6144
s_waitcnt lgkmcnt(10)
Zvaa 32,0,32
Ta 16,14,57344
Ta 18,14,58368
Zvaa 36,0,36
s_sub_u32 s90, s90, 1
s_cselect_b64 s[92:93], -15, 1
s_add_u32 m0, s72, 65536
Zvaa 40,0,40
global_load_lds_dwordx4 v26, s[18:19]
Zvaa 44,0,44
s_waitcnt lgkmcnt(10)
s_lshl_b64 s[92:93], s[92:93], 18
s_add_u32 s18, s18, s92
s_addc_u32 s19, s19, s93
Zvaa 80,4,32
Ta 20,17,57344
Ta 22,17,58368
Zvaa 84,4,36
Zvaa 88,4,40
Zvaa 92,4,44
s_waitcnt lgkmcnt(10)
Zvaa 48,8,32
Ta 24,12,49152
Ta 26,12,50176
Zvaa 52,8,36
)ASM"
R"ASM(
s_add_u32 m0, s72, 69632
Zvaa 56,8,40
global_load_lds_dwordx4 v26, s[60:61]
Zvaa 60,8,44
s_waitcnt lgkmcnt(10)
s_waitcnt lgkmcnt(6)
s_add_u32 s60, s60, s92
s_addc_u32 s61, s61, s93
Zvaa 96,12,32
Ta 28,13,49152
Ta 30,13,50176
Zvaa 100,12,36
s_sub_u32 s91, s91, 1
s_cselect_b64 s[20:21], -7, 1
s_add_u32 m0, s73, 0
Zvaa 104,12,40
global_load_lds_dwordx4 v30, s[62:63]
Zvaa 108,12,44
s_lshr_b32 s54, s41, 2
s_cmp_lg_u32 s54, 0
s_cbranch_scc1 Lnocs0_%=
Ta 176,20,49152
Ta 178,20,50176
Ta 180,21,49152
Ta 182,21,50176
Ta 184,22,49152
Ta 186,22,50176
Ta 188,23,49152
Ta 190,23,50176
s_waitcnt lgkmcnt(0)
Zaaa 96,208,176
Zaaa 100,208,180
Zaaa 104,208,184
Zaaa 108,208,188
Lnocs0_%=:
s_waitcnt lgkmcnt(6)
Zvaa 64,16,32
Ta 0,14,49152
Ta 2,14,50176
Zvaa 68,16,36
s_lshl_b64 s[20:21], s[20:21], 7
s_add_u32 m0, s73, 1024
Zvaa 72,16,40
global_load_lds_dwordx4 v31, s[62:63]
Zvaa 76,16,44
s_waitcnt lgkmcnt(6)
s_waitcnt vmcnt(15)
ds_write_b32 v2, v4 offset:0
s_add_u32 s62, s62, s20
s_addc_u32 s63, s63, s21
Zvaa 112,20,32
Ta 4,15,49152
Ta 6,15,50176
Zvaa 116,20,36
s_add_u32 s44, s44, 1
v_add_u32 v4, 1, v4
s_mov_b32 s95, s93
s_add_u32 m0, s72, 73728
Zvaa 120,20,40
global_load_lds_dwordx4 v[28:29], off
Zvaa 124,20,44
s_waitcnt lgkmcnt(7)
v_lshl_add_u64 v[28:29], v[28:29], 0, s[70:71]
Zvaa 128,24,32
Ta 8,16,49152
Ta 10,16,50176
Zvaa 132,24,36
s_add_u32 m0, s73, 2048
Zvaa 136,24,40
global_load_lds_dwordx4 v30, s[64:65]
Zvaa 140,24,44
s_waitcnt lgkmcnt(7)
Zvaa 144,28,32
Ta 12,17,49152
Ta 14,17,50176
Zvaa 148,28,36
s_cmp_eq_u32 s95, 0
s_cselect_b32 s94, s70, 4293602304
s_add_u32 m0, s72, 77824
Zvaa 152,28,40
global_load_lds_dwordx4 v[28:29], off
Zvaa 156,28,44
s_waitcnt lgkmcnt(7)
v_lshl_add_u64 v[28:29], v[28:29], 0, s[94:95]
Zvaa 160,0,32
Ta 16,18,49152
Ta 18,18,50176
Zvaa 164,0,36
v_add_u32 v12, 4294901760, v12
v_add_u32 v15, 4294901760, v15
s_add_u32 m0, s73, 3072
Zvaa 168,0,40
global_load_lds_dwordx4 v31, s[64:65]
Zvaa 172,0,44
v_add_u32 v13, 4294901760, v13
v_add_u32 v16, 4294901760, v16
s_waitcnt lgkmcnt(6)
ds_read_b32 v5, v3 offset:0
s_add_u32 s64, s64, s20
s_addc_u32 s65, s65, s21
Zvaa 176,4,32
Ta 20,19,49152
Ta 22,19,50176
Zvaa 180,4,36
v_add_u32 v20, 4294901760, v20
v_add_u32 v21, 4294901760, v21
s_waitcnt lgkmcnt(2)
v_cmp_gt_u32 vcc, s44, v5
Zvaa 184,4,40
Zvaa 188,4,44
s_cbranch_vccnz Lspin300_%=
Lback300_%=:
Zvaa 192,8,32
Ta 24,12,8192
Ta 26,12,9216
Zvaa 196,8,36
v_add_u32 v22, 4294901760, v22
v_add_u32 v23, 4294901760, v23
s_sub_u32 s90, s90, 1
s_cselect_b64 s[92:93], -15, 1
s_add_u32 m0, s72, 81920
Zvaa 200,8,40
global_load_lds_dwordx4 v26, s[18:19]
Zvaa 204,8,44
v_add_u32 v14, 4294901760, v14
v_add_u32 v17, 4294901760, v17
s_lshl_b64 s[92:93], s[92:93], 18
s_add_u32 s18, s18, s92
s_addc_u32 s19, s19, s93
Zvaa 208,12,32
Ta 28,15,8192
Ta 30,15,9216
Zvaa 212,12,36
s_add_u32 m0, s72, 86016
Zvaa 216,12,40
global_load_lds_dwordx4 v26, s[60:61]
Zvaa 220,12,44
v_add_u32 v18, 4294901760, v18
v_add_u32 v19, 4294901760, v19
s_add_u32 s60, s60, s92
s_addc_u32 s61, s61, s93
Zvaa 224,16,32
Ta 0,13,8192
Ta 2,13,9216
Zvaa 228,16,36
s_mov_b32 s95, s93
s_add_u32 m0, s72, 90112
Zvaa 232,16,40
global_load_lds_dwordx4 v[28:29], off
Zvaa 236,16,44
s_waitcnt lgkmcnt(6)
v_lshl_add_u64 v[28:29], v[28:29], 0, s[70:71]
Zaaa 144,20,32
Ta 4,16,8192
Ta 6,16,9216
Zaaa 148,20,36
s_cmp_eq_u32 s95, 0
s_cselect_b32 s94, s70, 4293602304
s_add_u32 m0, s72, 94208
Zaaa 152,20,40
global_load_lds_dwordx4 v[28:29], off
Zaaa 156,20,44
s_waitcnt lgkmcnt(6)
v_lshl_add_u64 v[28:29], v[28:29], 0, s[94:95]
Mvaa 32,24,48
Ta 8,14,8192
Ta 10,14,9216
Mvaa 36,24,52
s_sub_u32 s90, s90, 1
s_cselect_b64 s[92:93], -15, 1
s_add_u32 m0, s72, 98304
s_waitcnt vmcnt(20)
Mvaa 40,24,56
global_load_lds_dwordx4 v26, s[18:19]
Mvaa 44,24,60
s_waitcnt lgkmcnt(6)
s_lshl_b64 s[92:93], s[92:93], 18
s_add_u32 s18, s18, s92
s_addc_u32 s19, s19, s93
v_accvgpr_read_b32 v250, a254
Mvaa 80,28,48
Ta 12,17,8192
Ta 14,17,9216
Mvaa 84,28,52
v_lshlrev_b32 v248, 16, v240
v_and_b32 v249, 0xffff0000, v240
Mvaa 88,28,56
v_readfirstlane_b32 s47, v250
v_add_f32 v252, v248, v249
Mvaa 92,28,60
s_waitcnt lgkmcnt(6)
v_mul_f32 v254, v248, v248
Mvaa 48,0,48
Ta 16,12,0
Ta 18,12,1024
Mvaa 52,0,52
s_add_u32 m0, s72, 102400
v_fmac_f32 v254, v249, v249
Mvaa 56,0,56
global_load_lds_dwordx4 v26, s[60:61]
)ASM"
R"ASM(
Mvaa 60,0,60
s_waitcnt lgkmcnt(6)
s_add_u32 s60, s60, s92
s_addc_u32 s61, s61, s93
v_lshlrev_b32 v248, 16, v241
Mvaa 96,4,48
Ta 20,13,0
Ta 22,13,1024
Mvaa 100,4,52
s_add_u32 m0, s73, 4096
v_and_b32 v249, 0xffff0000, v241
Mvaa 104,4,56
global_load_lds_dwordx4 v30, s[66:67]
Mvaa 108,4,60
s_lshr_b32 s54, s41, 2
s_cmp_lg_u32 s54, 0
s_cbranch_scc1 Lnocs1_%=
Ta 176,20,0
Ta 178,20,1024
Ta 180,21,0
Ta 182,21,1024
Ta 184,22,0
Ta 186,22,1024
Ta 188,23,0
Ta 190,23,1024
s_waitcnt lgkmcnt(0)
Maaa 96,208,176
Maaa 100,208,180
Maaa 104,208,184
Maaa 108,208,188
Lnocs1_%=:
s_waitcnt lgkmcnt(6)
v_add_f32 v252, v252, v248
Mvaa 64,8,48
Ta 24,14,0
Ta 26,14,1024
Mvaa 68,8,52
s_add_u32 m0, s73, 5120
v_fmac_f32 v254, v248, v248
Mvaa 72,8,56
global_load_lds_dwordx4 v31, s[66:67]
Mvaa 76,8,60
s_waitcnt lgkmcnt(6)
s_waitcnt vmcnt(9)
ds_write_b32 v2, v4 offset:0
s_add_u32 s66, s66, s20
s_addc_u32 s67, s67, s21
v_add_f32 v252, v252, v249
Mvaa 112,12,48
Ta 28,15,0
Ta 30,15,1024
Mvaa 116,12,52
s_add_u32 s44, s44, 1
v_add_u32 v4, 1, v4
v_fmac_f32 v254, v249, v249
Mvaa 120,12,56
Ba 64,24,8192
Mvaa 124,12,60
s_waitcnt lgkmcnt(8)
v_lshlrev_b32 v248, 16, v242
Mvaa 128,16,48
Ta 0,16,0
Ta 2,16,1024
Mvaa 132,16,52
s_add_u32 m0, s73, 6144
v_and_b32 v249, 0xffff0000, v242
Mvaa 136,16,56
global_load_lds_dwordx4 v30, s[68:69]
Mvaa 140,16,60
Ba 80,25,8192
s_waitcnt lgkmcnt(9)
v_add_f32 v252, v252, v248
Mvaa 144,20,48
Ta 4,17,0
Ta 6,17,1024
Mvaa 148,20,52
Ba 84,25,10240
v_fmac_f32 v254, v248, v248
Mvaa 152,20,56
Ba 68,24,10240
Mvaa 156,20,60
s_waitcnt lgkmcnt(11)
v_add_f32 v252, v252, v249
Mvaa 160,24,48
Ta 8,18,0
Ta 10,18,1024
Mvaa 164,24,52
v_add_u32 v12, 65536, v12
v_add_u32 v15, 65536, v15
s_add_u32 m0, s73, 7168
v_fmac_f32 v254, v249, v249
Mvaa 168,24,56
global_load_lds_dwordx4 v31, s[68:69]
Mvaa 172,24,60
v_add_u32 v13, 65536, v13
v_add_u32 v16, 65536, v16
s_waitcnt lgkmcnt(10)
ds_read_b32 v5, v3 offset:0
s_add_u32 s68, s68, s20
s_addc_u32 s69, s69, s21
v_lshlrev_b32 v248, 16, v243
Mvaa 176,28,48
Ta 12,19,0
Ta 14,19,1024
Mvaa 180,28,52
Ba 88,25,12288
v_add_u32 v20, 65536, v20
v_add_u32 v21, 65536, v21
s_waitcnt lgkmcnt(3)
v_cmp_gt_u32 vcc, s44, v5
Mvaa 184,28,56
Mvaa 188,28,60
s_cbranch_vccnz Lspin301_%=
Lback301_%=:
Mvaa 192,0,48
Ta 16,12,8192
Ta 18,12,9216
Mvaa 196,0,52
v_add_u32 v22, 65536, v22
v_add_u32 v23, 65536, v23
s_mov_b32 s95, s93
s_add_u32 m0, s72, 106496
v_and_b32 v249, 0xffff0000, v243
Mvaa 200,0,56
global_load_lds_dwordx4 v[28:29], off
Mvaa 204,0,60
v_add_u32 v14, 65536, v14
v_add_u32 v17, 65536, v17
v_lshl_add_u64 v[28:29], v[28:29], 0, s[70:71]
v_add_f32 v252, v252, v248
Mvaa 208,4,48
Ta 20,15,8192
Ta 22,15,9216
Mvaa 212,4,52
Ba 72,24,12288
v_fmac_f32 v254, v248, v248
Mvaa 216,4,56
Ba 76,24,14336
Mvaa 220,4,60
Ba 92,25,14336
v_add_u32 v18, 65536, v18
v_add_u32 v19, 65536, v19
v_add_f32 v252, v252, v249
Mvaa 224,8,48
Ta 24,13,8192
Ta 26,13,9216
Mvaa 228,8,52
s_cmp_eq_u32 s95, 0
s_cselect_b32 s94, s70, 4293602304
s_add_u32 m0, s72, 110592
v_fmac_f32 v254, v249, v249
Mvaa 232,8,56
global_load_lds_dwordx4 v[28:29], off
Mvaa 236,8,60
s_waitcnt lgkmcnt(10)
v_lshl_add_u64 v[28:29], v[28:29], 0, s[94:95]
v_lshlrev_b32 v248, 16, v244
Maaa 144,12,48
Ta 28,16,8192
Ta 30,16,9216
Maaa 148,12,52
s_sub_u32 s90, s90, 1
s_cselect_b64 s[92:93], -15, 1
s_add_u32 m0, s72, 114688
v_and_b32 v249, 0xffff0000, v244
Maaa 152,12,56
global_load_lds_dwordx4 v26, s[18:19]
Maaa 156,12,60
s_waitcnt lgkmcnt(4)
s_lshl_b64 s[92:93], s[92:93], 18
s_add_u32 s18, s18, s92
s_addc_u32 s19, s19, s93
v_add_f32 v252, v252, v248
Mvaa 32,16,64
Ta 0,14,8192
Ta 2,14,9216
Mvaa 36,16,68
s_add_u32 m0, s72, 118784
v_fmac_f32 v254, v248, v248
Mvaa 40,16,72
global_load_lds_dwordx4 v26, s[60:61]
Mvaa 44,16,76
s_add_u32 s60, s60, s92
s_addc_u32 s61, s61, s93
v_add_f32 v252, v252, v249
Mvaa 80,20,64
Ta 4,17,8192
Ta 6,17,9216
Mvaa 84,20,68
v_fmac_f32 v254, v249, v249
v_lshlrev_b32 v248, 16, v245
Mvaa 88,20,72
v_and_b32 v249, 0xffff0000, v245
v_add_f32 v252, v252, v248
Mvaa 92,20,76
s_waitcnt lgkmcnt(6)
v_fmac_f32 v254, v248, v248
Mvaa 48,24,64
Ta 8,12,0
Ta 10,12,1024
)ASM"
R"ASM(
Mvaa 52,24,68
s_mov_b32 s95, s93
s_add_u32 m0, s72, 122880
v_add_f32 v252, v252, v249
Mvaa 56,24,72
global_load_lds_dwordx4 v[28:29], off
Mvaa 60,24,76
s_waitcnt lgkmcnt(6)
v_lshl_add_u64 v[28:29], v[28:29], 0, s[70:71]
v_fmac_f32 v254, v249, v249
Mvaa 96,28,64
Ta 12,13,0
Ta 14,13,1024
Mvaa 100,28,68
s_sub_u32 s91, s91, 1
s_cselect_b64 s[20:21], -7, 1
s_add_u32 m0, s73, 8192
v_lshlrev_b32 v248, 16, v246
Mvaa 104,28,72
global_load_lds_dwordx4 v30, s[62:63]
Mvaa 108,28,76
s_lshr_b32 s54, s41, 2
s_cmp_lg_u32 s54, 1
s_cbranch_scc1 Lnocs2_%=
Ta 176,20,0
Ta 178,20,1024
Ta 180,21,0
Ta 182,21,1024
Ta 184,22,0
Ta 186,22,1024
Ta 188,23,0
Ta 190,23,1024
s_waitcnt lgkmcnt(0)
Zaaa 96,208,176
Zaaa 100,208,180
Zaaa 104,208,184
Zaaa 108,208,188
Lnocs2_%=:
s_waitcnt lgkmcnt(6)
v_and_b32 v249, 0xffff0000, v246
Mvaa 64,0,64
Ta 16,14,0
Ta 18,14,1024
Mvaa 68,0,68
s_lshl_b64 s[20:21], s[20:21], 7
s_add_u32 m0, s73, 9216
v_add_f32 v252, v252, v248
Mvaa 72,0,72
global_load_lds_dwordx4 v31, s[62:63]
Mvaa 76,0,76
s_waitcnt lgkmcnt(6)
s_waitcnt vmcnt(13)
ds_write_b32 v2, v4 offset:0
s_add_u32 s62, s62, s20
s_addc_u32 s63, s63, s21
v_fmac_f32 v254, v248, v248
Mvaa 112,4,64
Ta 20,15,0
Ta 22,15,1024
Mvaa 116,4,68
s_add_u32 s44, s44, 1
v_add_u32 v4, 1, v4
s_cmp_eq_u32 s95, 0
s_cselect_b32 s94, s70, 4293602304
s_add_u32 m0, s72, 126976
v_add_f32 v252, v252, v249
Mvaa 120,4,72
global_load_lds_dwordx4 v[28:29], off
Mvaa 124,4,76
s_waitcnt lgkmcnt(7)
v_lshl_add_u64 v[28:29], v[28:29], 0, s[94:95]
v_fmac_f32 v254, v249, v249
Mvaa 128,8,64
Ta 24,16,0
Ta 26,16,1024
Mvaa 132,8,68
s_add_u32 m0, s73, 10240
v_lshlrev_b32 v248, 16, v247
Mvaa 136,8,72
global_load_lds_dwordx4 v30, s[64:65]
Mvaa 140,8,76
s_waitcnt lgkmcnt(7)
v_and_b32 v249, 0xffff0000, v247
Mvaa 144,12,64
Ta 28,17,0
Ta 30,17,1024
Mvaa 148,12,68
v_add_f32 v252, v252, v248
v_fmac_f32 v254, v248, v248
Mvaa 152,12,72
v_add_f32 v252, v252, v249
v_fmac_f32 v254, v249, v249
Mvaa 156,12,76
s_waitcnt lgkmcnt(7)
s_mul_i32 s47, s47, 0x0019660d
s_add_u32 s47, s47, 0x3c6ef35f
Mvaa 160,16,64
Ta 0,18,0
Ta 2,18,1024
Mvaa 164,16,68
s_add_u32 m0, s73, 11264
v_add_f32_dpp v252, v252, v252 row_ror:8 row_mask:0xf bank_mask:0xf
Mvaa 168,16,72
global_load_lds_dwordx4 v31, s[64:65]
Mvaa 172,16,76
s_waitcnt lgkmcnt(6)
ds_read_b32 v5, v3 offset:0
s_add_u32 s64, s64, s20
s_addc_u32 s65, s65, s21
v_add_f32_dpp v254, v254, v254 row_ror:8 row_mask:0xf bank_mask:0xf
Mvaa 176,20,64
Ta 4,19,0
Ta 6,19,1024
Mvaa 180,20,68
s_waitcnt lgkmcnt(2)
v_cmp_gt_u32 vcc, s44, v5
Mvaa 184,20,72
Mvaa 188,20,76
s_cbranch_vccnz Lspin302_%=
Lback302_%=:
Mvaa 192,24,64
Ta 8,12,24576
Ta 10,12,25600
Mvaa 196,24,68
s_sub_u32 s90, s90, 1
s_cselect_b64 s[92:93], -15, 1
s_add_u32 m0, s72, 0
v_add_f32_dpp v252, v252, v252 row_ror:4 row_mask:0xf bank_mask:0xf
Mvaa 200,24,72
global_load_lds_dwordx4 v26, s[18:19]
Mvaa 204,24,76
s_lshl_b64 s[92:93], s[92:93], 18
s_add_u32 s18, s18, s92
s_addc_u32 s19, s19, s93
v_add_f32_dpp v254, v254, v254 row_ror:4 row_mask:0xf bank_mask:0xf
Mvaa 208,28,64
Ta 12,15,24576
Ta 14,15,25600
Mvaa 212,28,68
s_add_u32 m0, s72, 4096
v_add_f32_dpp v252, v252, v252 row_ror:2 row_mask:0xf bank_mask:0xf
Mvaa 216,28,72
global_load_lds_dwordx4 v26, s[60:61]
Mvaa 220,28,76
s_add_u32 s60, s60, s92
s_addc_u32 s61, s61, s93
v_add_f32_dpp v254, v254, v254 row_ror:2 row_mask:0xf bank_mask:0xf
Mvaa 224,0,64
Ta 16,13,24576
Ta 18,13,25600
Mvaa 228,0,68
s_mov_b32 s95, s93
s_add_u32 m0, s72, 8192
v_add_f32_dpp v252, v252, v252 row_ror:1 row_mask:0xf bank_mask:0xf
Mvaa 232,0,72
global_load_lds_dwordx4 v[28:29], off
Mvaa 236,0,76
s_waitcnt lgkmcnt(6)
v_lshl_add_u64 v[28:29], v[28:29], 0, s[70:71]
v_add_f32_dpp v254, v254, v254 row_ror:1 row_mask:0xf bank_mask:0xf
Maaa 144,4,64
Ta 20,16,24576
Ta 22,16,25600
Maaa 148,4,68
s_cmp_eq_u32 s95, 0
s_cselect_b32 s94, s70, 4293602304
s_add_u32 m0, s72, 12288
v_mov_b32 v250, v252
Maaa 152,4,72
global_load_lds_dwordx4 v[28:29], off
Maaa 156,4,76
s_waitcnt lgkmcnt(6)
v_lshl_add_u64 v[28:29], v[28:29], 0, s[94:95]
v_mov_b32 v251, v254
Mvaa 32,8,80
Ta 24,14,24576
Ta 26,14,25600
Mvaa 36,8,84
v_mov_b32 v253, s47
v_mov_b32 v255, s47
Mvaa 40,8,88
v_permlane16_swap_b32 v252, v250
v_permlane16_swap_b32 v254, v251
Mvaa 44,8,92
s_waitcnt lgkmcnt(6)
v_add_f32 v252, v252, v250
Mvaa 80,12,80
Ta 28,17,24576
Ta 30,17,25600
Mvaa 84,12,84
v_add_f32 v254, v254, v251
v_mul_f32 v252, 0xbb000000, v252
Mvaa 88,12,88
v_mul_f32 v254, 0x3b000000, v254
v_fma_f32 v254, -v252, v252, v254
Mvaa 92,12,92
s_waitcnt lgkmcnt(6)
v_max_f32 v254, 0, v254
Mvaa 48,16,80
Ta 0,12,16384
)ASM"
R"ASM(
Ta 2,12,17408
Mvaa 52,16,84
v_add_f32 v254, 0x3727c5ac, v254
s_lshl_b32 s50, s50, 5
s_add_u32 s50, s50, 42008576
Mvaa 56,16,88
v_lshrrev_b32 v248, 1, v1
v_add_u32 v248, s50, v248
Mvaa 60,16,92
s_waitcnt lgkmcnt(6)
s_mov_b32 exec_lo, 1
s_mov_b32 exec_hi, 1
global_store_dwordx2 v248, v[252:253], s[28:29] offset:0 sc1
global_store_dwordx2 v248, v[254:255], s[28:29] offset:8 sc1
s_mov_b64 exec, -1
Mvaa 96,20,80
Ta 4,13,16384
Ta 6,13,17408
Mvaa 100,20,84
s_add_u32 m0, s73, 12288
Mvaa 104,20,88
global_load_lds_dwordx4 v30, s[66:67]
Mvaa 108,20,92
s_lshr_b32 s54, s41, 2
s_cmp_lg_u32 s54, 1
s_cbranch_scc1 Lnocs3_%=
Ta 176,20,16384
Ta 178,20,17408
Ta 180,21,16384
Ta 182,21,17408
Ta 184,22,16384
Ta 186,22,17408
Ta 188,23,16384
Ta 190,23,17408
s_waitcnt lgkmcnt(0)
Maaa 96,208,176
Maaa 100,208,180
Maaa 104,208,184
Maaa 108,208,188
Lnocs3_%=:
s_waitcnt lgkmcnt(6)
Mvaa 64,24,80
Ta 8,14,16384
Ta 10,14,17408
Mvaa 68,24,84
s_add_u32 m0, s73, 13312
Mvaa 72,24,88
global_load_lds_dwordx4 v31, s[66:67]
Mvaa 76,24,92
s_waitcnt lgkmcnt(6)
s_waitcnt vmcnt(16)
ds_write_b32 v2, v4 offset:0
s_add_u32 s66, s66, s20
s_addc_u32 s67, s67, s21
Mvaa 112,28,80
Ta 12,15,16384
Ta 14,15,17408
Mvaa 116,28,84
s_add_u32 s44, s44, 1
v_add_u32 v4, 1, v4
Mvaa 120,28,88
Ba 32,24,0
Mvaa 124,28,92
s_waitcnt lgkmcnt(8)
Mvaa 128,0,80
Ta 16,16,16384
Ta 18,16,17408
Mvaa 132,0,84
s_add_u32 m0, s73, 14336
Mvaa 136,0,88
global_load_lds_dwordx4 v30, s[68:69]
Mvaa 140,0,92
Ba 48,25,0
s_waitcnt lgkmcnt(9)
Mvaa 144,4,80
Ta 20,17,16384
Ta 22,17,17408
Mvaa 148,4,84
Ba 52,25,2048
Mvaa 152,4,88
Ba 36,24,2048
Mvaa 156,4,92
s_waitcnt lgkmcnt(11)
Mvaa 160,8,80
Ta 24,18,16384
Ta 26,18,17408
Mvaa 164,8,84
s_add_u32 m0, s73, 15360
Mvaa 168,8,88
global_load_lds_dwordx4 v31, s[68:69]
Mvaa 172,8,92
s_waitcnt lgkmcnt(10)
ds_read_b32 v5, v3 offset:0
s_add_u32 s68, s68, s20
s_addc_u32 s69, s69, s21
Mvaa 176,12,80
Ta 28,19,16384
Ta 30,19,17408
Mvaa 180,12,84
Ba 56,25,4096
s_waitcnt lgkmcnt(3)
v_cmp_gt_u32 vcc, s44, v5
Mvaa 184,12,88
Mvaa 188,12,92
s_cbranch_vccnz Lspin303_%=
Lback303_%=:
Mvaa 192,16,80
Ta 0,12,40960
Ta 2,12,41984
Mvaa 196,16,84
s_sub_u32 s90, s90, 1
s_cselect_b64 s[92:93], -15, 1
s_add_u32 m0, s72, 65536
Mvaa 200,16,88
global_load_lds_dwordx4 v26, s[18:19]
Mvaa 204,16,92
s_lshl_b64 s[92:93], s[92:93], 18
s_add_u32 s18, s18, s92
s_addc_u32 s19, s19, s93
Mvaa 208,20,80
Ta 4,15,40960
Ta 6,15,41984
Mvaa 212,20,84
Ba 40,24,4096
Mvaa 216,20,88
Ba 44,24,6144
Mvaa 220,20,92
Ba 60,25,6144
Mvaa 224,24,80
Ta 8,13,40960
Ta 10,13,41984
Mvaa 228,24,84
s_add_u32 m0, s72, 69632
Mvaa 232,24,88
global_load_lds_dwordx4 v26, s[60:61]
Mvaa 236,24,92
s_waitcnt lgkmcnt(10)
s_add_u32 s60, s60, s92
s_addc_u32 s61, s61, s93
Maaa 144,28,80
Ta 12,16,40960
Ta 14,16,41984
Maaa 148,28,84
s_lshl_b32 s58, s86, 16
s_add_u32 s58, s58, 0
s_add_u32 s58, s82, s58
s_addc_u32 s59, s83, 0
Maaa 152,28,88
global_store_dwordx4 v27, a[160:163], s[58:59] offset:0 sc1
Maaa 156,28,92
s_waitcnt lgkmcnt(4)
Mvaa 32,0,32
Ta 16,14,40960
Ta 18,14,41984
Mvaa 36,0,36
s_mov_b32 s95, s93
s_add_u32 m0, s72, 73728
v_accvgpr_read_b32 v240, a96
Mvaa 40,0,40
global_load_lds_dwordx4 v[28:29], off
Mvaa 44,0,44
v_lshl_add_u64 v[28:29], v[28:29], 0, s[70:71]
s_mov_b32 s52, 4294901760
s_mov_b32 s53, 0
Mvaa 80,4,32
Ta 20,17,40960
Ta 22,17,41984
Mvaa 84,4,36
v_accvgpr_read_b32 v242, a100
v_cndmask_b32 v240, v240, v242, s[52:53]
Mvaa 88,4,40
s_mov_b32 s52, 0
s_mov_b32 s53, 65535
v_accvgpr_read_b32 v242, a104
Mvaa 92,4,44
s_waitcnt lgkmcnt(6)
v_cndmask_b32 v240, v240, v242, s[52:53]
Mvaa 48,8,32
Ta 24,12,32768
Ta 26,12,33792
Mvaa 52,8,36
s_cmp_eq_u32 s95, 0
s_cselect_b32 s94, s70, 4293602304
s_add_u32 m0, s72, 77824
s_mov_b32 s52, 0
s_mov_b32 s53, 4294901760
Mvaa 56,8,40
global_load_lds_dwordx4 v[28:29], off
Mvaa 60,8,44
s_waitcnt lgkmcnt(6)
v_lshl_add_u64 v[28:29], v[28:29], 0, s[94:95]
v_accvgpr_read_b32 v242, a108
Mvaa 96,12,32
Ta 28,13,32768
Ta 30,13,33792
Mvaa 100,12,36
s_sub_u32 s91, s91, 1
s_cselect_b64 s[20:21], -7, 1
s_add_u32 m0, s73, 0
v_cndmask_b32 v240, v240, v242, s[52:53]
)ASM"
R"ASM(
Mvaa 104,12,40
global_load_lds_dwordx4 v30, s[62:63]
Mvaa 108,12,44
s_waitcnt lgkmcnt(6)
v_mov_b32 v241, s47
Mvaa 64,16,32
Ta 0,14,32768
Ta 2,14,33792
Mvaa 68,16,36
s_lshl_b64 s[20:21], s[20:21], 7
s_add_u32 m0, s73, 1024
s_lshl_b32 s50, s40, 14
s_add_u32 s50, s50, 42074112
s_lshl_b32 s51, s41, 11
Mvaa 72,16,40
global_load_lds_dwordx4 v31, s[62:63]
Mvaa 76,16,44
s_waitcnt lgkmcnt(6)
s_waitcnt vmcnt(19)
ds_write_b32 v2, v4 offset:0
s_add_u32 s62, s62, s20
s_addc_u32 s63, s63, s21
v_lshlrev_b32 v20, 3, v0
Mvaa 112,20,32
Ta 4,15,32768
Ta 6,15,33792
Mvaa 116,20,36
s_add_u32 s44, s44, 1
v_add_u32 v4, 1, v4
v_add_u32 v20, s50, v20
Mvaa 120,20,40
v_add_u32 v21, s51, v20
global_store_dwordx2 v21, v[240:241], s[28:29] sc1
Mvaa 124,20,44
s_waitcnt lgkmcnt(7)
v_add_u32 v21, 4096, v20
Mvaa 128,24,32
Ta 8,16,32768
Ta 10,16,33792
Mvaa 132,24,36
s_add_u32 m0, s73, 2048
v_add_u32 v22, 8192, v20
Mvaa 136,24,40
global_load_lds_dwordx4 v30, s[64:65]
Mvaa 140,24,44
s_waitcnt lgkmcnt(7)
v_add_u32 v23, 12288, v20
Mvaa 144,28,32
Ta 12,17,32768
Ta 14,17,33792
Mvaa 148,28,36
Mvaa 152,28,40
Mvaa 156,28,44
s_waitcnt lgkmcnt(7)
Mvaa 160,0,32
Ta 16,18,32768
Ta 18,18,33792
Mvaa 164,0,36
s_add_u32 m0, s73, 3072
Mvaa 168,0,40
global_load_lds_dwordx4 v31, s[64:65]
Mvaa 172,0,44
s_waitcnt lgkmcnt(6)
ds_read_b32 v5, v3 offset:0
s_add_u32 s64, s64, s20
s_addc_u32 s65, s65, s21
Mvaa 176,4,32
Ta 20,19,32768
Ta 22,19,33792
Mvaa 180,4,36
s_waitcnt lgkmcnt(2)
v_cmp_gt_u32 vcc, s44, v5
Mvaa 184,4,40
Mvaa 188,4,44
s_cbranch_vccnz Lspin304_%=
Lback304_%=:
Mvaa 192,8,32
Ta 24,12,57344
Ta 26,12,58368
Mvaa 196,8,36
s_sub_u32 s90, s90, 1
s_cselect_b64 s[92:93], -15, 1
s_add_u32 m0, s72, 81920
Mvaa 200,8,40
global_load_lds_dwordx4 v26, s[18:19]
Mvaa 204,8,44
s_lshl_b64 s[92:93], s[92:93], 18
s_add_u32 s18, s18, s92
s_addc_u32 s19, s19, s93
Mvaa 208,12,32
Ta 28,15,57344
Ta 30,15,58368
Mvaa 212,12,36
Mvaa 216,12,40
Mvaa 220,12,44
Mvaa 224,16,32
Ta 0,13,57344
Ta 2,13,58368
Mvaa 228,16,36
s_add_u32 m0, s72, 86016
Mvaa 232,16,40
global_load_lds_dwordx4 v26, s[60:61]
Mvaa 236,16,44
s_waitcnt lgkmcnt(6)
s_add_u32 s60, s60, s92
s_addc_u32 s61, s61, s93
Maaa 144,20,32
Ta 4,16,57344
Ta 6,16,58368
Maaa 148,20,36
Maaa 152,20,40
global_store_dwordx4 v27, a[160:163], s[58:59] offset:1024 sc1
Maaa 156,20,44
s_waitcnt lgkmcnt(6)
Mvaa 32,24,48
Ta 8,14,57344
Ta 10,14,58368
Mvaa 36,24,52
s_mov_b32 s95, s93
s_add_u32 m0, s72, 90112
Mvaa 40,24,56
global_load_lds_dwordx4 v[28:29], off
Mvaa 44,24,60
s_waitcnt lgkmcnt(6)
v_lshl_add_u64 v[28:29], v[28:29], 0, s[70:71]
Mvaa 80,28,48
Ta 12,17,57344
Ta 14,17,58368
Mvaa 84,28,52
Mvaa 88,28,56
Mvaa 92,28,60
s_waitcnt lgkmcnt(6)
Mvaa 48,0,48
Ta 16,12,49152
Ta 18,12,50176
Mvaa 52,0,52
s_cmp_eq_u32 s95, 0
s_cselect_b32 s94, s70, 4293602304
s_add_u32 m0, s72, 94208
Mvaa 56,0,56
global_load_lds_dwordx4 v[28:29], off
Mvaa 60,0,60
s_waitcnt lgkmcnt(6)
v_lshl_add_u64 v[28:29], v[28:29], 0, s[94:95]
Mvaa 96,4,48
Ta 20,13,49152
Ta 22,13,50176
Mvaa 100,4,52
s_add_u32 m0, s73, 4096
Mvaa 104,4,56
global_load_lds_dwordx4 v30, s[66:67]
Mvaa 108,4,60
s_waitcnt lgkmcnt(6)
Mvaa 64,8,48
Ta 24,14,49152
Ta 26,14,50176
Mvaa 68,8,52
s_add_u32 m0, s73, 5120
Mvaa 72,8,56
global_load_lds_dwordx4 v31, s[66:67]
Mvaa 76,8,60
s_waitcnt lgkmcnt(6)
s_waitcnt vmcnt(17)
ds_write_b32 v2, v4 offset:0
s_add_u32 s66, s66, s20
s_addc_u32 s67, s67, s21
Mvaa 112,12,48
Ta 28,15,49152
Ta 30,15,50176
Mvaa 116,12,52
s_add_u32 s44, s44, 1
v_add_u32 v4, 1, v4
Mvaa 120,12,56
Ba 64,24,8192
Mvaa 124,12,60
s_waitcnt lgkmcnt(8)
Mvaa 128,16,48
Ta 0,16,49152
Ta 2,16,50176
Mvaa 132,16,52
s_add_u32 m0, s73, 6144
Mvaa 136,16,56
global_load_lds_dwordx4 v30, s[68:69]
Mvaa 140,16,60
Ba 80,25,8192
s_waitcnt lgkmcnt(9)
Mvaa 144,20,48
Ta 4,17,49152
Ta 6,17,50176
Mvaa 148,20,52
Ba 84,25,10240
Mvaa 152,20,56
Ba 68,24,10240
Mvaa 156,20,60
s_waitcnt lgkmcnt(11)
Mvaa 160,24,48
Ta 8,18,49152
Ta 10,18,50176
Mvaa 164,24,52
v_add_u32 v12, 4294901760, v12
)ASM"
R"ASM(
v_add_u32 v15, 4294901760, v15
s_add_u32 m0, s73, 7168
Mvaa 168,24,56
global_load_lds_dwordx4 v31, s[68:69]
Mvaa 172,24,60
v_add_u32 v13, 4294901760, v13
v_add_u32 v16, 4294901760, v16
s_waitcnt lgkmcnt(10)
ds_read_b32 v5, v3 offset:0
s_add_u32 s68, s68, s20
s_addc_u32 s69, s69, s21
Mvaa 176,28,48
Ta 12,19,49152
Ta 14,19,50176
Mvaa 180,28,52
Ba 88,25,12288
s_waitcnt lgkmcnt(3)
v_cmp_gt_u32 vcc, s44, v5
Mvaa 184,28,56
Mvaa 188,28,60
s_cbranch_vccnz Lspin305_%=
Lback305_%=:
Mvaa 192,0,48
Ta 16,12,8192
Ta 18,12,9216
Mvaa 196,0,52
s_sub_u32 s90, s90, 1
s_cselect_b64 s[92:93], -15, 1
s_add_u32 m0, s72, 98304
Mvaa 200,0,56
global_load_lds_dwordx4 v26, s[18:19]
Mvaa 204,0,60
v_add_u32 v14, 4294901760, v14
v_add_u32 v17, 4294901760, v17
s_lshl_b64 s[92:93], s[92:93], 18
s_add_u32 s18, s18, s92
s_addc_u32 s19, s19, s93
Mvaa 208,4,48
Ta 20,15,8192
Ta 22,15,9216
Mvaa 212,4,52
Ba 72,24,12288
Mvaa 216,4,56
Ba 76,24,14336
Mvaa 220,4,60
Ba 92,25,14336
v_add_u32 v18, 4294901760, v18
v_add_u32 v19, 4294901760, v19
Mvaa 224,8,48
Ta 24,13,8192
Ta 26,13,9216
Mvaa 228,8,52
s_add_u32 m0, s72, 102400
Mvaa 232,8,56
global_load_lds_dwordx4 v26, s[60:61]
Mvaa 236,8,60
s_waitcnt lgkmcnt(10)
s_add_u32 s60, s60, s92
s_addc_u32 s61, s61, s93
Maaa 144,12,48
Ta 28,16,8192
Ta 30,16,9216
Maaa 148,12,52
Maaa 152,12,56
global_store_dwordx4 v27, a[160:163], s[58:59] offset:2048 sc1
Maaa 156,12,60
s_waitcnt lgkmcnt(4)
Mvaa 32,16,64
Ta 0,14,8192
Ta 2,14,9216
Mvaa 36,16,68
s_mov_b32 s95, s93
s_add_u32 m0, s72, 106496
Mvaa 40,16,72
global_load_lds_dwordx4 v[28:29], off
Mvaa 44,16,76
v_lshl_add_u64 v[28:29], v[28:29], 0, s[70:71]
Mvaa 80,20,64
Ta 4,17,8192
Ta 6,17,9216
Mvaa 84,20,68
Mvaa 88,20,72
Mvaa 92,20,76
s_waitcnt lgkmcnt(6)
Mvaa 48,24,64
Ta 8,12,0
Ta 10,12,1024
Mvaa 52,24,68
s_cmp_eq_u32 s95, 0
s_cselect_b32 s94, s70, 4293602304
s_add_u32 m0, s72, 110592
Mvaa 56,24,72
global_load_lds_dwordx4 v[28:29], off
Mvaa 60,24,76
s_waitcnt lgkmcnt(6)
v_lshl_add_u64 v[28:29], v[28:29], 0, s[94:95]
Mvaa 96,28,64
Ta 12,13,0
Ta 14,13,1024
Mvaa 100,28,68
s_sub_u32 s91, s91, 1
s_cselect_b64 s[20:21], -7, 1
s_add_u32 m0, s73, 8192
Mvaa 104,28,72
global_load_lds_dwordx4 v30, s[62:63]
Mvaa 108,28,76
s_waitcnt lgkmcnt(6)
Mvaa 64,0,64
Ta 16,14,0
Ta 18,14,1024
Mvaa 68,0,68
s_lshl_b64 s[20:21], s[20:21], 7
s_add_u32 m0, s73, 9216
Mvaa 72,0,72
global_load_lds_dwordx4 v31, s[62:63]
Mvaa 76,0,76
s_waitcnt lgkmcnt(6)
s_waitcnt vmcnt(21)
ds_write_b32 v2, v4 offset:0
s_add_u32 s62, s62, s20
s_addc_u32 s63, s63, s21
Mvaa 112,4,64
Ta 20,15,0
Ta 22,15,1024
Mvaa 116,4,68
s_add_u32 s44, s44, 1
v_add_u32 v4, 1, v4
s_lshl_b32 s56, s41, 12
s_add_u32 s56, s56, s88
s_add_u32 s56, s56, 0
s_add_u32 s56, s16, s56
s_addc_u32 s57, s17, 0
s_add_u32 m0, s88, 155648
Mvaa 120,4,72
global_load_lds_dwordx4 v27, s[56:57]
Mvaa 124,4,76
s_waitcnt lgkmcnt(7)
Mvaa 128,8,64
Ta 24,16,0
Ta 26,16,1024
Mvaa 132,8,68
s_add_u32 m0, s73, 10240
Mvaa 136,8,72
global_load_lds_dwordx4 v30, s[64:65]
Mvaa 140,8,76
s_waitcnt lgkmcnt(7)
Mvaa 144,12,64
Ta 28,17,0
Ta 30,17,1024
Mvaa 148,12,68
s_lshl_b32 s56, s41, 12
s_add_u32 s56, s56, s88
s_add_u32 s56, s56, 0
s_add_u32 s56, s14, s56
s_addc_u32 s57, s15, 0
s_add_u32 m0, s88, 155648
Mvaa 152,12,72
global_load_lds_dwordx4 v27, s[56:57]
Mvaa 156,12,76
s_waitcnt lgkmcnt(7)
Mvaa 160,16,64
Ta 0,18,0
Ta 2,18,1024
Mvaa 164,16,68
v_add_u32 v12, 65536, v12
v_add_u32 v15, 65536, v15
s_add_u32 m0, s73, 11264
Mvaa 168,16,72
global_load_lds_dwordx4 v31, s[64:65]
Mvaa 172,16,76
v_add_u32 v13, 65536, v13
v_add_u32 v16, 65536, v16
s_waitcnt lgkmcnt(6)
ds_read_b32 v5, v3 offset:0
s_add_u32 s64, s64, s20
s_addc_u32 s65, s65, s21
Mvaa 176,20,64
Ta 4,19,0
Ta 6,19,1024
Mvaa 180,20,68
s_waitcnt lgkmcnt(2)
v_cmp_gt_u32 vcc, s44, v5
Mvaa 184,20,72
Mvaa 188,20,76
s_cbranch_vccnz Lspin306_%=
Lback306_%=:
Mvaa 192,24,64
Ta 8,12,8192
Ta 10,12,9216
Mvaa 196,24,68
s_sub_u32 s90, s90, 1
s_cselect_b64 s[92:93], -15, 1
s_add_u32 m0, s72, 114688
Mvaa 200,24,72
global_load_lds_dwordx4 v26, s[18:19]
Mvaa 204,24,76
v_add_u32 v14, 65536, v14
v_add_u32 v17, 65536, v17
s_lshl_b64 s[92:93], s[92:93], 18
s_add_u32 s18, s18, s92
)ASM"
R"ASM(
s_addc_u32 s19, s19, s93
Mvaa 208,28,64
Ta 12,15,8192
Ta 14,15,9216
Mvaa 212,28,68
Mvaa 216,28,72
Mvaa 220,28,76
v_add_u32 v18, 65536, v18
v_add_u32 v19, 65536, v19
Mvaa 224,0,64
Ta 16,13,8192
Ta 18,13,9216
Mvaa 228,0,68
s_add_u32 m0, s72, 118784
Mvaa 232,0,72
global_load_lds_dwordx4 v26, s[60:61]
Mvaa 236,0,76
s_waitcnt lgkmcnt(6)
s_add_u32 s60, s60, s92
s_addc_u32 s61, s61, s93
Maaa 144,4,64
Ta 20,16,8192
Ta 22,16,9216
Maaa 148,4,68
Maaa 152,4,72
global_store_dwordx4 v27, a[160:163], s[58:59] offset:3072 sc1
Maaa 156,4,76
s_waitcnt lgkmcnt(6)
Mvaa 32,8,80
Ta 24,14,8192
Ta 26,14,9216
Mvaa 36,8,84
s_mov_b32 s95, s93
s_add_u32 m0, s72, 122880
Mvaa 40,8,88
global_load_lds_dwordx4 v[28:29], off
Mvaa 44,8,92
s_waitcnt lgkmcnt(6)
v_lshl_add_u64 v[28:29], v[28:29], 0, s[70:71]
Mvaa 80,12,80
Ta 28,17,8192
Ta 30,17,9216
Mvaa 84,12,84
Mvaa 88,12,88
Mvaa 92,12,92
s_waitcnt lgkmcnt(6)
Mvaa 48,16,80
Ta 0,12,0
Ta 2,12,1024
Mvaa 52,16,84
s_cmp_eq_u32 s95, 0
s_cselect_b32 s94, s70, 4293602304
s_add_u32 m0, s72, 126976
Mvaa 56,16,88
global_load_lds_dwordx4 v[28:29], off
Mvaa 60,16,92
s_waitcnt lgkmcnt(6)
v_lshl_add_u64 v[28:29], v[28:29], 0, s[94:95]
Mvaa 96,20,80
Ta 4,13,0
Ta 6,13,1024
Mvaa 100,20,84
s_add_u32 m0, s73, 12288
Mvaa 104,20,88
global_load_lds_dwordx4 v30, s[66:67]
Mvaa 108,20,92
s_waitcnt lgkmcnt(6)
Mvaa 64,24,80
Ta 8,14,0
Ta 10,14,1024
Mvaa 68,24,84
s_add_u32 m0, s73, 13312
Mvaa 72,24,88
global_load_lds_dwordx4 v31, s[66:67]
Mvaa 76,24,92
s_waitcnt lgkmcnt(6)
s_waitcnt vmcnt(18)
ds_write_b32 v2, v4 offset:0
s_add_u32 s66, s66, s20
s_addc_u32 s67, s67, s21
Mvaa 112,28,80
Ta 12,15,0
Ta 14,15,1024
Mvaa 116,28,84
s_add_u32 s44, s44, 1
v_add_u32 v4, 1, v4
Mvaa 120,28,88
Ba 32,24,0
Mvaa 124,28,92
s_waitcnt lgkmcnt(8)
Mvaa 128,0,80
Ta 16,16,0
Ta 18,16,1024
Mvaa 132,0,84
s_add_u32 m0, s73, 14336
Mvaa 136,0,88
global_load_lds_dwordx4 v30, s[68:69]
Mvaa 140,0,92
Ba 48,25,0
s_waitcnt lgkmcnt(9)
Mvaa 144,4,80
Ta 20,17,0
Ta 22,17,1024
Mvaa 148,4,84
Ba 52,25,2048
Mvaa 152,4,88
Ba 36,24,2048
Mvaa 156,4,92
s_waitcnt lgkmcnt(11)
Mvaa 160,8,80
Ta 24,18,0
Ta 26,18,1024
Mvaa 164,8,84
s_add_u32 m0, s73, 15360
Mvaa 168,8,88
global_load_lds_dwordx4 v31, s[68:69]
Mvaa 172,8,92
s_waitcnt lgkmcnt(10)
ds_read_b32 v5, v3 offset:0
s_add_u32 s68, s68, s20
s_addc_u32 s69, s69, s21
Mvaa 176,12,80
Ta 28,19,0
Ta 30,19,1024
Mvaa 180,12,84
Ba 56,25,4096
s_waitcnt lgkmcnt(3)
v_cmp_gt_u32 vcc, s44, v5
Mvaa 184,12,88
Mvaa 188,12,92
s_cbranch_vccnz Lspin307_%=
Lback307_%=:
Mvaa 192,16,80
Ta 0,12,24576
Ta 2,12,25600
Mvaa 196,16,84
s_sub_u32 s90, s90, 1
s_cselect_b64 s[92:93], -15, 1
s_add_u32 m0, s72, 0
Mvaa 200,16,88
global_load_lds_dwordx4 v26, s[18:19]
Mvaa 204,16,92
s_lshl_b64 s[92:93], s[92:93], 18
s_add_u32 s18, s18, s92
s_addc_u32 s19, s19, s93
Mvaa 208,20,80
Ta 4,15,24576
Ta 6,15,25600
Mvaa 212,20,84
Ba 40,24,4096
Mvaa 216,20,88
Ba 44,24,6144
Mvaa 220,20,92
Ba 60,25,6144
Mvaa 224,24,80
Ta 8,13,24576
Ta 10,13,25600
Mvaa 228,24,84
s_add_u32 m0, s72, 4096
Mvaa 232,24,88
global_load_lds_dwordx4 v26, s[60:61]
Mvaa 236,24,92
s_waitcnt lgkmcnt(10)
s_add_u32 s60, s60, s92
s_addc_u32 s61, s61, s93
Maaa 144,28,80
Ta 12,16,24576
Ta 14,16,25600
Maaa 148,28,84
s_lshl_b32 s58, s86, 16
s_add_u32 s58, s58, 4096
s_add_u32 s58, s82, s58
s_addc_u32 s59, s83, 0
Maaa 152,28,88
global_store_dwordx4 v27, a[160:163], s[58:59] offset:0 sc1
Maaa 156,28,92
s_waitcnt lgkmcnt(4)
Mvaa 32,0,32
Ta 16,14,24576
Ta 18,14,25600
Mvaa 36,0,36
s_mov_b32 s95, s93
s_add_u32 m0, s72, 8192
Mvaa 40,0,40
global_load_lds_dwordx4 v[28:29], off
Mvaa 44,0,44
v_lshl_add_u64 v[28:29], v[28:29], 0, s[70:71]
Mvaa 80,4,32
Ta 20,17,24576
Ta 22,17,25600
Mvaa 84,4,36
Mvaa 88,4,40
Mvaa 92,4,44
s_waitcnt lgkmcnt(6)
Mvaa 48,8,32
Ta 24,12,16384
Ta 26,12,17408
Mvaa 52,8,36
s_cmp_eq_u32 s95, 0
s_cselect_b32 s94, s70, 4293602304
)ASM"
R"ASM(
s_add_u32 m0, s72, 12288
Mvaa 56,8,40
global_load_lds_dwordx4 v[28:29], off
Mvaa 60,8,44
s_waitcnt lgkmcnt(6)
v_lshl_add_u64 v[28:29], v[28:29], 0, s[94:95]
Mvaa 96,12,32
Ta 28,13,16384
Ta 30,13,17408
Mvaa 100,12,36
s_sub_u32 s91, s91, 1
s_cselect_b64 s[20:21], -7, 1
s_add_u32 m0, s73, 0
Mvaa 104,12,40
global_load_lds_dwordx4 v30, s[62:63]
Mvaa 108,12,44
s_waitcnt lgkmcnt(6)
Mvaa 64,16,32
Ta 0,14,16384
Ta 2,14,17408
Mvaa 68,16,36
s_lshl_b64 s[20:21], s[20:21], 7
s_add_u32 m0, s73, 1024
Mvaa 72,16,40
global_load_lds_dwordx4 v31, s[62:63]
Mvaa 76,16,44
s_waitcnt lgkmcnt(6)
s_waitcnt vmcnt(22)
ds_write_b32 v2, v4 offset:0
s_add_u32 s62, s62, s20
s_addc_u32 s63, s63, s21
Mvaa 112,20,32
Ta 4,15,16384
Ta 6,15,17408
Mvaa 116,20,36
s_add_u32 s44, s44, 1
v_add_u32 v4, 1, v4
Mvaa 120,20,40
Mvaa 124,20,44
s_waitcnt lgkmcnt(7)
Mvaa 128,24,32
Ta 8,16,16384
Ta 10,16,17408
Mvaa 132,24,36
s_add_u32 m0, s73, 2048
Mvaa 136,24,40
global_load_lds_dwordx4 v30, s[64:65]
Mvaa 140,24,44
s_waitcnt lgkmcnt(7)
Mvaa 144,28,32
Ta 12,17,16384
Ta 14,17,17408
Mvaa 148,28,36
Mvaa 152,28,40
Mvaa 156,28,44
s_waitcnt lgkmcnt(7)
Mvaa 160,0,32
Ta 16,18,16384
Ta 18,18,17408
Mvaa 164,0,36
s_add_u32 m0, s73, 3072
Mvaa 168,0,40
global_load_lds_dwordx4 v31, s[64:65]
Mvaa 172,0,44
s_waitcnt lgkmcnt(6)
ds_read_b32 v5, v3 offset:0
s_add_u32 s64, s64, s20
s_addc_u32 s65, s65, s21
Mvaa 176,4,32
Ta 20,19,16384
Ta 22,19,17408
Mvaa 180,4,36
s_waitcnt lgkmcnt(2)
v_cmp_gt_u32 vcc, s44, v5
Mvaa 184,4,40
Mvaa 188,4,44
s_cbranch_vccnz Lspin308_%=
Lback308_%=:
Mvaa 192,8,32
Ta 24,12,40960
Ta 26,12,41984
Mvaa 196,8,36
s_sub_u32 s90, s90, 1
s_cselect_b64 s[92:93], -15, 1
s_add_u32 m0, s72, 65536
Mvaa 200,8,40
global_load_lds_dwordx4 v26, s[18:19]
Mvaa 204,8,44
s_lshl_b64 s[92:93], s[92:93], 18
s_add_u32 s18, s18, s92
s_addc_u32 s19, s19, s93
Mvaa 208,12,32
Ta 28,15,40960
Ta 30,15,41984
Mvaa 212,12,36
Mvaa 216,12,40
Mvaa 220,12,44
Mvaa 224,16,32
Ta 0,13,40960
Ta 2,13,41984
Mvaa 228,16,36
s_add_u32 m0, s72, 69632
Mvaa 232,16,40
global_load_lds_dwordx4 v26, s[60:61]
Mvaa 236,16,44
s_waitcnt lgkmcnt(6)
s_add_u32 s60, s60, s92
s_addc_u32 s61, s61, s93
Maaa 144,20,32
Ta 4,16,40960
Ta 6,16,41984
Maaa 148,20,36
Maaa 152,20,40
global_store_dwordx4 v27, a[160:163], s[58:59] offset:1024 sc1
Maaa 156,20,44
s_waitcnt lgkmcnt(6)
Mvaa 32,24,48
Ta 8,14,40960
Ta 10,14,41984
Mvaa 36,24,52
s_mov_b32 s95, s93
s_add_u32 m0, s72, 73728
Mvaa 40,24,56
global_load_lds_dwordx4 v[28:29], off
Mvaa 44,24,60
s_waitcnt lgkmcnt(6)
v_lshl_add_u64 v[28:29], v[28:29], 0, s[70:71]
Mvaa 80,28,48
Ta 12,17,40960
Ta 14,17,41984
Mvaa 84,28,52
Mvaa 88,28,56
Mvaa 92,28,60
s_waitcnt lgkmcnt(6)
Mvaa 48,0,48
Ta 16,12,32768
Ta 18,12,33792
Mvaa 52,0,52
s_cmp_eq_u32 s95, 0
s_cselect_b32 s94, s70, 4293602304
s_add_u32 m0, s72, 77824
Mvaa 56,0,56
global_load_lds_dwordx4 v[28:29], off
Mvaa 60,0,60
s_waitcnt lgkmcnt(6)
v_lshl_add_u64 v[28:29], v[28:29], 0, s[94:95]
Mvaa 96,4,48
Ta 20,13,32768
Ta 22,13,33792
Mvaa 100,4,52
s_add_u32 m0, s73, 4096
Mvaa 104,4,56
global_load_lds_dwordx4 v30, s[66:67]
Mvaa 108,4,60
s_waitcnt lgkmcnt(6)
Mvaa 64,8,48
Ta 24,14,32768
Ta 26,14,33792
Mvaa 68,8,52
s_add_u32 m0, s73, 5120
Mvaa 72,8,56
global_load_lds_dwordx4 v31, s[66:67]
Mvaa 76,8,60
s_waitcnt lgkmcnt(6)
s_waitcnt vmcnt(16)
ds_write_b32 v2, v4 offset:0
s_add_u32 s66, s66, s20
s_addc_u32 s67, s67, s21
Mvaa 112,12,48
Ta 28,15,32768
Ta 30,15,33792
Mvaa 116,12,52
s_add_u32 s44, s44, 1
v_add_u32 v4, 1, v4
Mvaa 120,12,56
Ba 64,24,8192
Mvaa 124,12,60
s_waitcnt lgkmcnt(8)
Mvaa 128,16,48
Ta 0,16,32768
Ta 2,16,33792
Mvaa 132,16,52
s_add_u32 m0, s73, 6144
Mvaa 136,16,56
global_load_lds_dwordx4 v30, s[68:69]
Mvaa 140,16,60
Ba 80,25,8192
s_waitcnt lgkmcnt(9)
Mvaa 144,20,48
Ta 4,17,32768
Ta 6,17,33792
Mvaa 148,20,52
Ba 84,25,10240
Mvaa 152,20,56
Ba 68,24,10240
Mvaa 156,20,60
s_waitcnt lgkmcnt(11)
Mvaa 160,24,48
Ta 8,18,32768
Ta 10,18,33792
)ASM"
R"ASM(
Mvaa 164,24,52
s_add_u32 m0, s73, 7168
Mvaa 168,24,56
global_load_lds_dwordx4 v31, s[68:69]
Mvaa 172,24,60
s_waitcnt lgkmcnt(10)
ds_read_b32 v5, v3 offset:0
s_add_u32 s68, s68, s20
s_addc_u32 s69, s69, s21
Mvaa 176,28,48
Ta 12,19,32768
Ta 14,19,33792
Mvaa 180,28,52
Ba 88,25,12288
s_waitcnt lgkmcnt(3)
v_cmp_gt_u32 vcc, s44, v5
Mvaa 184,28,56
Mvaa 188,28,60
s_cbranch_vccnz Lspin309_%=
Lback309_%=:
Mvaa 192,0,48
Ta 16,12,57344
Ta 18,12,58368
Mvaa 196,0,52
s_sub_u32 s90, s90, 1
s_cselect_b64 s[92:93], -15, 1
s_add_u32 m0, s72, 81920
Mvaa 200,0,56
global_load_lds_dwordx4 v26, s[18:19]
Mvaa 204,0,60
s_lshl_b64 s[92:93], s[92:93], 18
s_add_u32 s18, s18, s92
s_addc_u32 s19, s19, s93
Mvaa 208,4,48
Ta 20,15,57344
Ta 22,15,58368
Mvaa 212,4,52
Ba 72,24,12288
Mvaa 216,4,56
Ba 76,24,14336
Mvaa 220,4,60
Ba 92,25,14336
Mvaa 224,8,48
Ta 24,13,57344
Ta 26,13,58368
Mvaa 228,8,52
s_add_u32 m0, s72, 86016
Mvaa 232,8,56
global_load_lds_dwordx4 v26, s[60:61]
Mvaa 236,8,60
s_waitcnt lgkmcnt(10)
s_add_u32 s60, s60, s92
s_addc_u32 s61, s61, s93
Maaa 144,12,48
Ta 28,16,57344
Ta 30,16,58368
Maaa 148,12,52
Maaa 152,12,56
global_store_dwordx4 v27, a[160:163], s[58:59] offset:2048 sc1
Maaa 156,12,60
s_waitcnt lgkmcnt(4)
Mvaa 32,16,64
Ta 0,14,57344
Ta 2,14,58368
Mvaa 36,16,68
s_mov_b32 s95, s93
s_add_u32 m0, s72, 90112
Mvaa 40,16,72
global_load_lds_dwordx4 v[28:29], off
Mvaa 44,16,76
v_lshl_add_u64 v[28:29], v[28:29], 0, s[70:71]
Mvaa 80,20,64
Ta 4,17,57344
Ta 6,17,58368
Mvaa 84,20,68
Mvaa 88,20,72
Mvaa 92,20,76
s_waitcnt lgkmcnt(6)
Mvaa 48,24,64
Ta 8,12,49152
Ta 10,12,50176
Mvaa 52,24,68
s_cmp_eq_u32 s95, 0
s_cselect_b32 s94, s70, 4293602304
s_add_u32 m0, s72, 94208
Mvaa 56,24,72
global_load_lds_dwordx4 v[28:29], off
Mvaa 60,24,76
s_waitcnt lgkmcnt(6)
v_lshl_add_u64 v[28:29], v[28:29], 0, s[94:95]
Mvaa 96,28,64
Ta 12,13,49152
Ta 14,13,50176
Mvaa 100,28,68
s_sub_u32 s91, s91, 1
s_cselect_b64 s[20:21], -7, 1
s_add_u32 m0, s73, 8192
Mvaa 104,28,72
global_load_lds_dwordx4 v30, s[62:63]
Mvaa 108,28,76
s_waitcnt lgkmcnt(6)
Mvaa 64,0,64
Ta 16,14,49152
Ta 18,14,50176
Mvaa 68,0,68
s_lshl_b64 s[20:21], s[20:21], 7
s_add_u32 m0, s73, 9216
Mvaa 72,0,72
global_load_lds_dwordx4 v31, s[62:63]
Mvaa 76,0,76
s_waitcnt lgkmcnt(6)
s_waitcnt vmcnt(20)
ds_write_b32 v2, v4 offset:0
s_add_u32 s62, s62, s20
s_addc_u32 s63, s63, s21
Mvaa 112,4,64
Ta 20,15,49152
Ta 22,15,50176
Mvaa 116,4,68
s_add_u32 s44, s44, 1
v_add_u32 v4, 1, v4
Mvaa 120,4,72
Mvaa 124,4,76
s_waitcnt lgkmcnt(7)
Mvaa 128,8,64
Ta 24,16,49152
Ta 26,16,50176
Mvaa 132,8,68
s_add_u32 m0, s73, 10240
Mvaa 136,8,72
global_load_lds_dwordx4 v30, s[64:65]
Mvaa 140,8,76
s_waitcnt lgkmcnt(7)
Mvaa 144,12,64
Ta 28,17,49152
Ta 30,17,50176
Mvaa 148,12,68
Mvaa 152,12,72
Mvaa 156,12,76
s_waitcnt lgkmcnt(7)
Mvaa 160,16,64
Ta 0,18,49152
Ta 2,18,50176
Mvaa 164,16,68
v_add_u32 v12, 4294901760, v12
v_add_u32 v15, 4294901760, v15
s_add_u32 m0, s73, 11264
Mvaa 168,16,72
global_load_lds_dwordx4 v31, s[64:65]
Mvaa 172,16,76
v_add_u32 v13, 4294901760, v13
v_add_u32 v16, 4294901760, v16
s_waitcnt lgkmcnt(6)
ds_read_b32 v5, v3 offset:0
s_add_u32 s64, s64, s20
s_addc_u32 s65, s65, s21
Mvaa 176,20,64
Ta 4,19,49152
Ta 6,19,50176
Mvaa 180,20,68
s_waitcnt lgkmcnt(2)
v_cmp_gt_u32 vcc, s44, v5
Mvaa 184,20,72
Mvaa 188,20,76
s_cbranch_vccnz Lspin310_%=
Lback310_%=:
Mvaa 192,24,64
Ta 8,12,8192
Ta 10,12,9216
Mvaa 196,24,68
s_sub_u32 s90, s90, 1
s_cselect_b64 s[92:93], -15, 1
s_add_u32 m0, s72, 98304
Mvaa 200,24,72
global_load_lds_dwordx4 v26, s[18:19]
Mvaa 204,24,76
v_add_u32 v14, 4294901760, v14
v_add_u32 v17, 4294901760, v17
s_lshl_b64 s[92:93], s[92:93], 18
s_add_u32 s18, s18, s92
s_addc_u32 s19, s19, s93
Mvaa 208,28,64
Ta 12,15,8192
Ta 14,15,9216
Mvaa 212,28,68
Mvaa 216,28,72
Mvaa 220,28,76
v_add_u32 v18, 4294901760, v18
v_add_u32 v19, 4294901760, v19
Mvaa 224,0,64
Ta 16,13,8192
Ta 18,13,9216
Mvaa 228,0,68
s_add_u32 m0, s72, 102400
Mvaa 232,0,72
global_load_lds_dwordx4 v26, s[60:61]
Mvaa 236,0,76
s_waitcnt lgkmcnt(6)
s_add_u32 s60, s60, s92
s_addc_u32 s61, s61, s93
)ASM"
R"ASM(
Maaa 144,4,64
Ta 20,16,8192
Ta 22,16,9216
Maaa 148,4,68
Maaa 152,4,72
global_store_dwordx4 v27, a[160:163], s[58:59] offset:3072 sc1
Maaa 156,4,76
s_waitcnt lgkmcnt(6)
Mvaa 32,8,80
Ta 24,14,8192
Ta 26,14,9216
Mvaa 36,8,84
s_mov_b32 s95, s93
s_add_u32 m0, s72, 106496
global_load_dwordx2 v[240:241], v20, s[28:29] offset:0 sc1
Mvaa 40,8,88
global_load_lds_dwordx4 v[28:29], off
Mvaa 44,8,92
s_waitcnt lgkmcnt(6)
v_lshl_add_u64 v[28:29], v[28:29], 0, s[70:71]
global_load_dwordx2 v[242:243], v20, s[28:29] offset:2048 sc1
Mvaa 80,12,80
Ta 28,17,8192
Ta 30,17,9216
Mvaa 84,12,84
global_load_dwordx2 v[244:245], v21, s[28:29] offset:0 sc1
Mvaa 88,12,88
global_load_dwordx2 v[246:247], v21, s[28:29] offset:2048 sc1
Mvaa 92,12,92
s_waitcnt lgkmcnt(6)
global_load_dwordx2 v[248:249], v22, s[28:29] offset:0 sc1
Mvaa 48,16,80
Ta 0,12,0
Ta 2,12,1024
Mvaa 52,16,84
s_cmp_eq_u32 s95, 0
s_cselect_b32 s94, s70, 4293602304
s_add_u32 m0, s72, 110592
global_load_dwordx2 v[250:251], v22, s[28:29] offset:2048 sc1
Mvaa 56,16,88
global_load_lds_dwordx4 v[28:29], off
Mvaa 60,16,92
s_waitcnt lgkmcnt(6)
v_lshl_add_u64 v[28:29], v[28:29], 0, s[94:95]
global_load_dwordx2 v[252:253], v23, s[28:29] offset:0 sc1
Mvaa 96,20,80
Ta 4,13,0
Ta 6,13,1024
Mvaa 100,20,84
s_add_u32 m0, s73, 12288
global_load_dwordx2 v[254:255], v23, s[28:29] offset:2048 sc1
Mvaa 104,20,88
global_load_lds_dwordx4 v30, s[66:67]
Mvaa 108,20,92
s_waitcnt lgkmcnt(6)
Mvaa 64,24,80
Ta 8,14,0
Ta 10,14,1024
Mvaa 68,24,84
s_add_u32 m0, s73, 13312
Mvaa 72,24,88
global_load_lds_dwordx4 v31, s[66:67]
Mvaa 76,24,92
s_waitcnt lgkmcnt(6)
s_waitcnt vmcnt(24)
ds_write_b32 v2, v4 offset:0
s_add_u32 s66, s66, s20
s_addc_u32 s67, s67, s21
Mvaa 112,28,80
Ta 12,15,0
Ta 14,15,1024
Mvaa 116,28,84
s_add_u32 s44, s44, 1
v_add_u32 v4, 1, v4
Mvaa 120,28,88
Ba 32,24,0
Mvaa 124,28,92
s_waitcnt lgkmcnt(8)
Mvaa 128,0,80
Ta 16,16,0
Ta 18,16,1024
Mvaa 132,0,84
s_add_u32 m0, s73, 14336
Mvaa 136,0,88
global_load_lds_dwordx4 v30, s[68:69]
Mvaa 140,0,92
Ba 48,25,0
s_waitcnt lgkmcnt(9)
Mvaa 144,4,80
Ta 20,17,0
Ta 22,17,1024
Mvaa 148,4,84
Ba 52,25,2048
Mvaa 152,4,88
Ba 36,24,2048
Mvaa 156,4,92
s_waitcnt lgkmcnt(11)
Mvaa 160,8,80
Ta 24,18,0
Ta 26,18,1024
Mvaa 164,8,84
v_add_u32 v12, 65536, v12
v_add_u32 v15, 65536, v15
s_add_u32 m0, s73, 15360
Mvaa 168,8,88
global_load_lds_dwordx4 v31, s[68:69]
Mvaa 172,8,92
v_add_u32 v13, 65536, v13
v_add_u32 v16, 65536, v16
s_waitcnt lgkmcnt(10)
ds_read_b32 v5, v3 offset:0
s_add_u32 s68, s68, s20
s_addc_u32 s69, s69, s21
Mvaa 176,12,80
Ta 28,19,0
Ta 30,19,1024
Mvaa 180,12,84
Ba 56,25,4096
s_waitcnt lgkmcnt(3)
v_cmp_gt_u32 vcc, s44, v5
Mvaa 184,12,88
Mvaa 188,12,92
s_cbranch_vccnz Lspin311_%=
Lback311_%=:
Mvaa 192,16,80
Ta 0,12,8192
Ta 2,12,9216
Mvaa 196,16,84
s_sub_u32 s90, s90, 1
s_cselect_b64 s[92:93], -15, 1
s_add_u32 m0, s72, 114688
Mvaa 200,16,88
global_load_lds_dwordx4 v26, s[18:19]
Mvaa 204,16,92
v_add_u32 v14, 65536, v14
v_add_u32 v17, 65536, v17
s_lshl_b64 s[92:93], s[92:93], 18
s_add_u32 s18, s18, s92
s_addc_u32 s19, s19, s93
Mvaa 208,20,80
Ta 4,15,8192
Ta 6,15,9216
Mvaa 212,20,84
Ba 40,24,4096
Mvaa 216,20,88
Ba 44,24,6144
Mvaa 220,20,92
Ba 60,25,6144
v_add_u32 v18, 65536, v18
v_add_u32 v19, 65536, v19
Mvaa 224,24,80
Ta 8,13,8192
Ta 10,13,9216
Mvaa 228,24,84
s_add_u32 m0, s72, 118784
Mvaa 232,24,88
global_load_lds_dwordx4 v26, s[60:61]
Mvaa 236,24,92
s_waitcnt lgkmcnt(10)
s_add_u32 s60, s60, s92
s_addc_u32 s61, s61, s93
Maaa 144,28,80
Ta 12,16,8192
Ta 14,16,9216
Maaa 148,28,84
s_lshl_b32 s58, s86, 16
s_add_u32 s58, s58, 8192
s_add_u32 s58, s82, s58
s_addc_u32 s59, s83, 0
Maaa 152,28,88
global_store_dwordx4 v27, a[160:163], s[58:59] offset:0 sc1
Maaa 156,28,92
s_waitcnt lgkmcnt(4)
Mvaa 32,0,32
Ta 16,14,8192
Ta 18,14,9216
Mvaa 36,0,36
s_mov_b32 s95, s93
s_add_u32 m0, s72, 122880
Mvaa 40,0,40
global_load_lds_dwordx4 v[28:29], off
Mvaa 44,0,44
v_lshl_add_u64 v[28:29], v[28:29], 0, s[70:71]
Mvaa 80,4,32
Ta 20,17,8192
Ta 22,17,9216
Mvaa 84,4,36
Mvaa 88,4,40
Mvaa 92,4,44
s_waitcnt lgkmcnt(6)
Mvaa 48,8,32
Ta 24,12,0
Ta 26,12,1024
Mvaa 52,8,36
s_cmp_eq_u32 s95, 0
s_cselect_b32 s94, s70, 4293602304
s_add_u32 m0, s72, 126976
Mvaa 56,8,40
global_load_lds_dwordx4 v[28:29], off
Mvaa 60,8,44
)ASM"
R"ASM(
s_waitcnt lgkmcnt(6)
v_lshl_add_u64 v[28:29], v[28:29], 0, s[94:95]
Mvaa 96,12,32
Ta 28,13,0
Ta 30,13,1024
Mvaa 100,12,36
Mvaa 104,12,40
Mvaa 108,12,44
s_waitcnt lgkmcnt(6)
Mvaa 64,16,32
Ta 0,14,0
Ta 2,14,1024
Mvaa 68,16,36
Mvaa 72,16,40
Mvaa 76,16,44
s_waitcnt lgkmcnt(6)
s_waitcnt vmcnt(26)
ds_write_b32 v2, v4 offset:0
Mvaa 112,20,32
Ta 4,15,0
Ta 6,15,1024
Mvaa 116,20,36
s_add_u32 s44, s44, 1
v_add_u32 v4, 1, v4
Mvaa 120,20,40
Mvaa 124,20,44
s_waitcnt lgkmcnt(7)
Mvaa 128,24,32
Ta 8,16,0
Ta 10,16,1024
Mvaa 132,24,36
Mvaa 136,24,40
Mvaa 140,24,44
s_waitcnt lgkmcnt(7)
Mvaa 144,28,32
Ta 12,17,0
Ta 14,17,1024
Mvaa 148,28,36
Mvaa 152,28,40
Mvaa 156,28,44
s_waitcnt lgkmcnt(7)
Mvaa 160,0,32
Ta 16,18,0
Ta 18,18,1024
Mvaa 164,0,36
Mvaa 168,0,40
Mvaa 172,0,44
s_waitcnt lgkmcnt(6)
ds_read_b32 v5, v3 offset:0
Mvaa 176,4,32
Ta 20,19,0
Ta 22,19,1024
Mvaa 180,4,36
s_waitcnt lgkmcnt(2)
v_cmp_gt_u32 vcc, s44, v5
Mvaa 184,4,40
Mvaa 188,4,44
s_cbranch_vccnz Lspin312_%=
Lback312_%=:
Mvaa 192,8,32
Ta 24,12,24576
Ta 26,12,25600
Mvaa 196,8,36
s_add_u32 m0, s34, 0
Mvaa 200,8,40
global_load_lds_dwordx4 v6, s[36:37] offset:0
Mvaa 204,8,44
Mvaa 208,12,32
Ta 28,15,24576
Ta 30,15,25600
Mvaa 212,12,36
Mvaa 216,12,40
Mvaa 220,12,44
Mvaa 224,16,32
Ta 0,13,24576
Ta 2,13,25600
Mvaa 228,16,36
s_add_u32 m0, s34, 0
Mvaa 232,16,40
global_load_lds_dwordx4 v7, s[36:37] offset:1024
Mvaa 236,16,44
s_waitcnt lgkmcnt(6)
Maaa 144,20,32
Ta 4,16,24576
Ta 6,16,25600
Maaa 148,20,36
Maaa 152,20,40
global_store_dwordx4 v27, a[160:163], s[58:59] offset:1024 sc1
Maaa 156,20,44
s_waitcnt lgkmcnt(6)
Mvaa 32,24,48
Ta 8,14,24576
Ta 10,14,25600
Mvaa 36,24,52
s_add_u32 m0, s34, 0
s_waitcnt vmcnt(12)
Mvaa 40,24,56
global_load_lds_dwordx4 v8, s[36:37] offset:2048
Mvaa 44,24,60
s_waitcnt lgkmcnt(6)
v_xor_b32 v31, s47, v241
Mvaa 80,28,48
Ta 12,17,24576
Ta 14,17,25600
Mvaa 84,28,52
v_xor_b32 v243, s47, v243
v_xor_b32 v245, s47, v245
Mvaa 88,28,56
v_xor_b32 v247, s47, v247
v_xor_b32 v249, s47, v249
Mvaa 92,28,60
s_waitcnt lgkmcnt(6)
v_xor_b32 v251, s47, v251
Mvaa 48,0,48
Ta 16,12,16384
Ta 18,12,17408
Mvaa 52,0,52
s_add_u32 m0, s34, 0
v_xor_b32 v253, s47, v253
Mvaa 56,0,56
global_load_lds_dwordx4 v9, s[36:37] offset:3072
Mvaa 60,0,60
s_waitcnt lgkmcnt(6)
s_add_u32 s36, s36, 16384
s_addc_u32 s37, s37, 0
v_xor_b32 v255, s47, v255
Mvaa 96,4,48
Ta 20,13,16384
Ta 22,13,17408
Mvaa 100,4,52
v_or3_b32 v31, v31, v243, v245
v_or3_b32 v31, v31, v247, v249
Mvaa 104,4,56
v_or3_b32 v31, v31, v251, v253
v_or_b32 v31, v31, v255
Mvaa 108,4,60
s_waitcnt lgkmcnt(6)
v_cmp_ne_u32 vcc, 0, v31
s_cbranch_vccnz Lcsw_%=
Lcsok_%=:
Mvaa 64,8,48
Ta 24,14,16384
Ta 26,14,17408
Mvaa 68,8,52
v_add_f32 v240, v240, v242
v_add_f32 v240, v240, v244
Mvaa 72,8,56
v_add_f32 v240, v240, v246
v_add_f32 v240, v240, v248
Mvaa 76,8,60
s_waitcnt lgkmcnt(6)
s_waitcnt vmcnt(10)
ds_write_b32 v2, v4 offset:0
v_add_f32 v240, v240, v250
Mvaa 112,12,48
Ta 28,15,16384
Ta 30,15,17408
Mvaa 116,12,52
s_add_u32 s44, s44, 1
v_add_u32 v4, 1, v4
v_add_f32 v240, v240, v252
Mvaa 120,12,56
Ba 64,24,8192
Mvaa 124,12,60
s_waitcnt lgkmcnt(8)
v_add_f32 v240, v240, v254
Mvaa 128,16,48
Ta 0,16,16384
Ta 2,16,17408
Mvaa 132,16,52
v_and_b32 v0, 48, v1
v_and_b32 v241, 15, v1
Mvaa 136,16,56
v_lshlrev_b32 v241, 2, v241
s_lshl_b32 s50, s35, 6
s_add_u32 s50, s50, 149504
Mvaa 140,16,60
Ba 80,25,8192
s_waitcnt lgkmcnt(9)
v_lshl_add_u32 v241, v0, 4, v241
Mvaa 144,20,48
Ta 4,17,16384
Ta 6,17,17408
Mvaa 148,20,52
Ba 84,25,10240
v_add_u32 v241, s50, v241
Mvaa 152,20,56
Ba 68,24,10240
Mvaa 156,20,60
s_waitcnt lgkmcnt(11)
ds_write_b32 v241, v240 offset:0
Mvaa 160,24,48
Ta 8,18,16384
Ta 10,18,17408
Mvaa 164,24,52
v_add_u32 v0, 149504, v0
Mvaa 168,24,56
Mvaa 172,24,60
s_waitcnt lgkmcnt(11)
ds_read_b32 v5, v3 offset:0
)ASM"
R"ASM(
Mvaa 176,28,48
Ta 12,19,16384
Ta 14,19,17408
Mvaa 180,28,52
Ba 88,25,12288
s_waitcnt lgkmcnt(3)
v_cmp_gt_u32 vcc, s44, v5
Mvaa 184,28,56
Mvaa 188,28,60
s_cbranch_vccnz Lspin313_%=
Lback313_%=:
Mvaa 192,0,48
Ta 16,12,40960
Ta 18,12,41984
Mvaa 196,0,52
s_add_u32 m0, s42, 0
Mvaa 200,0,56
global_load_lds_dwordx4 v10, s[38:39] offset:0
Mvaa 204,0,60
Mvaa 208,4,48
Ta 20,15,40960
Ta 22,15,41984
Mvaa 212,4,52
Ba 72,24,12288
Mvaa 216,4,56
Ba 76,24,14336
Mvaa 220,4,60
Ba 92,25,14336
Mvaa 224,8,48
Ta 24,13,40960
Ta 26,13,41984
Mvaa 228,8,52
s_add_u32 m0, s42, 0
Mvaa 232,8,56
global_load_lds_dwordx4 v11, s[38:39] offset:1024
Mvaa 236,8,60
s_waitcnt lgkmcnt(10)
Maaa 144,12,48
Ta 28,16,40960
Ta 30,16,41984
Maaa 148,12,52
Maaa 152,12,56
global_store_dwordx4 v27, a[160:163], s[58:59] offset:2048 sc1
Maaa 156,12,60
s_waitcnt lgkmcnt(4)
Mvaa 32,16,64
Ta 0,14,40960
Ta 2,14,41984
Mvaa 36,16,68
s_add_u32 m0, s42, 0
Mvaa 40,16,72
global_load_lds_dwordx4 v10, s[38:39] offset:2048
Mvaa 44,16,76
Mvaa 80,20,64
Ta 4,17,40960
Ta 6,17,41984
Mvaa 84,20,68
Mvaa 88,20,72
Mvaa 92,20,76
s_waitcnt lgkmcnt(6)
Mvaa 48,24,64
Ta 8,12,32768
Ta 10,12,33792
Mvaa 52,24,68
s_add_u32 m0, s42, 0
Mvaa 56,24,72
global_load_lds_dwordx4 v11, s[38:39] offset:3072
Mvaa 60,24,76
s_waitcnt lgkmcnt(6)
s_add_u32 s38, s38, 16384
s_addc_u32 s39, s39, 0
Mvaa 96,28,64
Ta 12,13,32768
Ta 14,13,33792
Mvaa 100,28,68
s_lshl_b32 s50, s41, 2
s_add_u32 s50, s50, s35
s_lshl_b32 s50, s50, 10
s_add_u32 s50, s50, 42008576
v_and_b32 v30, 0xf0, v27
v_add_u32 v30, s50, v30
Mvaa 104,28,72
global_load_dwordx4 v[240:243], v30, s[28:29] offset:0 sc1
global_load_dwordx4 v[244:247], v30, s[28:29] offset:256 sc1
global_load_dwordx4 v[248:251], v30, s[28:29] offset:512 sc1
global_load_dwordx4 v[252:255], v30, s[28:29] offset:768 sc1
Mvaa 108,28,76
s_waitcnt lgkmcnt(6)
Mvaa 64,0,64
Ta 16,14,32768
Ta 18,14,33792
Mvaa 68,0,68
Mvaa 72,0,72
Mvaa 76,0,76
s_waitcnt lgkmcnt(6)
s_waitcnt vmcnt(14)
ds_write_b32 v2, v4 offset:0
Mvaa 112,4,64
Ta 20,15,32768
Ta 22,15,33792
Mvaa 116,4,68
s_add_u32 s44, s44, 1
v_add_u32 v4, 1, v4
Mvaa 120,4,72
Mvaa 124,4,76
s_waitcnt lgkmcnt(7)
Mvaa 128,8,64
Ta 24,16,32768
Ta 26,16,33792
Mvaa 132,8,68
Mvaa 136,8,72
Mvaa 140,8,76
s_waitcnt lgkmcnt(7)
Mvaa 144,12,64
Ta 28,17,32768
Ta 30,17,33792
Mvaa 148,12,68
Mvaa 152,12,72
Mvaa 156,12,76
s_waitcnt lgkmcnt(7)
Mvaa 160,16,64
Ta 0,18,32768
Ta 2,18,33792
Mvaa 164,16,68
Mvaa 168,16,72
Mvaa 172,16,76
s_waitcnt lgkmcnt(6)
ds_read_b32 v5, v3 offset:0
Mvaa 176,20,64
Ta 4,19,32768
Ta 6,19,33792
Mvaa 180,20,68
s_waitcnt lgkmcnt(2)
v_cmp_gt_u32 vcc, s44, v5
Mvaa 184,20,72
Mvaa 188,20,76
s_cbranch_vccnz Lspin314_%=
Lback314_%=:
Mvaa 192,24,64
Ta 8,12,57344
Ta 10,12,58368
Mvaa 196,24,68
s_add_u32 m0, s42, 16384
Mvaa 200,24,72
global_load_lds_dwordx4 v10, s[38:39] offset:0
Mvaa 204,24,76
Mvaa 208,28,64
Ta 12,15,57344
Ta 14,15,58368
Mvaa 212,28,68
Mvaa 216,28,72
Mvaa 220,28,76
Mvaa 224,0,64
Ta 16,13,57344
Ta 18,13,58368
Mvaa 228,0,68
s_add_u32 m0, s42, 16384
Mvaa 232,0,72
global_load_lds_dwordx4 v11, s[38:39] offset:1024
Mvaa 236,0,76
s_waitcnt lgkmcnt(6)
Maaa 144,4,64
Ta 20,16,57344
Ta 22,16,58368
Maaa 148,4,68
Maaa 152,4,72
global_store_dwordx4 v27, a[160:163], s[58:59] offset:3072 sc1
Maaa 156,4,76
s_waitcnt lgkmcnt(6)
Mvaa 32,8,80
Ta 24,14,57344
Ta 26,14,58368
Mvaa 36,8,84
s_add_u32 m0, s42, 16384
Mvaa 40,8,88
global_load_lds_dwordx4 v10, s[38:39] offset:2048
Mvaa 44,8,92
s_waitcnt lgkmcnt(6)
Mvaa 80,12,80
Ta 28,17,57344
Ta 30,17,58368
Mvaa 84,12,84
Mvaa 88,12,88
Mvaa 92,12,92
s_waitcnt lgkmcnt(6)
Mvaa 48,16,80
Ta 0,12,49152
Ta 2,12,50176
Mvaa 52,16,84
s_add_u32 m0, s42, 16384
Mvaa 56,16,88
global_load_lds_dwordx4 v11, s[38:39] offset:3072
Mvaa 60,16,92
s_waitcnt lgkmcnt(6)
s_add_u32 s38, s38, 16384
s_addc_u32 s39, s39, 0
Mvaa 96,20,80
Ta 4,13,49152
Ta 6,13,50176
Mvaa 100,20,84
)ASM"
R"ASM(
Mvaa 104,20,88
Mvaa 108,20,92
s_waitcnt lgkmcnt(6)
Mvaa 64,24,80
Ta 8,14,49152
Ta 10,14,50176
Mvaa 68,24,84
Mvaa 72,24,88
Mvaa 76,24,92
s_waitcnt lgkmcnt(6)
s_waitcnt vmcnt(14)
ds_write_b32 v2, v4 offset:0
Mvaa 112,28,80
Ta 12,15,49152
Ta 14,15,50176
Mvaa 116,28,84
s_add_u32 s44, s44, 1
v_add_u32 v4, 1, v4
Mvaa 120,28,88
s_waitcnt vmcnt(5)
v_xor_b32 v31, s47, v241
Mvaa 124,28,92
s_waitcnt lgkmcnt(7)
v_xor_b32 v243, s47, v243
v_xor_b32 v245, s47, v245
Mvaa 128,0,80
Ta 16,16,49152
Ta 18,16,50176
Mvaa 132,0,84
v_xor_b32 v247, s47, v247
v_xor_b32 v249, s47, v249
Mvaa 136,0,88
v_xor_b32 v251, s47, v251
v_xor_b32 v253, s47, v253
Mvaa 140,0,92
s_waitcnt lgkmcnt(7)
v_xor_b32 v255, s47, v255
v_or3_b32 v31, v31, v243, v245
Mvaa 144,4,80
Ta 20,17,49152
Ta 22,17,50176
Mvaa 148,4,84
v_or3_b32 v31, v31, v247, v249
v_or3_b32 v31, v31, v251, v253
Mvaa 152,4,88
v_or_b32 v31, v31, v255
v_cmp_ne_u32 vcc, 0, v31
Mvaa 156,4,92
s_waitcnt lgkmcnt(7)
s_cbranch_vccnz Lstat_%=
Lstatok_%=:
v_mov_b32 v20, v240
Mvaa 160,8,80
Ta 24,18,49152
Ta 26,18,50176
Mvaa 164,8,84
v_mov_b32 v21, v244
v_mov_b32 v22, v248
Mvaa 168,8,88
v_mov_b32 v23, v252
v_rcp_f32 v28, v242
Mvaa 172,8,92
s_waitcnt lgkmcnt(6)
ds_read_b32 v5, v3 offset:0
v_rcp_f32 v29, v246
v_rcp_f32 v30, v250
Mvaa 176,12,80
Ta 28,19,49152
Ta 30,19,50176
Mvaa 180,12,84
s_waitcnt lgkmcnt(2)
v_cmp_gt_u32 vcc, s44, v5
Mvaa 184,12,88
Mvaa 188,12,92
s_cbranch_vccnz Lspin315_%=
Lback315_%=:
Mvaa 192,16,80
Mvaa 196,16,84
s_add_u32 m0, s34, 16384
v_rcp_f32 v31, v254
v_rsq_f32 v24, v242
Mvaa 200,16,88
global_load_lds_dwordx4 v6, s[36:37] offset:0
Mvaa 204,16,92
v_rsq_f32 v25, v246
v_rsq_f32 v26, v250
Mvaa 208,20,80
Mvaa 212,20,84
s_add_u32 m0, s34, 16384
v_rsq_f32 v27, v254
v_mul_f32 v24, 0x3e0293ee, v24
Mvaa 216,20,88
global_load_lds_dwordx4 v7, s[36:37] offset:1024
Mvaa 220,20,92
v_mul_f32 v25, 0x3e0293ee, v25
v_mul_f32 v26, 0x3e0293ee, v26
Mvaa 224,24,80
Mvaa 228,24,84
s_add_u32 m0, s34, 16384
v_mul_f32 v27, 0x3e0293ee, v27
Mvaa 232,24,88
global_load_lds_dwordx4 v8, s[36:37] offset:2048
Mvaa 236,24,92
s_waitcnt lgkmcnt(0)
Maaa 144,28,80
Maaa 148,28,84
s_add_u32 m0, s34, 16384
Maaa 152,28,88
global_load_lds_dwordx4 v9, s[36:37] offset:3072
Maaa 156,28,92
s_add_u32 s36, s36, 16384
s_addc_u32 s37, s37, 0
Bv 12,0,512
Bv 16,0,704
v_lshlrev_b32 v5, 4, v1
s_waitcnt lgkmcnt(0)
v_pk_fma_f32 v[32:33], v[12:13], v[20:21], v[32:33] op_sel:[0,0,0] op_sel_hi:[1,0,1]
v_pk_fma_f32 v[34:35], v[14:15], v[20:21], v[34:35] op_sel:[0,0,0] op_sel_hi:[1,0,1]
v_pk_fma_f32 v[80:81], v[16:17], v[20:21], v[80:81] op_sel:[0,0,0] op_sel_hi:[1,0,1]
v_pk_fma_f32 v[82:83], v[18:19], v[20:21], v[82:83] op_sel:[0,0,0] op_sel_hi:[1,0,1]
v_pk_mul_f32 v[32:33], v[32:33], v[28:29] op_sel:[0,0] op_sel_hi:[1,0]
v_pk_mul_f32 v[34:35], v[34:35], v[28:29] op_sel:[0,0] op_sel_hi:[1,0]
v_pk_mul_f32 v[32:33], v[32:33], v[80:81]
v_pk_mul_f32 v[34:35], v[34:35], v[82:83]
CV 80,32,33
CV 81,34,35
v_pk_fma_f32 v[36:37], v[12:13], v[20:21], v[36:37] op_sel:[0,1,0] op_sel_hi:[1,1,1]
v_pk_fma_f32 v[38:39], v[14:15], v[20:21], v[38:39] op_sel:[0,1,0] op_sel_hi:[1,1,1]
v_pk_fma_f32 v[84:85], v[16:17], v[20:21], v[84:85] op_sel:[0,1,0] op_sel_hi:[1,1,1]
v_pk_fma_f32 v[86:87], v[18:19], v[20:21], v[86:87] op_sel:[0,1,0] op_sel_hi:[1,1,1]
v_pk_mul_f32 v[36:37], v[36:37], v[28:29] op_sel:[0,1] op_sel_hi:[1,1]
v_pk_mul_f32 v[38:39], v[38:39], v[28:29] op_sel:[0,1] op_sel_hi:[1,1]
v_pk_mul_f32 v[36:37], v[36:37], v[84:85]
v_pk_mul_f32 v[38:39], v[38:39], v[86:87]
CV 84,36,37
CV 85,38,39
v_pk_fma_f32 v[40:41], v[12:13], v[22:23], v[40:41] op_sel:[0,0,0] op_sel_hi:[1,0,1]
v_pk_fma_f32 v[42:43], v[14:15], v[22:23], v[42:43] op_sel:[0,0,0] op_sel_hi:[1,0,1]
v_pk_fma_f32 v[88:89], v[16:17], v[22:23], v[88:89] op_sel:[0,0,0] op_sel_hi:[1,0,1]
v_pk_fma_f32 v[90:91], v[18:19], v[22:23], v[90:91] op_sel:[0,0,0] op_sel_hi:[1,0,1]
v_pk_mul_f32 v[40:41], v[40:41], v[30:31] op_sel:[0,0] op_sel_hi:[1,0]
v_pk_mul_f32 v[42:43], v[42:43], v[30:31] op_sel:[0,0] op_sel_hi:[1,0]
v_pk_mul_f32 v[40:41], v[40:41], v[88:89]
v_pk_mul_f32 v[42:43], v[42:43], v[90:91]
CV 88,40,41
CV 89,42,43
v_pk_fma_f32 v[44:45], v[12:13], v[22:23], v[44:45] op_sel:[0,1,0] op_sel_hi:[1,1,1]
v_pk_fma_f32 v[46:47], v[14:15], v[22:23], v[46:47] op_sel:[0,1,0] op_sel_hi:[1,1,1]
v_pk_fma_f32 v[92:93], v[16:17], v[22:23], v[92:93] op_sel:[0,1,0] op_sel_hi:[1,1,1]
v_pk_fma_f32 v[94:95], v[18:19], v[22:23], v[94:95] op_sel:[0,1,0] op_sel_hi:[1,1,1]
v_pk_mul_f32 v[44:45], v[44:45], v[30:31] op_sel:[0,1] op_sel_hi:[1,1]
v_pk_mul_f32 v[46:47], v[46:47], v[30:31] op_sel:[0,1] op_sel_hi:[1,1]
v_pk_mul_f32 v[44:45], v[44:45], v[92:93]
v_pk_mul_f32 v[46:47], v[46:47], v[94:95]
CV 92,44,45
CV 93,46,47
Bv 32,0,576
Bv 36,0,768
Bv 40,0,640
Bv 44,0,832
Bv 12,0,0
Bv 16,0,64
s_waitcnt lgkmcnt(4)
v_pk_fma_f32 v[48:49], v[32:33], v[20:21], v[48:49] op_sel:[0,0,0] op_sel_hi:[1,0,1]
v_pk_fma_f32 v[50:51], v[34:35], v[20:21], v[50:51] op_sel:[0,0,0] op_sel_hi:[1,0,1]
v_pk_fma_f32 v[96:97], v[36:37], v[20:21], v[96:97] op_sel:[0,0,0] op_sel_hi:[1,0,1]
v_pk_fma_f32 v[98:99], v[38:39], v[20:21], v[98:99] op_sel:[0,0,0] op_sel_hi:[1,0,1]
v_pk_mul_f32 v[48:49], v[48:49], v[28:29] op_sel:[0,0] op_sel_hi:[1,0]
v_pk_mul_f32 v[50:51], v[50:51], v[28:29] op_sel:[0,0] op_sel_hi:[1,0]
v_pk_mul_f32 v[48:49], v[48:49], v[96:97]
v_pk_mul_f32 v[50:51], v[50:51], v[98:99]
CV 82,48,49
CV 83,50,51
global_store_dwordx4 v5, v[80:83], s[74:75] sc1
v_pk_fma_f32 v[52:53], v[32:33], v[20:21], v[52:53] op_sel:[0,1,0] op_sel_hi:[1,1,1]
v_pk_fma_f32 v[54:55], v[34:35], v[20:21], v[54:55] op_sel:[0,1,0] op_sel_hi:[1,1,1]
v_pk_fma_f32 v[100:101], v[36:37], v[20:21], v[100:101] op_sel:[0,1,0] op_sel_hi:[1,1,1]
v_pk_fma_f32 v[102:103], v[38:39], v[20:21], v[102:103] op_sel:[0,1,0] op_sel_hi:[1,1,1]
v_pk_mul_f32 v[52:53], v[52:53], v[28:29] op_sel:[0,1] op_sel_hi:[1,1]
v_pk_mul_f32 v[54:55], v[54:55], v[28:29] op_sel:[0,1] op_sel_hi:[1,1]
v_pk_mul_f32 v[52:53], v[52:53], v[100:101]
v_pk_mul_f32 v[54:55], v[54:55], v[102:103]
CV 86,52,53
CV 87,54,55
v_add_u32 v102, 45056, v5
global_store_dwordx4 v102, v[84:87], s[74:75] sc1
v_pk_fma_f32 v[56:57], v[32:33], v[22:23], v[56:57] op_sel:[0,0,0] op_sel_hi:[1,0,1]
v_pk_fma_f32 v[58:59], v[34:35], v[22:23], v[58:59] op_sel:[0,0,0] op_sel_hi:[1,0,1]
v_pk_fma_f32 v[104:105], v[36:37], v[22:23], v[104:105] op_sel:[0,0,0] op_sel_hi:[1,0,1]
v_pk_fma_f32 v[106:107], v[38:39], v[22:23], v[106:107] op_sel:[0,0,0] op_sel_hi:[1,0,1]
v_pk_mul_f32 v[56:57], v[56:57], v[30:31] op_sel:[0,0] op_sel_hi:[1,0]
v_pk_mul_f32 v[58:59], v[58:59], v[30:31] op_sel:[0,0] op_sel_hi:[1,0]
v_pk_mul_f32 v[56:57], v[56:57], v[104:105]
v_pk_mul_f32 v[58:59], v[58:59], v[106:107]
CV 90,56,57
CV 91,58,59
v_add_u32 v106, 90112, v5
global_store_dwordx4 v106, v[88:91], s[74:75] sc1
v_pk_fma_f32 v[60:61], v[32:33], v[22:23], v[60:61] op_sel:[0,1,0] op_sel_hi:[1,1,1]
v_pk_fma_f32 v[62:63], v[34:35], v[22:23], v[62:63] op_sel:[0,1,0] op_sel_hi:[1,1,1]
)ASM"
R"ASM(
v_pk_fma_f32 v[108:109], v[36:37], v[22:23], v[108:109] op_sel:[0,1,0] op_sel_hi:[1,1,1]
v_pk_fma_f32 v[110:111], v[38:39], v[22:23], v[110:111] op_sel:[0,1,0] op_sel_hi:[1,1,1]
v_pk_mul_f32 v[60:61], v[60:61], v[30:31] op_sel:[0,1] op_sel_hi:[1,1]
v_pk_mul_f32 v[62:63], v[62:63], v[30:31] op_sel:[0,1] op_sel_hi:[1,1]
v_pk_mul_f32 v[60:61], v[60:61], v[108:109]
v_pk_mul_f32 v[62:63], v[62:63], v[110:111]
CV 94,60,61
CV 95,62,63
v_add_u32 v110, 135168, v5
global_store_dwordx4 v110, v[92:95], s[74:75] sc1
s_waitcnt lgkmcnt(2)
v_pk_fma_f32 v[64:65], v[40:41], v[20:21], v[64:65] op_sel:[0,0,0] op_sel_hi:[1,0,1]
v_pk_fma_f32 v[66:67], v[42:43], v[20:21], v[66:67] op_sel:[0,0,0] op_sel_hi:[1,0,1]
v_pk_fma_f32 v[112:113], v[44:45], v[20:21], v[112:113] op_sel:[0,0,0] op_sel_hi:[1,0,1]
v_pk_fma_f32 v[114:115], v[46:47], v[20:21], v[114:115] op_sel:[0,0,0] op_sel_hi:[1,0,1]
v_pk_mul_f32 v[64:65], v[64:65], v[28:29] op_sel:[0,0] op_sel_hi:[1,0]
v_pk_mul_f32 v[66:67], v[66:67], v[28:29] op_sel:[0,0] op_sel_hi:[1,0]
v_pk_mul_f32 v[64:65], v[64:65], v[112:113]
v_pk_mul_f32 v[66:67], v[66:67], v[114:115]
CV 112,64,65
CV 113,66,67
global_store_dwordx2 v5, v[112:113], s[76:77] sc1
v_pk_fma_f32 v[68:69], v[40:41], v[20:21], v[68:69] op_sel:[0,1,0] op_sel_hi:[1,1,1]
v_pk_fma_f32 v[70:71], v[42:43], v[20:21], v[70:71] op_sel:[0,1,0] op_sel_hi:[1,1,1]
v_pk_fma_f32 v[116:117], v[44:45], v[20:21], v[116:117] op_sel:[0,1,0] op_sel_hi:[1,1,1]
v_pk_fma_f32 v[118:119], v[46:47], v[20:21], v[118:119] op_sel:[0,1,0] op_sel_hi:[1,1,1]
v_pk_mul_f32 v[68:69], v[68:69], v[28:29] op_sel:[0,1] op_sel_hi:[1,1]
v_pk_mul_f32 v[70:71], v[70:71], v[28:29] op_sel:[0,1] op_sel_hi:[1,1]
v_pk_mul_f32 v[68:69], v[68:69], v[116:117]
v_pk_mul_f32 v[70:71], v[70:71], v[118:119]
CV 116,68,69
CV 117,70,71
v_add_u32 v118, 45056, v5
global_store_dwordx2 v118, v[116:117], s[76:77] sc1
v_pk_fma_f32 v[72:73], v[40:41], v[22:23], v[72:73] op_sel:[0,0,0] op_sel_hi:[1,0,1]
v_pk_fma_f32 v[74:75], v[42:43], v[22:23], v[74:75] op_sel:[0,0,0] op_sel_hi:[1,0,1]
v_pk_fma_f32 v[120:121], v[44:45], v[22:23], v[120:121] op_sel:[0,0,0] op_sel_hi:[1,0,1]
v_pk_fma_f32 v[122:123], v[46:47], v[22:23], v[122:123] op_sel:[0,0,0] op_sel_hi:[1,0,1]
v_pk_mul_f32 v[72:73], v[72:73], v[30:31] op_sel:[0,0] op_sel_hi:[1,0]
v_pk_mul_f32 v[74:75], v[74:75], v[30:31] op_sel:[0,0] op_sel_hi:[1,0]
v_pk_mul_f32 v[72:73], v[72:73], v[120:121]
v_pk_mul_f32 v[74:75], v[74:75], v[122:123]
CV 120,72,73
CV 121,74,75
v_add_u32 v122, 90112, v5
global_store_dwordx2 v122, v[120:121], s[76:77] sc1
v_pk_fma_f32 v[76:77], v[40:41], v[22:23], v[76:77] op_sel:[0,1,0] op_sel_hi:[1,1,1]
v_pk_fma_f32 v[78:79], v[42:43], v[22:23], v[78:79] op_sel:[0,1,0] op_sel_hi:[1,1,1]
v_pk_fma_f32 v[124:125], v[44:45], v[22:23], v[124:125] op_sel:[0,1,0] op_sel_hi:[1,1,1]
v_pk_fma_f32 v[126:127], v[46:47], v[22:23], v[126:127] op_sel:[0,1,0] op_sel_hi:[1,1,1]
v_pk_mul_f32 v[76:77], v[76:77], v[30:31] op_sel:[0,1] op_sel_hi:[1,1]
v_pk_mul_f32 v[78:79], v[78:79], v[30:31] op_sel:[0,1] op_sel_hi:[1,1]
v_pk_mul_f32 v[76:77], v[76:77], v[124:125]
v_pk_mul_f32 v[78:79], v[78:79], v[126:127]
CV 124,76,77
CV 125,78,79
v_add_u32 v126, 135168, v5
global_store_dwordx2 v126, v[124:125], s[76:77] sc1
Bv 32,0,128
Bv 36,0,192
v_accvgpr_read_b32 v240, a144
v_accvgpr_read_b32 v241, a145
v_accvgpr_read_b32 v242, a146
v_accvgpr_read_b32 v243, a147
v_accvgpr_read_b32 v244, a148
v_accvgpr_read_b32 v245, a149
v_accvgpr_read_b32 v246, a150
v_accvgpr_read_b32 v247, a151
v_accvgpr_read_b32 v248, a152
v_accvgpr_read_b32 v249, a153
v_accvgpr_read_b32 v250, a154
v_accvgpr_read_b32 v251, a155
v_accvgpr_read_b32 v252, a156
v_accvgpr_read_b32 v253, a157
v_accvgpr_read_b32 v254, a158
v_accvgpr_read_b32 v255, a159
s_waitcnt lgkmcnt(2)
v_pk_fma_f32 v[128:129], v[12:13], v[20:21], v[128:129] op_sel:[0,0,0] op_sel_hi:[1,0,1]
v_pk_fma_f32 v[130:131], v[14:15], v[20:21], v[130:131] op_sel:[0,0,0] op_sel_hi:[1,0,1]
v_pk_fma_f32 v[144:145], v[16:17], v[20:21], v[144:145] op_sel:[0,0,0] op_sel_hi:[1,0,1]
v_pk_fma_f32 v[146:147], v[18:19], v[20:21], v[146:147] op_sel:[0,0,0] op_sel_hi:[1,0,1]
v_pk_mul_f32 v[128:129], v[128:129], v[24:25] op_sel:[0,0] op_sel_hi:[1,0]
v_pk_mul_f32 v[130:131], v[130:131], v[24:25] op_sel:[0,0] op_sel_hi:[1,0]
v_pk_mul_f32 v[144:145], v[144:145], v[24:25] op_sel:[0,0] op_sel_hi:[1,0]
v_pk_mul_f32 v[146:147], v[146:147], v[24:25] op_sel:[0,0] op_sel_hi:[1,0]
CV 48,128,129
CV 49,130,131
CV 50,144,145
CV 51,146,147
v_pk_fma_f32 v[132:133], v[12:13], v[20:21], v[132:133] op_sel:[0,1,0] op_sel_hi:[1,1,1]
v_pk_fma_f32 v[134:135], v[14:15], v[20:21], v[134:135] op_sel:[0,1,0] op_sel_hi:[1,1,1]
v_pk_fma_f32 v[148:149], v[16:17], v[20:21], v[148:149] op_sel:[0,1,0] op_sel_hi:[1,1,1]
v_pk_fma_f32 v[150:151], v[18:19], v[20:21], v[150:151] op_sel:[0,1,0] op_sel_hi:[1,1,1]
v_pk_mul_f32 v[132:133], v[132:133], v[24:25] op_sel:[0,1] op_sel_hi:[1,1]
v_pk_mul_f32 v[134:135], v[134:135], v[24:25] op_sel:[0,1] op_sel_hi:[1,1]
v_pk_mul_f32 v[148:149], v[148:149], v[24:25] op_sel:[0,1] op_sel_hi:[1,1]
v_pk_mul_f32 v[150:151], v[150:151], v[24:25] op_sel:[0,1] op_sel_hi:[1,1]
CV 64,132,133
CV 65,134,135
CV 66,148,149
CV 67,150,151
v_pk_fma_f32 v[136:137], v[12:13], v[22:23], v[136:137] op_sel:[0,0,0] op_sel_hi:[1,0,1]
v_pk_fma_f32 v[138:139], v[14:15], v[22:23], v[138:139] op_sel:[0,0,0] op_sel_hi:[1,0,1]
v_pk_fma_f32 v[152:153], v[16:17], v[22:23], v[152:153] op_sel:[0,0,0] op_sel_hi:[1,0,1]
v_pk_fma_f32 v[154:155], v[18:19], v[22:23], v[154:155] op_sel:[0,0,0] op_sel_hi:[1,0,1]
v_pk_mul_f32 v[136:137], v[136:137], v[26:27] op_sel:[0,0] op_sel_hi:[1,0]
v_pk_mul_f32 v[138:139], v[138:139], v[26:27] op_sel:[0,0] op_sel_hi:[1,0]
v_pk_mul_f32 v[152:153], v[152:153], v[26:27] op_sel:[0,0] op_sel_hi:[1,0]
v_pk_mul_f32 v[154:155], v[154:155], v[26:27] op_sel:[0,0] op_sel_hi:[1,0]
CV 80,136,137
CV 81,138,139
CV 82,152,153
CV 83,154,155
v_pk_fma_f32 v[140:141], v[12:13], v[22:23], v[140:141] op_sel:[0,1,0] op_sel_hi:[1,1,1]
v_pk_fma_f32 v[142:143], v[14:15], v[22:23], v[142:143] op_sel:[0,1,0] op_sel_hi:[1,1,1]
v_pk_fma_f32 v[156:157], v[16:17], v[22:23], v[156:157] op_sel:[0,1,0] op_sel_hi:[1,1,1]
v_pk_fma_f32 v[158:159], v[18:19], v[22:23], v[158:159] op_sel:[0,1,0] op_sel_hi:[1,1,1]
v_pk_mul_f32 v[140:141], v[140:141], v[26:27] op_sel:[0,1] op_sel_hi:[1,1]
v_pk_mul_f32 v[142:143], v[142:143], v[26:27] op_sel:[0,1] op_sel_hi:[1,1]
v_pk_mul_f32 v[156:157], v[156:157], v[26:27] op_sel:[0,1] op_sel_hi:[1,1]
v_pk_mul_f32 v[158:159], v[158:159], v[26:27] op_sel:[0,1] op_sel_hi:[1,1]
CV 96,140,141
CV 97,142,143
CV 98,156,157
CV 99,158,159
v_permlane16_swap_b32 v48, v50
v_permlane16_swap_b32 v49, v51
v_permlane16_swap_b32 v64, v66
v_permlane16_swap_b32 v65, v67
v_permlane16_swap_b32 v80, v82
v_permlane16_swap_b32 v81, v83
v_permlane16_swap_b32 v96, v98
v_permlane16_swap_b32 v97, v99
Bv 12,0,256
Bv 16,0,320
s_waitcnt lgkmcnt(2)
v_pk_fma_f32 v[160:161], v[32:33], v[20:21], v[160:161] op_sel:[0,0,0] op_sel_hi:[1,0,1]
v_pk_fma_f32 v[162:163], v[34:35], v[20:21], v[162:163] op_sel:[0,0,0] op_sel_hi:[1,0,1]
v_pk_fma_f32 v[176:177], v[36:37], v[20:21], v[176:177] op_sel:[0,0,0] op_sel_hi:[1,0,1]
v_pk_fma_f32 v[178:179], v[38:39], v[20:21], v[178:179] op_sel:[0,0,0] op_sel_hi:[1,0,1]
v_pk_mul_f32 v[160:161], v[160:161], v[24:25] op_sel:[0,0] op_sel_hi:[1,0]
v_pk_mul_f32 v[162:163], v[162:163], v[24:25] op_sel:[0,0] op_sel_hi:[1,0]
v_pk_mul_f32 v[176:177], v[176:177], v[24:25] op_sel:[0,0] op_sel_hi:[1,0]
v_pk_mul_f32 v[178:179], v[178:179], v[24:25] op_sel:[0,0] op_sel_hi:[1,0]
CV 52,160,161
CV 53,162,163
CV 54,176,177
CV 55,178,179
v_pk_fma_f32 v[164:165], v[32:33], v[20:21], v[164:165] op_sel:[0,1,0] op_sel_hi:[1,1,1]
v_pk_fma_f32 v[166:167], v[34:35], v[20:21], v[166:167] op_sel:[0,1,0] op_sel_hi:[1,1,1]
v_pk_fma_f32 v[180:181], v[36:37], v[20:21], v[180:181] op_sel:[0,1,0] op_sel_hi:[1,1,1]
v_pk_fma_f32 v[182:183], v[38:39], v[20:21], v[182:183] op_sel:[0,1,0] op_sel_hi:[1,1,1]
v_pk_mul_f32 v[164:165], v[164:165], v[24:25] op_sel:[0,1] op_sel_hi:[1,1]
v_pk_mul_f32 v[166:167], v[166:167], v[24:25] op_sel:[0,1] op_sel_hi:[1,1]
v_pk_mul_f32 v[180:181], v[180:181], v[24:25] op_sel:[0,1] op_sel_hi:[1,1]
v_pk_mul_f32 v[182:183], v[182:183], v[24:25] op_sel:[0,1] op_sel_hi:[1,1]
CV 68,164,165
CV 69,166,167
CV 70,180,181
CV 71,182,183
v_pk_fma_f32 v[168:169], v[32:33], v[22:23], v[168:169] op_sel:[0,0,0] op_sel_hi:[1,0,1]
v_pk_fma_f32 v[170:171], v[34:35], v[22:23], v[170:171] op_sel:[0,0,0] op_sel_hi:[1,0,1]
v_pk_fma_f32 v[184:185], v[36:37], v[22:23], v[184:185] op_sel:[0,0,0] op_sel_hi:[1,0,1]
v_pk_fma_f32 v[186:187], v[38:39], v[22:23], v[186:187] op_sel:[0,0,0] op_sel_hi:[1,0,1]
v_pk_mul_f32 v[168:169], v[168:169], v[26:27] op_sel:[0,0] op_sel_hi:[1,0]
v_pk_mul_f32 v[170:171], v[170:171], v[26:27] op_sel:[0,0] op_sel_hi:[1,0]
v_pk_mul_f32 v[184:185], v[184:185], v[26:27] op_sel:[0,0] op_sel_hi:[1,0]
v_pk_mul_f32 v[186:187], v[186:187], v[26:27] op_sel:[0,0] op_sel_hi:[1,0]
CV 84,168,169
CV 85,170,171
CV 86,184,185
CV 87,186,187
v_pk_fma_f32 v[172:173], v[32:33], v[22:23], v[172:173] op_sel:[0,1,0] op_sel_hi:[1,1,1]
v_pk_fma_f32 v[174:175], v[34:35], v[22:23], v[174:175] op_sel:[0,1,0] op_sel_hi:[1,1,1]
v_pk_fma_f32 v[188:189], v[36:37], v[22:23], v[188:189] op_sel:[0,1,0] op_sel_hi:[1,1,1]
v_pk_fma_f32 v[190:191], v[38:39], v[22:23], v[190:191] op_sel:[0,1,0] op_sel_hi:[1,1,1]
v_pk_mul_f32 v[172:173], v[172:173], v[26:27] op_sel:[0,1] op_sel_hi:[1,1]
v_pk_mul_f32 v[174:175], v[174:175], v[26:27] op_sel:[0,1] op_sel_hi:[1,1]
v_pk_mul_f32 v[188:189], v[188:189], v[26:27] op_sel:[0,1] op_sel_hi:[1,1]
v_pk_mul_f32 v[190:191], v[190:191], v[26:27] op_sel:[0,1] op_sel_hi:[1,1]
CV 100,172,173
CV 101,174,175
CV 102,188,189
CV 103,190,191
v_permlane16_swap_b32 v52, v54
v_permlane16_swap_b32 v53, v55
v_permlane16_swap_b32 v68, v70
v_permlane16_swap_b32 v69, v71
v_permlane16_swap_b32 v84, v86
v_permlane16_swap_b32 v85, v87
v_permlane16_swap_b32 v100, v102
v_permlane16_swap_b32 v101, v103
Bv 32,0,384
Bv 36,0,448
s_waitcnt lgkmcnt(2)
v_pk_fma_f32 v[192:193], v[12:13], v[20:21], v[192:193] op_sel:[0,0,0] op_sel_hi:[1,0,1]
v_pk_fma_f32 v[194:195], v[14:15], v[20:21], v[194:195] op_sel:[0,0,0] op_sel_hi:[1,0,1]
v_pk_fma_f32 v[208:209], v[16:17], v[20:21], v[208:209] op_sel:[0,0,0] op_sel_hi:[1,0,1]
v_pk_fma_f32 v[210:211], v[18:19], v[20:21], v[210:211] op_sel:[0,0,0] op_sel_hi:[1,0,1]
v_pk_mul_f32 v[192:193], v[192:193], v[24:25] op_sel:[0,0] op_sel_hi:[1,0]
)ASM"
R"ASM(
v_pk_mul_f32 v[194:195], v[194:195], v[24:25] op_sel:[0,0] op_sel_hi:[1,0]
v_pk_mul_f32 v[208:209], v[208:209], v[24:25] op_sel:[0,0] op_sel_hi:[1,0]
v_pk_mul_f32 v[210:211], v[210:211], v[24:25] op_sel:[0,0] op_sel_hi:[1,0]
CV 56,192,193
CV 57,194,195
CV 58,208,209
CV 59,210,211
v_pk_fma_f32 v[196:197], v[12:13], v[20:21], v[196:197] op_sel:[0,1,0] op_sel_hi:[1,1,1]
v_pk_fma_f32 v[198:199], v[14:15], v[20:21], v[198:199] op_sel:[0,1,0] op_sel_hi:[1,1,1]
v_pk_fma_f32 v[212:213], v[16:17], v[20:21], v[212:213] op_sel:[0,1,0] op_sel_hi:[1,1,1]
v_pk_fma_f32 v[214:215], v[18:19], v[20:21], v[214:215] op_sel:[0,1,0] op_sel_hi:[1,1,1]
v_pk_mul_f32 v[196:197], v[196:197], v[24:25] op_sel:[0,1] op_sel_hi:[1,1]
v_pk_mul_f32 v[198:199], v[198:199], v[24:25] op_sel:[0,1] op_sel_hi:[1,1]
v_pk_mul_f32 v[212:213], v[212:213], v[24:25] op_sel:[0,1] op_sel_hi:[1,1]
v_pk_mul_f32 v[214:215], v[214:215], v[24:25] op_sel:[0,1] op_sel_hi:[1,1]
CV 72,196,197
CV 73,198,199
CV 74,212,213
CV 75,214,215
v_pk_fma_f32 v[200:201], v[12:13], v[22:23], v[200:201] op_sel:[0,0,0] op_sel_hi:[1,0,1]
v_pk_fma_f32 v[202:203], v[14:15], v[22:23], v[202:203] op_sel:[0,0,0] op_sel_hi:[1,0,1]
v_pk_fma_f32 v[216:217], v[16:17], v[22:23], v[216:217] op_sel:[0,0,0] op_sel_hi:[1,0,1]
v_pk_fma_f32 v[218:219], v[18:19], v[22:23], v[218:219] op_sel:[0,0,0] op_sel_hi:[1,0,1]
v_pk_mul_f32 v[200:201], v[200:201], v[26:27] op_sel:[0,0] op_sel_hi:[1,0]
v_pk_mul_f32 v[202:203], v[202:203], v[26:27] op_sel:[0,0] op_sel_hi:[1,0]
v_pk_mul_f32 v[216:217], v[216:217], v[26:27] op_sel:[0,0] op_sel_hi:[1,0]
v_pk_mul_f32 v[218:219], v[218:219], v[26:27] op_sel:[0,0] op_sel_hi:[1,0]
CV 88,200,201
CV 89,202,203
CV 90,216,217
CV 91,218,219
v_pk_fma_f32 v[204:205], v[12:13], v[22:23], v[204:205] op_sel:[0,1,0] op_sel_hi:[1,1,1]
v_pk_fma_f32 v[206:207], v[14:15], v[22:23], v[206:207] op_sel:[0,1,0] op_sel_hi:[1,1,1]
v_pk_fma_f32 v[220:221], v[16:17], v[22:23], v[220:221] op_sel:[0,1,0] op_sel_hi:[1,1,1]
v_pk_fma_f32 v[222:223], v[18:19], v[22:23], v[222:223] op_sel:[0,1,0] op_sel_hi:[1,1,1]
v_pk_mul_f32 v[204:205], v[204:205], v[26:27] op_sel:[0,1] op_sel_hi:[1,1]
v_pk_mul_f32 v[206:207], v[206:207], v[26:27] op_sel:[0,1] op_sel_hi:[1,1]
v_pk_mul_f32 v[220:221], v[220:221], v[26:27] op_sel:[0,1] op_sel_hi:[1,1]
v_pk_mul_f32 v[222:223], v[222:223], v[26:27] op_sel:[0,1] op_sel_hi:[1,1]
CV 104,204,205
CV 105,206,207
CV 106,220,221
CV 107,222,223
v_permlane16_swap_b32 v56, v58
v_permlane16_swap_b32 v57, v59
v_permlane16_swap_b32 v72, v74
v_permlane16_swap_b32 v73, v75
v_permlane16_swap_b32 v88, v90
v_permlane16_swap_b32 v89, v91
v_permlane16_swap_b32 v104, v106
v_permlane16_swap_b32 v105, v107
s_waitcnt lgkmcnt(0)
v_pk_fma_f32 v[224:225], v[32:33], v[20:21], v[224:225] op_sel:[0,0,0] op_sel_hi:[1,0,1]
v_pk_fma_f32 v[226:227], v[34:35], v[20:21], v[226:227] op_sel:[0,0,0] op_sel_hi:[1,0,1]
v_pk_fma_f32 v[240:241], v[36:37], v[20:21], v[240:241] op_sel:[0,0,0] op_sel_hi:[1,0,1]
v_pk_fma_f32 v[242:243], v[38:39], v[20:21], v[242:243] op_sel:[0,0,0] op_sel_hi:[1,0,1]
v_pk_mul_f32 v[224:225], v[224:225], v[24:25] op_sel:[0,0] op_sel_hi:[1,0]
v_pk_mul_f32 v[226:227], v[226:227], v[24:25] op_sel:[0,0] op_sel_hi:[1,0]
v_pk_mul_f32 v[240:241], v[240:241], v[24:25] op_sel:[0,0] op_sel_hi:[1,0]
v_pk_mul_f32 v[242:243], v[242:243], v[24:25] op_sel:[0,0] op_sel_hi:[1,0]
CV 60,224,225
CV 61,226,227
CV 62,240,241
CV 63,242,243
v_pk_fma_f32 v[228:229], v[32:33], v[20:21], v[228:229] op_sel:[0,1,0] op_sel_hi:[1,1,1]
v_pk_fma_f32 v[230:231], v[34:35], v[20:21], v[230:231] op_sel:[0,1,0] op_sel_hi:[1,1,1]
v_pk_fma_f32 v[244:245], v[36:37], v[20:21], v[244:245] op_sel:[0,1,0] op_sel_hi:[1,1,1]
v_pk_fma_f32 v[246:247], v[38:39], v[20:21], v[246:247] op_sel:[0,1,0] op_sel_hi:[1,1,1]
v_pk_mul_f32 v[228:229], v[228:229], v[24:25] op_sel:[0,1] op_sel_hi:[1,1]
v_pk_mul_f32 v[230:231], v[230:231], v[24:25] op_sel:[0,1] op_sel_hi:[1,1]
v_pk_mul_f32 v[244:245], v[244:245], v[24:25] op_sel:[0,1] op_sel_hi:[1,1]
v_pk_mul_f32 v[246:247], v[246:247], v[24:25] op_sel:[0,1] op_sel_hi:[1,1]
CV 76,228,229
CV 77,230,231
CV 78,244,245
CV 79,246,247
v_pk_fma_f32 v[232:233], v[32:33], v[22:23], v[232:233] op_sel:[0,0,0] op_sel_hi:[1,0,1]
v_pk_fma_f32 v[234:235], v[34:35], v[22:23], v[234:235] op_sel:[0,0,0] op_sel_hi:[1,0,1]
v_pk_fma_f32 v[248:249], v[36:37], v[22:23], v[248:249] op_sel:[0,0,0] op_sel_hi:[1,0,1]
v_pk_fma_f32 v[250:251], v[38:39], v[22:23], v[250:251] op_sel:[0,0,0] op_sel_hi:[1,0,1]
v_pk_mul_f32 v[232:233], v[232:233], v[26:27] op_sel:[0,0] op_sel_hi:[1,0]
v_pk_mul_f32 v[234:235], v[234:235], v[26:27] op_sel:[0,0] op_sel_hi:[1,0]
v_pk_mul_f32 v[248:249], v[248:249], v[26:27] op_sel:[0,0] op_sel_hi:[1,0]
v_pk_mul_f32 v[250:251], v[250:251], v[26:27] op_sel:[0,0] op_sel_hi:[1,0]
CV 92,232,233
CV 93,234,235
CV 94,248,249
CV 95,250,251
v_pk_fma_f32 v[236:237], v[32:33], v[22:23], v[236:237] op_sel:[0,1,0] op_sel_hi:[1,1,1]
v_pk_fma_f32 v[238:239], v[34:35], v[22:23], v[238:239] op_sel:[0,1,0] op_sel_hi:[1,1,1]
v_pk_fma_f32 v[252:253], v[36:37], v[22:23], v[252:253] op_sel:[0,1,0] op_sel_hi:[1,1,1]
v_pk_fma_f32 v[254:255], v[38:39], v[22:23], v[254:255] op_sel:[0,1,0] op_sel_hi:[1,1,1]
v_pk_mul_f32 v[236:237], v[236:237], v[26:27] op_sel:[0,1] op_sel_hi:[1,1]
v_pk_mul_f32 v[238:239], v[238:239], v[26:27] op_sel:[0,1] op_sel_hi:[1,1]
v_pk_mul_f32 v[252:253], v[252:253], v[26:27] op_sel:[0,1] op_sel_hi:[1,1]
v_pk_mul_f32 v[254:255], v[254:255], v[26:27] op_sel:[0,1] op_sel_hi:[1,1]
CV 108,236,237
CV 109,238,239
CV 110,252,253
CV 111,254,255
v_accvgpr_read_b32 v12, a112
v_accvgpr_read_b32 v13, a113
v_accvgpr_read_b32 v14, a114
v_accvgpr_read_b32 v15, a115
v_permlane16_swap_b32 v60, v62
v_permlane16_swap_b32 v61, v63
v_permlane16_swap_b32 v76, v78
v_permlane16_swap_b32 v77, v79
v_permlane16_swap_b32 v92, v94
v_permlane16_swap_b32 v93, v95
v_permlane16_swap_b32 v108, v110
v_permlane16_swap_b32 v109, v111
v_accvgpr_read_b32 v16, a116
v_accvgpr_read_b32 v17, a117
v_accvgpr_read_b32 v18, a118
v_accvgpr_read_b32 v19, a119
v_accvgpr_read_b32 v20, a120
v_accvgpr_read_b32 v21, a121
v_accvgpr_read_b32 v22, a122
v_accvgpr_read_b32 v23, a123
v_accvgpr_read_b32 v24, a124
v_accvgpr_read_b32 v25, a125
v_accvgpr_read_b32 v26, a126
v_accvgpr_read_b32 v27, a127
v_accvgpr_read_b32 v240, a128
v_accvgpr_read_b32 v241, a129
v_accvgpr_read_b32 v242, a130
v_accvgpr_read_b32 v243, a131
v_accvgpr_read_b32 v244, a132
v_accvgpr_read_b32 v245, a133
v_accvgpr_read_b32 v246, a134
v_accvgpr_read_b32 v247, a135
v_accvgpr_read_b32 v248, a136
v_accvgpr_read_b32 v249, a137
v_accvgpr_read_b32 v250, a138
v_accvgpr_read_b32 v220, a142
s_mov_b32 s73, 0
s_add_u32 m0, s34, 32768
s_nop 0
global_load_lds_dwordx4 v6, s[36:37] offset:0
global_load_lds_dwordx4 v7, s[36:37] offset:1024
global_load_lds_dwordx4 v8, s[36:37] offset:2048
global_load_lds_dwordx4 v9, s[36:37] offset:3072
s_add_u32 s36, s36, 16384
s_addc_u32 s37, s37, 0
Lhead_%=:
Ba 128,12,0
Ba 132,13,0
Ba 136,14,0
Ba 140,15,0
s_waitcnt lgkmcnt(0)
Zvav 112,128,48
Mvav 112,132,52
Ba 144,12,4096
Mvav 112,136,56
Mvav 112,140,60
Ba 148,13,4096
Zvav 116,128,64
Mvav 116,132,68
Ba 152,14,4096
Mvav 116,136,72
Mvav 116,140,76
Ba 156,15,4096
Zvav 120,128,80
Mvav 120,132,84
Mvav 120,136,88
Mvav 120,140,92
Zvav 124,128,96
Mvav 124,132,100
Mvav 124,136,104
Mvav 124,140,108
s_cmp_lg_u32 s46, 0
s_cbranch_scc1 Lhave_max_%=
v_max3_f32 v208, v112, v113, v114
v_max_f32 v208, v208, v115
v_max3_f32 v209, v116, v117, v118
v_max_f32 v209, v209, v119
v_max3_f32 v210, v120, v121, v122
v_max_f32 v210, v210, v123
v_max3_f32 v211, v124, v125, v126
v_max_f32 v211, v211, v127
v_mov_b32 v212, v208
v_mov_b32 v213, v209
v_mov_b32 v214, v210
v_mov_b32 v215, v211
v_permlane32_swap_b32 v208, v212
v_permlane32_swap_b32 v209, v213
v_permlane32_swap_b32 v210, v214
v_permlane32_swap_b32 v211, v215
v_max_f32 v208, v208, v212
v_max_f32 v209, v209, v213
v_max_f32 v210, v210, v214
v_max_f32 v211, v211, v215
v_mov_b32 v212, v208
v_mov_b32 v213, v209
v_mov_b32 v214, v210
v_mov_b32 v215, v211
v_permlane16_swap_b32 v208, v212
v_permlane16_swap_b32 v209, v213
v_permlane16_swap_b32 v210, v214
)ASM"
R"ASM(
v_permlane16_swap_b32 v211, v215
v_max_f32 v208, v208, v212
v_max_f32 v209, v209, v213
v_max_f32 v210, v210, v214
v_max_f32 v211, v211, v215
v_sub_f32 v32, 0, v208
v_sub_f32 v33, 0, v208
v_sub_f32 v34, 0, v208
v_sub_f32 v35, 0, v208
v_sub_f32 v36, 0, v209
v_sub_f32 v37, 0, v209
v_sub_f32 v38, 0, v209
v_sub_f32 v39, 0, v209
v_sub_f32 v40, 0, v210
v_sub_f32 v41, 0, v210
v_sub_f32 v42, 0, v210
v_sub_f32 v43, 0, v210
v_sub_f32 v44, 0, v211
v_sub_f32 v45, 0, v211
v_sub_f32 v46, 0, v211
v_sub_f32 v47, 0, v211
Lhave_max_%=:
s_nop 7
v_add_f32 v112, v112, v32
v_add_f32 v113, v113, v32
v_add_f32 v114, v114, v32
v_add_f32 v115, v115, v32
v_add_f32 v116, v116, v36
v_add_f32 v117, v117, v36
v_add_f32 v118, v118, v36
v_add_f32 v119, v119, v36
v_add_f32 v120, v120, v40
v_add_f32 v121, v121, v40
v_add_f32 v122, v122, v40
v_add_f32 v123, v123, v40
v_add_f32 v124, v124, v44
v_add_f32 v125, v125, v44
v_add_f32 v126, v126, v44
v_add_f32 v127, v127, v44
s_waitcnt lgkmcnt(0)
Cvav 128,144,48,32
EX 112
Mvav 128,148,52
Ba 128,12,8192
v_mov_b32 v28, v112
Mvav 128,152,56
EX 113
Mvav 128,156,60
Ba 132,13,8192
CV 144,112,113
Cvav 132,144,64,36
EX 114
Mvav 132,148,68
Ba 136,14,8192
v_add_f32 v28, v28, v113
Mvav 132,152,72
EX 115
Mvav 132,156,76
Ba 140,15,8192
CV 145,114,115
Cvav 136,144,80,40
EX 116
Mvav 136,148,84
v_add_f32 v28, v28, v114
Mvav 136,152,88
EX 117
Mvav 136,156,92
CV 148,116,117
Cvav 140,144,96,44
EX 118
Mvav 140,148,100
v_add_f32 v28, v28, v115
Mvav 140,152,104
EX 119
Mvav 140,156,108
CV 149,118,119
EX 120
v_mov_b32 v29, v116
EX 121
s_nop 0
CV 152,120,121
EX 122
v_add_f32 v29, v29, v117
EX 123
s_nop 0
CV 153,122,123
EX 124
EX 125
s_nop 0
CV 156,124,125
v_add_f32 v29, v29, v118
EX 126
s_waitcnt lgkmcnt(0)
EX 127
s_nop 0
CV 157,126,127
v_add_f32 v29, v29, v119
Cvav 112,128,48,32
s_waitcnt vmcnt(12)
EX 128
Mvav 112,132,52
ds_write_b32 v2, v4 offset:0
Mvav 112,136,56
Ba 144,12,12288
v_add_f32 v28, v28, v128
Mvav 112,140,60
s_add_u32 s44, s44, 1
EX 129
Cvav 116,128,64,36
Ba 148,13,12288
CV 146,128,129
Mvav 116,132,68
EX 130
Mvav 116,136,72
Ba 152,14,12288
v_add_f32 v28, v28, v129
Mvav 116,140,76
EX 131
Cvav 120,128,80,40
Ba 156,15,12288
CV 147,130,131
Mvav 120,132,84
EX 132
Mvav 120,136,88
v_add_f32 v28, v28, v130
Mvav 120,140,92
EX 133
Cvav 124,128,96,44
CV 150,132,133
Mvav 124,132,100
EX 134
Mvav 124,136,104
v_add_f32 v28, v28, v131
Mvav 124,140,108
EX 135
s_nop 0
CV 151,134,135
EX 136
v_add_f32 v29, v29, v132
EX 137
s_nop 0
CV 154,136,137
ds_read_b32 v5, v3 offset:0
v_add_u32 v4, 1, v4
EX 138
v_add_f32 v29, v29, v133
EX 139
s_nop 0
CV 155,138,139
s_add_u32 m0, s34, 49152
EX 140
EX 141
s_waitcnt lgkmcnt(0)
v_cmp_gt_u32 vcc, s44, v5
CV 158,140,141
v_add_f32 v29, v29, v134
EX 142
EX 143
s_nop 0
CV 159,142,143
v_add_f32 v29, v29, v135
s_cbranch_vccnz Lspin102_%=
Lback102_%=:
global_load_lds_dwordx4 v6, s[36:37] offset:0
global_load_lds_dwordx4 v7, s[36:37] offset:1024
Cvav 128,144,48,32
EX 112
Mvav 128,148,52
Ba 128,12,16384
v_add_f32 v28, v28, v112
Mvav 128,152,56
EX 113
Mvav 128,156,60
Ba 132,13,16384
CV 160,112,113
Cvav 132,144,64,36
EX 114
Mvav 132,148,68
Ba 136,14,16384
v_add_f32 v28, v28, v113
Mvav 132,152,72
EX 115
Mvav 132,156,76
Ba 140,15,16384
CV 161,114,115
Cvav 136,144,80,40
EX 116
Mvav 136,148,84
v_add_f32 v28, v28, v114
Mvav 136,152,88
EX 117
Mvav 136,156,92
CV 164,116,117
Cvav 140,144,96,44
EX 118
Mvav 140,148,100
v_add_f32 v28, v28, v115
Mvav 140,152,104
EX 119
Mvav 140,156,108
)ASM"
R"ASM(
CV 165,118,119
EX 120
v_add_f32 v29, v29, v116
EX 121
s_nop 0
CV 168,120,121
EX 122
v_add_f32 v29, v29, v117
EX 123
s_nop 0
CV 169,122,123
EX 124
EX 125
s_nop 0
CV 172,124,125
v_add_f32 v29, v29, v118
global_load_lds_dwordx4 v8, s[36:37] offset:2048
EX 126
s_waitcnt lgkmcnt(0)
EX 127
s_nop 0
CV 173,126,127
v_add_f32 v29, v29, v119
global_load_lds_dwordx4 v9, s[36:37] offset:3072
Cvav 112,128,48,32
EX 128
Mvav 112,132,52
Ba 144,12,20480
v_add_f32 v28, v28, v128
Mvav 112,136,56
EX 129
Mvav 112,140,60
Ba 148,13,20480
CV 162,128,129
Cvav 116,128,64,36
s_add_u32 s36, s36, 16384
EX 130
Mvav 116,132,68
Ba 152,14,20480
v_add_f32 v28, v28, v129
Mvav 116,136,72
s_addc_u32 s37, s37, 0
EX 131
Mvav 116,140,76
Ba 156,15,20480
CV 163,130,131
Cvav 120,128,80,40
EX 132
Mvav 120,132,84
Ta 176,16,0
v_add_f32 v28, v28, v130
Mvav 120,136,88
EX 133
Mvav 120,140,92
Ta 178,16,4096
CV 166,132,133
Cvav 124,128,96,44
EX 134
Mvav 124,132,100
Ta 180,16,8192
v_add_f32 v28, v28, v131
Mvav 124,136,104
EX 135
Mvav 124,140,108
Ta 182,16,12288
CV 167,134,135
EX 136
Ta 184,17,0
v_add_f32 v29, v29, v132
EX 137
Ta 186,17,4096
CV 170,136,137
EX 138
Ta 188,17,8192
v_add_f32 v29, v29, v133
EX 139
Ta 190,17,12288
CV 171,138,139
s_add_u32 m0, s42, 32768
EX 140
EX 141
s_nop 0
CV 174,140,141
v_add_f32 v29, v29, v134
global_load_lds_dwordx4 v10, s[38:39] offset:0
EX 142
s_waitcnt lgkmcnt(8)
EX 143
s_nop 0
CV 175,142,143
v_add_f32 v29, v29, v135
global_load_lds_dwordx4 v11, s[38:39] offset:1024
Cvav 128,144,48,32
EX 112
v_subrev_u32 v236, 147456, v2
Mvav 128,148,52
Ba 128,12,24576
v_add_f32 v28, v28, v112
v_lshlrev_b32 v236, 4, v236
Mvav 128,152,56
EX 113
s_and_b32 s50, s40, 3
s_lshl_b32 s50, s50, 8
s_sub_u32 s52, s24, s50
s_subb_u32 s53, s25, 0
s_sub_u32 s54, s26, s50
s_subb_u32 s55, s27, 0
s_lshl_b32 s50, %1, 14
s_mul_i32 s51, %1, 5504
Mvav 128,156,60
Ba 132,13,24576
CV 176,112,113
v_add_u32 v237, s50, v236
Cvav 132,144,64,36
EX 114
global_load_dword a255, v237, s[52:53]
Mvav 132,148,68
Ba 136,14,24576
v_add_f32 v28, v28, v113
v_min_u32 v236, 5440, v236
Mvav 132,152,72
EX 115
v_add_u32 v237, s51, v236
Mvav 132,156,76
Ba 140,15,24576
CV 177,114,115
global_load_dword a255, v237, s[54:55]
Cvav 136,144,80,40
EX 116
Mvav 136,148,84
Ta 160,18,0
v_add_f32 v28, v28, v114
Mvav 136,152,88
EX 117
Mvav 136,156,92
Ta 162,18,4096
CV 180,116,117
Cvav 140,144,96,44
EX 118
Mvav 140,148,100
Ta 164,18,8192
v_add_f32 v28, v28, v115
Mvav 140,152,104
s_waitcnt lgkmcnt(7)
EX 119
Mvav 140,156,108
Ta 166,18,12288
CV 181,118,119
Zaav 0,176,144
EX 120
Maav 0,180,160
Ta 168,19,0
v_add_f32 v29, v29, v116
Zaav 4,176,148
EX 121
Maav 4,180,164
Ta 170,19,4096
CV 184,120,121
Zaav 8,176,152
EX 122
Maav 8,180,168
Ta 172,19,8192
v_add_f32 v29, v29, v117
Zaav 12,176,156
EX 123
Maav 12,180,172
Ta 174,19,12288
CV 185,122,123
Zaav 16,184,144
EX 124
Maav 16,188,160
EX 125
Zaav 20,184,148
CV 188,124,125
v_add_f32 v29, v29, v118
Maav 20,188,164
global_load_lds_dwordx4 v10, s[38:39] offset:2048
Zaav 24,184,152
EX 126
Maav 24,188,168
s_waitcnt lgkmcnt(8)
EX 127
Zaav 28,184,156
CV 189,126,127
v_add_f32 v29, v29, v119
Maav 28,188,172
global_load_lds_dwordx4 v11, s[38:39] offset:3072
Cvav 112,128,48,32
s_waitcnt vmcnt(10)
EX 128
Mvav 112,132,52
ds_write_b32 v2, v4 offset:0
Mvav 112,136,56
Ba 144,12,28672
v_add_f32 v28, v28, v128
Mvav 112,140,60
s_add_u32 s44, s44, 1
EX 129
Cvav 116,128,64,36
Ba 148,13,28672
)ASM"
R"ASM(
CV 178,128,129
Mvav 116,132,68
s_add_u32 s38, s38, 16384
EX 130
Mvav 116,136,72
Ba 152,14,28672
v_add_f32 v28, v28, v129
Mvav 116,140,76
s_addc_u32 s39, s39, 0
EX 131
Cvav 120,128,80,40
Ba 156,15,28672
CV 179,130,131
Mvav 120,132,84
EX 132
Mvav 120,136,88
Ta 176,20,0
v_add_f32 v28, v28, v130
Mvav 120,140,92
EX 133
Cvav 124,128,96,44
Ta 178,20,4096
CV 182,132,133
Mvav 124,132,100
EX 134
Mvav 124,136,104
Ta 180,20,8192
v_add_f32 v28, v28, v131
Mvav 124,140,108
s_waitcnt lgkmcnt(8)
EX 135
Zaav 32,160,144
Ta 182,20,12288
CV 183,134,135
Maav 32,164,160
EX 136
Zaav 36,160,148
Ta 184,21,0
v_add_f32 v29, v29, v132
Maav 36,164,164
EX 137
Zaav 40,160,152
Ta 186,21,4096
CV 186,136,137
Maav 40,164,168
ds_read_b32 v5, v3 offset:0
v_add_u32 v4, 1, v4
Zaav 44,160,156
EX 138
Maav 44,164,172
Ta 188,21,8192
v_add_f32 v29, v29, v133
Zaav 48,168,144
EX 139
Maav 48,172,160
Ta 190,21,12288
CV 187,138,139
Zaav 52,168,148
s_add_u32 m0, s34, 0
EX 140
Maav 52,172,164
EX 141
Zaav 56,168,152
s_waitcnt lgkmcnt(2)
v_cmp_gt_u32 vcc, s44, v5
Maav 56,172,168
CV 190,140,141
v_add_f32 v29, v29, v134
Zaav 60,168,156
EX 142
Maav 60,172,172
EX 143
Zaav 200,208,152
CV 191,142,143
v_add_f32 v29, v29, v135
Maav 200,208,168
s_cbranch_vccnz Lspin106_%=
Lback106_%=:
Zaav 204,208,156
global_load_lds_dwordx4 v6, s[36:37] offset:0
Maav 204,208,172
global_load_lds_dwordx4 v7, s[36:37] offset:1024
Cvav 128,144,48,32
EX 112
Mvav 128,148,52
Ba 128,12,32768
v_add_f32 v28, v28, v112
Mvav 128,152,56
EX 113
Mvav 128,156,60
Ba 132,13,32768
CV 192,112,113
Cvav 132,144,64,36
EX 114
Mvav 132,148,68
Ba 136,14,32768
v_add_f32 v28, v28, v113
Mvav 132,152,72
EX 115
Mvav 132,156,76
Ba 140,15,32768
CV 193,114,115
Cvav 136,144,80,40
EX 116
Mvav 136,148,84
Ta 160,22,0
v_add_f32 v28, v28, v114
Mvav 136,152,88
EX 117
Mvav 136,156,92
Ta 162,22,4096
CV 196,116,117
Cvav 140,144,96,44
EX 118
Mvav 140,148,100
Ta 164,22,8192
v_add_f32 v28, v28, v115
Mvav 140,152,104
s_waitcnt lgkmcnt(7)
EX 119
Mvav 140,156,108
Ta 166,22,12288
CV 197,118,119
Zaav 64,176,144
EX 120
Maav 64,180,160
Ta 168,23,0
v_add_f32 v29, v29, v116
Zaav 68,176,148
EX 121
Maav 68,180,164
Ta 170,23,4096
CV 200,120,121
Zaav 72,176,152
EX 122
Maav 72,180,168
Ta 172,23,8192
v_add_f32 v29, v29, v117
Zaav 76,176,156
EX 123
Maav 76,180,172
Ta 174,23,12288
CV 201,122,123
Zaav 80,184,144
EX 124
Maav 80,188,160
EX 125
Zaav 84,184,148
CV 204,124,125
v_add_f32 v29, v29, v118
Maav 84,188,164
global_load_lds_dwordx4 v8, s[36:37] offset:2048
Zaav 88,184,152
EX 126
Maav 88,188,168
s_waitcnt lgkmcnt(8)
EX 127
Zaav 92,184,156
CV 205,126,127
v_add_f32 v29, v29, v119
Maav 92,188,172
global_load_lds_dwordx4 v9, s[36:37] offset:3072
Cvav 112,128,48,32
EX 128
Mvav 112,132,52
Ba 144,12,36864
v_add_f32 v28, v28, v128
Mvav 112,136,56
EX 129
Mvav 112,140,60
Ba 148,13,36864
CV 194,128,129
Cvav 116,128,64,36
s_add_u32 s36, s36, 16384
EX 130
Mvav 116,132,68
Ba 152,14,36864
v_add_f32 v28, v28, v129
Mvav 116,136,72
s_addc_u32 s37, s37, 0
EX 131
Mvav 116,140,76
Ba 156,15,36864
CV 195,130,131
Cvav 120,128,80,40
EX 132
Mvav 120,132,84
Ta 176,16,16384
v_add_f32 v28, v28, v130
Mvav 120,136,88
EX 133
Mvav 120,140,92
Ta 178,16,20480
CV 198,132,133
Cvav 124,128,96,44
EX 134
Mvav 124,132,100
Ta 180,16,24576
v_add_f32 v28, v28, v131
Mvav 124,136,104
)ASM"
R"ASM(
s_waitcnt lgkmcnt(7)
EX 135
Mvav 124,140,108
Ta 182,16,28672
CV 199,134,135
Zaav 96,160,144
EX 136
Maav 96,164,160
Ta 184,17,16384
v_add_f32 v29, v29, v132
Zaav 100,160,148
EX 137
Maav 100,164,164
Ta 186,17,20480
CV 202,136,137
Zaav 104,160,152
EX 138
Maav 104,164,168
Ta 188,17,24576
v_add_f32 v29, v29, v133
Zaav 108,160,156
EX 139
Maav 108,164,172
Ta 190,17,28672
CV 203,138,139
Zaav 112,168,144
s_add_u32 m0, s42, 49152
EX 140
Maav 112,172,160
EX 141
Zaav 116,168,148
CV 206,140,141
v_add_f32 v29, v29, v134
Maav 116,172,164
global_load_lds_dwordx4 v10, s[38:39] offset:0
Zaav 120,168,152
EX 142
Maav 120,172,168
s_waitcnt lgkmcnt(8)
EX 143
Zaav 124,168,156
CV 207,142,143
v_add_f32 v29, v29, v135
Maav 124,172,172
global_load_lds_dwordx4 v11, s[38:39] offset:1024
Cvav 128,144,48,32
EX 112
Mvav 128,148,52
Ba 128,12,40960
v_add_f32 v28, v28, v112
Mvav 128,152,56
EX 113
Mvav 128,156,60
Ba 132,13,40960
CV 144,112,113
Cvav 132,144,64,36
EX 114
Mvav 132,148,68
Ba 136,14,40960
v_add_f32 v28, v28, v113
Mvav 132,152,72
EX 115
Mvav 132,156,76
Ba 140,15,40960
CV 145,114,115
Cvav 136,144,80,40
EX 116
Mvav 136,148,84
Ta 160,18,16384
v_add_f32 v28, v28, v114
Mvav 136,152,88
EX 117
Mvav 136,156,92
Ta 162,18,20480
CV 148,116,117
Cvav 140,144,96,44
EX 118
Mvav 140,148,100
Ta 164,18,24576
v_add_f32 v28, v28, v115
Mvav 140,152,104
s_waitcnt lgkmcnt(7)
EX 119
Mvav 140,156,108
Ta 166,18,28672
CV 149,118,119
Maav 0,176,176
EX 120
Maav 0,180,192
Ta 168,19,16384
v_add_f32 v29, v29, v116
Maav 4,176,180
EX 121
Maav 4,180,196
Ta 170,19,20480
CV 152,120,121
Maav 8,176,184
EX 122
Maav 8,180,200
Ta 172,19,24576
v_add_f32 v29, v29, v117
Maav 12,176,188
EX 123
Maav 12,180,204
Ta 174,19,28672
CV 153,122,123
Maav 16,184,176
EX 124
Maav 16,188,192
EX 125
Maav 20,184,180
CV 156,124,125
v_add_f32 v29, v29, v118
Maav 20,188,196
global_load_lds_dwordx4 v10, s[38:39] offset:2048
Maav 24,184,184
EX 126
Maav 24,188,200
s_waitcnt lgkmcnt(8)
EX 127
Maav 28,184,188
CV 157,126,127
v_add_f32 v29, v29, v119
Maav 28,188,204
global_load_lds_dwordx4 v11, s[38:39] offset:3072
s_mov_b32 s45, 7
.p2align 6
LloopB_%=:
Cvav 112,128,48,32
s_waitcnt vmcnt(8)
EX 128
Mvav 112,132,52
ds_write_b32 v2, v4 offset:0
Mvav 112,136,56
Ba 144,12,45056
v_add_f32 v28, v28, v128
Mvav 112,140,60
s_add_u32 s44, s44, 1
EX 129
Cvav 116,128,64,36
Ba 148,13,45056
CV 146,128,129
Mvav 116,132,68
s_add_u32 s38, s38, 16384
EX 130
Mvav 116,136,72
Ba 152,14,45056
v_add_f32 v28, v28, v129
Mvav 116,140,76
s_addc_u32 s39, s39, 0
EX 131
Cvav 120,128,80,40
Ba 156,15,45056
CV 147,130,131
Mvav 120,132,84
s_sub_u32 s45, s45, 1
EX 132
Mvav 120,136,88
Ta 176,20,16384
v_add_f32 v28, v28, v130
Mvav 120,140,92
s_cmp_lg_u32 s45, 0
EX 133
Cvav 124,128,96,44
Ta 178,20,20480
CV 150,132,133
Mvav 124,132,100
s_cselect_b32 s43, 16384, 0
EX 134
Mvav 124,136,104
Ta 180,20,24576
v_add_f32 v28, v28, v131
Mvav 124,140,108
s_waitcnt lgkmcnt(8)
EX 135
Maav 32,160,176
Ta 182,20,28672
CV 151,134,135
Maav 32,164,192
s_cmp_eq_u32 s45, 6
EX 136
Maav 36,160,180
Ta 184,21,16384
v_add_f32 v29, v29, v132
Maav 36,164,196
s_cselect_b32 s90, -1, s44
EX 137
Maav 40,160,184
Ta 186,21,20480
CV 154,136,137
Maav 40,164,200
ds_read_b32 v5, v3 offset:0
v_add_u32 v4, 1, v4
Maav 44,160,188
EX 138
Maav 44,164,204
Ta 188,21,24576
v_add_f32 v29, v29, v133
Maav 48,168,176
EX 139
)ASM"
R"ASM(
Maav 48,172,192
Ta 190,21,28672
CV 155,138,139
Maav 52,168,180
s_add_u32 m0, s34, 16384
EX 140
Maav 52,172,196
EX 141
Maav 56,168,184
s_waitcnt lgkmcnt(2)
v_cmp_gt_u32 vcc, s90, v5
Maav 56,172,200
CV 158,140,141
v_add_f32 v29, v29, v134
Maav 60,168,188
EX 142
Maav 60,172,204
EX 143
Maav 200,208,184
CV 159,142,143
v_add_f32 v29, v29, v135
Maav 200,208,200
s_cbranch_vccnz Lspin10_%=
Lback10_%=:
Maav 204,208,188
global_load_lds_dwordx4 v6, s[36:37] offset:0
Maav 204,208,204
global_load_lds_dwordx4 v7, s[36:37] offset:1024
Cvav 128,144,48,32
EX 112
Mvav 128,148,52
Ba 128,12,49152
v_add_f32 v28, v28, v112
Mvav 128,152,56
EX 113
Mvav 128,156,60
Ba 132,13,49152
CV 160,112,113
Cvav 132,144,64,36
EX 114
Mvav 132,148,68
Ba 136,14,49152
v_add_f32 v28, v28, v113
Mvav 132,152,72
EX 115
Mvav 132,156,76
Ba 140,15,49152
CV 161,114,115
Cvav 136,144,80,40
EX 116
Mvav 136,148,84
Ta 160,22,16384
v_add_f32 v28, v28, v114
Mvav 136,152,88
EX 117
Mvav 136,156,92
Ta 162,22,20480
CV 164,116,117
Cvav 140,144,96,44
EX 118
Mvav 140,148,100
Ta 164,22,24576
v_add_f32 v28, v28, v115
Mvav 140,152,104
s_waitcnt lgkmcnt(7)
EX 119
Mvav 140,156,108
Ta 166,22,28672
CV 165,118,119
Maav 64,176,176
EX 120
Maav 64,180,192
Ta 168,23,16384
v_add_f32 v29, v29, v116
Maav 68,176,180
EX 121
Maav 68,180,196
Ta 170,23,20480
CV 168,120,121
Maav 72,176,184
EX 122
Maav 72,180,200
Ta 172,23,24576
v_add_f32 v29, v29, v117
Maav 76,176,188
EX 123
Maav 76,180,204
Ta 174,23,28672
CV 169,122,123
Maav 80,184,176
EX 124
Maav 80,188,192
EX 125
Maav 84,184,180
CV 172,124,125
v_add_f32 v29, v29, v118
Maav 84,188,196
global_load_lds_dwordx4 v8, s[36:37] offset:2048
Maav 88,184,184
EX 126
Maav 88,188,200
s_waitcnt lgkmcnt(8)
EX 127
Maav 92,184,188
CV 173,126,127
v_add_f32 v29, v29, v119
Maav 92,188,204
global_load_lds_dwordx4 v9, s[36:37] offset:3072
Cvav 112,128,48,32
EX 128
Mvav 112,132,52
Ba 144,12,53248
v_add_f32 v28, v28, v128
Mvav 112,136,56
EX 129
Mvav 112,140,60
Ba 148,13,53248
CV 162,128,129
Cvav 116,128,64,36
s_add_u32 s36, s36, 16384
EX 130
Mvav 116,132,68
Ba 152,14,53248
v_add_f32 v28, v28, v129
Mvav 116,136,72
s_addc_u32 s37, s37, 0
EX 131
Mvav 116,140,76
Ba 156,15,53248
CV 163,130,131
Cvav 120,128,80,40
EX 132
Mvav 120,132,84
Ta 176,16,32768
v_add_f32 v28, v28, v130
Mvav 120,136,88
EX 133
Mvav 120,140,92
Ta 178,16,36864
CV 166,132,133
Cvav 124,128,96,44
EX 134
Mvav 124,132,100
Ta 180,16,40960
v_add_f32 v28, v28, v131
Mvav 124,136,104
s_waitcnt lgkmcnt(7)
EX 135
Mvav 124,140,108
Ta 182,16,45056
CV 167,134,135
Maav 96,160,176
EX 136
Maav 96,164,192
Ta 184,17,32768
v_add_f32 v29, v29, v132
Maav 100,160,180
EX 137
Maav 100,164,196
Ta 186,17,36864
CV 170,136,137
Maav 104,160,184
EX 138
Maav 104,164,200
Ta 188,17,40960
v_add_f32 v29, v29, v133
Maav 108,160,188
EX 139
Maav 108,164,204
Ta 190,17,45056
CV 171,138,139
Maav 112,168,176
s_add_u32 m0, s42, 0
EX 140
Maav 112,172,192
EX 141
Maav 116,168,180
CV 174,140,141
v_add_f32 v29, v29, v134
Maav 116,172,196
global_load_lds_dwordx4 v10, s[38:39] offset:0
Maav 120,168,184
EX 142
Maav 120,172,200
s_waitcnt lgkmcnt(8)
EX 143
Maav 124,168,188
CV 175,142,143
v_add_f32 v29, v29, v135
Maav 124,172,204
global_load_lds_dwordx4 v11, s[38:39] offset:1024
Cvav 128,144,48,32
EX 112
Mvav 128,148,52
Ba 128,12,57344
v_add_f32 v28, v28, v112
Mvav 128,152,56
EX 113
Mvav 128,156,60
Ba 132,13,57344
)ASM"
R"ASM(
CV 176,112,113
Cvav 132,144,64,36
EX 114
Mvav 132,148,68
Ba 136,14,57344
v_add_f32 v28, v28, v113
Mvav 132,152,72
EX 115
Mvav 132,156,76
Ba 140,15,57344
CV 177,114,115
Cvav 136,144,80,40
EX 116
Mvav 136,148,84
Ta 160,18,32768
v_add_f32 v28, v28, v114
Mvav 136,152,88
EX 117
Mvav 136,156,92
Ta 162,18,36864
CV 180,116,117
Cvav 140,144,96,44
EX 118
Mvav 140,148,100
Ta 164,18,40960
v_add_f32 v28, v28, v115
Mvav 140,152,104
s_waitcnt lgkmcnt(7)
EX 119
Mvav 140,156,108
Ta 166,18,45056
CV 181,118,119
Maav 0,176,144
EX 120
Maav 0,180,160
Ta 168,19,32768
v_add_f32 v29, v29, v116
Maav 4,176,148
EX 121
Maav 4,180,164
Ta 170,19,36864
CV 184,120,121
Maav 8,176,152
EX 122
Maav 8,180,168
Ta 172,19,40960
v_add_f32 v29, v29, v117
Maav 12,176,156
EX 123
Maav 12,180,172
Ta 174,19,45056
CV 185,122,123
Maav 16,184,144
EX 124
Maav 16,188,160
EX 125
Maav 20,184,148
CV 188,124,125
v_add_f32 v29, v29, v118
Maav 20,188,164
global_load_lds_dwordx4 v10, s[38:39] offset:2048
Maav 24,184,152
EX 126
Maav 24,188,168
s_waitcnt lgkmcnt(8)
EX 127
Maav 28,184,156
CV 189,126,127
v_add_f32 v29, v29, v119
Maav 28,188,172
global_load_lds_dwordx4 v11, s[38:39] offset:3072
Cvav 112,128,48,32
s_waitcnt vmcnt(8)
EX 128
Mvav 112,132,52
ds_write_b32 v2, v4 offset:0
Mvav 112,136,56
Ba 144,12,61440
v_add_f32 v28, v28, v128
Mvav 112,140,60
s_add_u32 s44, s44, 1
EX 129
Cvav 116,128,64,36
Ba 148,13,61440
CV 178,128,129
Mvav 116,132,68
s_add_u32 s38, s38, 16384
EX 130
Mvav 116,136,72
Ba 152,14,61440
v_add_f32 v28, v28, v129
Mvav 116,140,76
s_addc_u32 s39, s39, 0
EX 131
Cvav 120,128,80,40
Ba 156,15,61440
CV 179,130,131
Mvav 120,132,84
EX 132
Mvav 120,136,88
Ta 176,20,32768
v_add_f32 v28, v28, v130
Mvav 120,140,92
EX 133
Cvav 124,128,96,44
Ta 178,20,36864
CV 182,132,133
Mvav 124,132,100
EX 134
Mvav 124,136,104
Ta 180,20,40960
v_add_f32 v28, v28, v131
Mvav 124,140,108
s_waitcnt lgkmcnt(8)
EX 135
Maav 32,160,144
Ta 182,20,45056
CV 183,134,135
Maav 32,164,160
s_cmp_eq_u32 s45, 0
EX 136
Maav 36,160,148
Ta 184,21,32768
v_add_f32 v29, v29, v132
Maav 36,164,164
s_cselect_b32 s90, -1, s44
EX 137
Maav 40,160,152
Ta 186,21,36864
CV 186,136,137
Maav 40,164,168
ds_read_b32 v5, v3 offset:0
v_add_u32 v4, 1, v4
Maav 44,160,156
EX 138
Maav 44,164,172
Ta 188,21,40960
v_add_f32 v29, v29, v133
Maav 48,168,144
EX 139
Maav 48,172,160
Ta 190,21,45056
CV 187,138,139
Maav 52,168,148
s_add_u32 m0, s34, 32768
EX 140
Maav 52,172,164
EX 141
Maav 56,168,152
s_waitcnt lgkmcnt(2)
v_cmp_gt_u32 vcc, s90, v5
Maav 56,172,168
CV 190,140,141
v_add_f32 v29, v29, v134
Maav 60,168,156
EX 142
Maav 60,172,172
EX 143
Maav 200,208,152
CV 191,142,143
v_add_f32 v29, v29, v135
Maav 200,208,168
s_cbranch_vccnz Lspin14_%=
Lback14_%=:
Maav 204,208,156
global_load_lds_dwordx4 v6, s[36:37] offset:0
Maav 204,208,172
global_load_lds_dwordx4 v7, s[36:37] offset:1024
Cvav 128,144,48,32
EX 112
Mvav 128,148,52
Ba 128,12,0
v_add_f32 v28, v28, v112
Mvav 128,152,56
EX 113
Mvav 128,156,60
Ba 132,13,0
CV 192,112,113
Cvav 132,144,64,36
EX 114
Mvav 132,148,68
Ba 136,14,0
v_add_f32 v28, v28, v113
Mvav 132,152,72
EX 115
Mvav 132,156,76
Ba 140,15,0
CV 193,114,115
Cvav 136,144,80,40
EX 116
Mvav 136,148,84
Ta 160,22,32768
v_add_f32 v28, v28, v114
Mvav 136,152,88
EX 117
Mvav 136,156,92
Ta 162,22,36864
CV 196,116,117
Cvav 140,144,96,44
EX 118
)ASM"
R"ASM(
Mvav 140,148,100
Ta 164,22,40960
v_add_f32 v28, v28, v115
Mvav 140,152,104
s_waitcnt lgkmcnt(7)
EX 119
Mvav 140,156,108
Ta 166,22,45056
CV 197,118,119
Maav 64,176,144
EX 120
Maav 64,180,160
Ta 168,23,32768
v_add_f32 v29, v29, v116
Maav 68,176,148
EX 121
Maav 68,180,164
Ta 170,23,36864
CV 200,120,121
Maav 72,176,152
EX 122
Maav 72,180,168
Ta 172,23,40960
v_add_f32 v29, v29, v117
Maav 76,176,156
EX 123
Maav 76,180,172
Ta 174,23,45056
CV 201,122,123
Maav 80,184,144
EX 124
Maav 80,188,160
EX 125
Maav 84,184,148
CV 204,124,125
v_add_f32 v29, v29, v118
Maav 84,188,164
global_load_lds_dwordx4 v8, s[36:37] offset:2048
Maav 88,184,152
EX 126
Maav 88,188,168
s_waitcnt lgkmcnt(8)
EX 127
Maav 92,184,156
CV 205,126,127
v_add_f32 v29, v29, v119
Maav 92,188,172
global_load_lds_dwordx4 v9, s[36:37] offset:3072
Cvav 112,128,48,32
EX 128
Mvav 112,132,52
Ba 144,12,4096
v_add_f32 v28, v28, v128
Mvav 112,136,56
EX 129
Mvav 112,140,60
Ba 148,13,4096
CV 194,128,129
Cvav 116,128,64,36
s_add_u32 s36, s36, 16384
EX 130
Mvav 116,132,68
Ba 152,14,4096
v_add_f32 v28, v28, v129
Mvav 116,136,72
s_addc_u32 s37, s37, 0
EX 131
Mvav 116,140,76
Ba 156,15,4096
CV 195,130,131
Cvav 120,128,80,40
EX 132
Mvav 120,132,84
Ta 176,16,49152
v_add_f32 v28, v28, v130
Mvav 120,136,88
EX 133
Mvav 120,140,92
Ta 178,16,53248
CV 198,132,133
Cvav 124,128,96,44
EX 134
Mvav 124,132,100
Ta 180,16,57344
v_add_f32 v28, v28, v131
Mvav 124,136,104
s_waitcnt lgkmcnt(7)
EX 135
Mvav 124,140,108
Ta 182,16,61440
CV 199,134,135
Maav 96,160,144
EX 136
Maav 96,164,160
Ta 184,17,49152
v_add_f32 v29, v29, v132
Maav 100,160,148
EX 137
Maav 100,164,164
Ta 186,17,53248
CV 202,136,137
Maav 104,160,152
EX 138
Maav 104,164,168
Ta 188,17,57344
v_add_f32 v29, v29, v133
Maav 108,160,156
EX 139
Maav 108,164,172
Ta 190,17,61440
CV 203,138,139
Maav 112,168,144
s_add_u32 m0, s42, 16384
EX 140
Maav 112,172,160
EX 141
Maav 116,168,148
CV 206,140,141
v_add_f32 v29, v29, v134
Maav 116,172,164
global_load_lds_dwordx4 v10, s[38:39] offset:0
Maav 120,168,152
EX 142
Maav 120,172,168
s_waitcnt lgkmcnt(8)
EX 143
Maav 124,168,156
CV 207,142,143
v_add_f32 v29, v29, v135
Maav 124,172,172
global_load_lds_dwordx4 v11, s[38:39] offset:1024
Cvav 128,144,48,32
EX 112
Mvav 128,148,52
Ba 128,12,8192
v_add_f32 v28, v28, v112
Mvav 128,152,56
EX 113
Mvav 128,156,60
Ba 132,13,8192
CV 144,112,113
Cvav 132,144,64,36
EX 114
Mvav 132,148,68
Ba 136,14,8192
v_add_f32 v28, v28, v113
Mvav 132,152,72
EX 115
Mvav 132,156,76
Ba 140,15,8192
CV 145,114,115
Cvav 136,144,80,40
EX 116
Mvav 136,148,84
Ta 160,18,49152
v_add_f32 v28, v28, v114
Mvav 136,152,88
EX 117
Mvav 136,156,92
Ta 162,18,53248
CV 148,116,117
Cvav 140,144,96,44
EX 118
Mvav 140,148,100
Ta 164,18,57344
v_add_f32 v28, v28, v115
Mvav 140,152,104
s_waitcnt lgkmcnt(7)
EX 119
Mvav 140,156,108
Ta 166,18,61440
CV 149,118,119
Maav 0,176,176
EX 120
Maav 0,180,192
Ta 168,19,49152
v_add_f32 v29, v29, v116
Maav 4,176,180
EX 121
Maav 4,180,196
Ta 170,19,53248
CV 152,120,121
Maav 8,176,184
EX 122
Maav 8,180,200
Ta 172,19,57344
v_add_f32 v29, v29, v117
Maav 12,176,188
EX 123
Maav 12,180,204
Ta 174,19,61440
CV 153,122,123
Maav 16,184,176
EX 124
Maav 16,188,192
EX 125
Maav 20,184,180
CV 156,124,125
v_add_f32 v29, v29, v118
Maav 20,188,196
)ASM"
R"ASM(
global_load_lds_dwordx4 v10, s[38:39] offset:2048
Maav 24,184,184
EX 126
Maav 24,188,200
s_waitcnt lgkmcnt(8)
EX 127
Maav 28,184,188
CV 157,126,127
v_add_f32 v29, v29, v119
Maav 28,188,204
global_load_lds_dwordx4 v11, s[38:39] offset:3072
Cvav 112,128,48,32
s_waitcnt vmcnt(8)
EX 128
Mvav 112,132,52
ds_write_b32 v2, v4 offset:0
Mvav 112,136,56
Ba 144,12,12288
v_add_f32 v28, v28, v128
Mvav 112,140,60
s_add_u32 s44, s44, 1
EX 129
Cvav 116,128,64,36
Ba 148,13,12288
CV 146,128,129
Mvav 116,132,68
s_add_u32 s38, s38, 16384
EX 130
Mvav 116,136,72
Ba 152,14,12288
v_add_f32 v28, v28, v129
Mvav 116,140,76
s_addc_u32 s39, s39, 0
EX 131
Cvav 120,128,80,40
Ba 156,15,12288
CV 147,130,131
Mvav 120,132,84
EX 132
Mvav 120,136,88
Ta 176,20,49152
v_add_f32 v28, v28, v130
Mvav 120,140,92
EX 133
Cvav 124,128,96,44
Ta 178,20,53248
CV 150,132,133
Mvav 124,132,100
EX 134
Mvav 124,136,104
Ta 180,20,57344
v_add_f32 v28, v28, v131
Mvav 124,140,108
s_waitcnt lgkmcnt(8)
EX 135
Maav 32,160,176
Ta 182,20,61440
CV 151,134,135
Maav 32,164,192
s_cmp_eq_u32 s45, 0
EX 136
Maav 36,160,180
Ta 184,21,49152
v_add_f32 v29, v29, v132
Maav 36,164,196
s_cselect_b32 s90, -1, s44
EX 137
Maav 40,160,184
Ta 186,21,53248
CV 154,136,137
Maav 40,164,200
ds_read_b32 v5, v3 offset:0
v_add_u32 v4, 1, v4
Maav 44,160,188
EX 138
Maav 44,164,204
Ta 188,21,57344
v_add_f32 v29, v29, v133
Maav 48,168,176
EX 139
Maav 48,172,192
Ta 190,21,61440
CV 155,138,139
Maav 52,168,180
s_add_u32 m0, s34, 49152
EX 140
Maav 52,172,196
EX 141
Maav 56,168,184
s_waitcnt lgkmcnt(2)
v_cmp_gt_u32 vcc, s90, v5
Maav 56,172,200
CV 158,140,141
v_add_f32 v29, v29, v134
Maav 60,168,188
EX 142
Maav 60,172,204
EX 143
Maav 200,208,184
CV 159,142,143
v_add_f32 v29, v29, v135
Maav 200,208,200
s_cbranch_vccnz Lspin2_%=
Lback2_%=:
Maav 204,208,188
global_load_lds_dwordx4 v6, s[36:37] offset:0
Maav 204,208,204
global_load_lds_dwordx4 v7, s[36:37] offset:1024
Cvav 128,144,48,32
EX 112
Mvav 128,148,52
Ba 128,12,16384
v_add_f32 v28, v28, v112
Mvav 128,152,56
EX 113
Mvav 128,156,60
Ba 132,13,16384
CV 160,112,113
Cvav 132,144,64,36
EX 114
Mvav 132,148,68
Ba 136,14,16384
v_add_f32 v28, v28, v113
Mvav 132,152,72
EX 115
Mvav 132,156,76
Ba 140,15,16384
CV 161,114,115
Cvav 136,144,80,40
EX 116
Mvav 136,148,84
Ta 160,22,49152
v_add_f32 v28, v28, v114
Mvav 136,152,88
EX 117
Mvav 136,156,92
Ta 162,22,53248
CV 164,116,117
Cvav 140,144,96,44
EX 118
Mvav 140,148,100
Ta 164,22,57344
v_add_f32 v28, v28, v115
Mvav 140,152,104
s_waitcnt lgkmcnt(7)
EX 119
Mvav 140,156,108
Ta 166,22,61440
CV 165,118,119
Maav 64,176,176
EX 120
Maav 64,180,192
Ta 168,23,49152
v_add_f32 v29, v29, v116
Maav 68,176,180
EX 121
Maav 68,180,196
Ta 170,23,53248
CV 168,120,121
Maav 72,176,184
EX 122
Maav 72,180,200
Ta 172,23,57344
v_add_f32 v29, v29, v117
Maav 76,176,188
EX 123
Maav 76,180,204
Ta 174,23,61440
CV 169,122,123
Maav 80,184,176
EX 124
Maav 80,188,192
EX 125
Maav 84,184,180
CV 172,124,125
v_add_f32 v29, v29, v118
Maav 84,188,196
global_load_lds_dwordx4 v8, s[36:37] offset:2048
Maav 88,184,184
EX 126
Maav 88,188,200
s_waitcnt lgkmcnt(8)
EX 127
Maav 92,184,188
CV 173,126,127
v_add_f32 v29, v29, v119
Maav 92,188,204
global_load_lds_dwordx4 v9, s[36:37] offset:3072
Cvav 112,128,48,32
EX 128
Mvav 112,132,52
Ba 144,12,20480
v_add_f32 v28, v28, v128
Mvav 112,136,56
EX 129
Mvav 112,140,60
Ba 148,13,20480
CV 162,128,129
Cvav 116,128,64,36
s_add_u32 s36, s36, s43
)ASM"
R"ASM(
EX 130
Mvav 116,132,68
Ba 152,14,20480
v_add_f32 v28, v28, v129
Mvav 116,136,72
s_addc_u32 s37, s37, 0
EX 131
Mvav 116,140,76
Ba 156,15,20480
CV 163,130,131
Cvav 120,128,80,40
EX 132
Mvav 120,132,84
Ta 176,16,0
v_add_f32 v28, v28, v130
Mvav 120,136,88
EX 133
Mvav 120,140,92
Ta 178,16,4096
CV 166,132,133
Cvav 124,128,96,44
EX 134
Mvav 124,132,100
Ta 180,16,8192
v_add_f32 v28, v28, v131
Mvav 124,136,104
s_waitcnt lgkmcnt(7)
EX 135
Mvav 124,140,108
Ta 182,16,12288
CV 167,134,135
Maav 96,160,176
EX 136
Maav 96,164,192
Ta 184,17,0
v_add_f32 v29, v29, v132
Maav 100,160,180
EX 137
Maav 100,164,196
Ta 186,17,4096
CV 170,136,137
Maav 104,160,184
EX 138
Maav 104,164,200
Ta 188,17,8192
v_add_f32 v29, v29, v133
Maav 108,160,188
EX 139
Maav 108,164,204
Ta 190,17,12288
CV 171,138,139
Maav 112,168,176
s_add_u32 m0, s42, 32768
EX 140
Maav 112,172,192
EX 141
Maav 116,168,180
CV 174,140,141
v_add_f32 v29, v29, v134
Maav 116,172,196
global_load_lds_dwordx4 v10, s[38:39] offset:0
Maav 120,168,184
EX 142
Maav 120,172,200
s_waitcnt lgkmcnt(8)
EX 143
Maav 124,168,188
CV 175,142,143
v_add_f32 v29, v29, v135
Maav 124,172,204
global_load_lds_dwordx4 v11, s[38:39] offset:1024
Cvav 128,144,48,32
EX 112
Mvav 128,148,52
Ba 128,12,24576
v_add_f32 v28, v28, v112
Mvav 128,152,56
EX 113
Mvav 128,156,60
Ba 132,13,24576
CV 176,112,113
Cvav 132,144,64,36
EX 114
Mvav 132,148,68
Ba 136,14,24576
v_add_f32 v28, v28, v113
Mvav 132,152,72
EX 115
Mvav 132,156,76
Ba 140,15,24576
CV 177,114,115
Cvav 136,144,80,40
EX 116
Mvav 136,148,84
Ta 160,18,0
v_add_f32 v28, v28, v114
Mvav 136,152,88
EX 117
Mvav 136,156,92
Ta 162,18,4096
CV 180,116,117
Cvav 140,144,96,44
EX 118
Mvav 140,148,100
Ta 164,18,8192
v_add_f32 v28, v28, v115
Mvav 140,152,104
s_waitcnt lgkmcnt(7)
EX 119
Mvav 140,156,108
Ta 166,18,12288
CV 181,118,119
Maav 0,176,144
EX 120
Maav 0,180,160
Ta 168,19,0
v_add_f32 v29, v29, v116
Maav 4,176,148
EX 121
Maav 4,180,164
Ta 170,19,4096
CV 184,120,121
Maav 8,176,152
EX 122
Maav 8,180,168
Ta 172,19,8192
v_add_f32 v29, v29, v117
Maav 12,176,156
EX 123
Maav 12,180,172
Ta 174,19,12288
CV 185,122,123
Maav 16,184,144
EX 124
Maav 16,188,160
EX 125
Maav 20,184,148
CV 188,124,125
v_add_f32 v29, v29, v118
Maav 20,188,164
global_load_lds_dwordx4 v10, s[38:39] offset:2048
Maav 24,184,152
EX 126
Maav 24,188,168
s_waitcnt lgkmcnt(8)
EX 127
Maav 28,184,156
CV 189,126,127
v_add_f32 v29, v29, v119
Maav 28,188,172
global_load_lds_dwordx4 v11, s[38:39] offset:3072
Cvav 112,128,48,32
s_waitcnt vmcnt(8)
EX 128
Mvav 112,132,52
ds_write_b32 v2, v4 offset:0
Mvav 112,136,56
Ba 144,12,28672
v_add_f32 v28, v28, v128
Mvav 112,140,60
s_add_u32 s44, s44, 1
EX 129
Cvav 116,128,64,36
Ba 148,13,28672
CV 178,128,129
Mvav 116,132,68
s_add_u32 s38, s38, 16384
EX 130
Mvav 116,136,72
Ba 152,14,28672
v_add_f32 v28, v28, v129
Mvav 116,140,76
s_addc_u32 s39, s39, 0
EX 131
Cvav 120,128,80,40
Ba 156,15,28672
CV 179,130,131
Mvav 120,132,84
EX 132
Mvav 120,136,88
Ta 176,20,0
v_add_f32 v28, v28, v130
Mvav 120,140,92
EX 133
Cvav 124,128,96,44
Ta 178,20,4096
CV 182,132,133
Mvav 124,132,100
EX 134
Mvav 124,136,104
Ta 180,20,8192
v_add_f32 v28, v28, v131
Mvav 124,140,108
s_waitcnt lgkmcnt(8)
EX 135
Maav 32,160,144
Ta 182,20,12288
CV 183,134,135
Maav 32,164,160
s_cmp_eq_u32 s45, 1
)ASM"
R"ASM(
EX 136
Maav 36,160,148
Ta 184,21,0
v_add_f32 v29, v29, v132
Maav 36,164,164
s_cselect_b32 s90, -1, s44
EX 137
Maav 40,160,152
Ta 186,21,4096
CV 186,136,137
Maav 40,164,168
ds_read_b32 v5, v3 offset:0
v_add_u32 v4, 1, v4
Maav 44,160,156
EX 138
Maav 44,164,172
Ta 188,21,8192
v_add_f32 v29, v29, v133
Maav 48,168,144
EX 139
Maav 48,172,160
Ta 190,21,12288
CV 187,138,139
Maav 52,168,148
s_add_u32 m0, s34, 0
EX 140
Maav 52,172,164
EX 141
Maav 56,168,152
s_waitcnt lgkmcnt(2)
v_cmp_gt_u32 vcc, s90, v5
Maav 56,172,168
CV 190,140,141
v_add_f32 v29, v29, v134
Maav 60,168,156
EX 142
Maav 60,172,172
EX 143
Maav 200,208,152
CV 191,142,143
v_add_f32 v29, v29, v135
Maav 200,208,168
s_cbranch_vccnz Lspin6_%=
Lback6_%=:
Maav 204,208,156
global_load_lds_dwordx4 v6, s[36:37] offset:0
Maav 204,208,172
global_load_lds_dwordx4 v7, s[36:37] offset:1024
Cvav 128,144,48,32
EX 112
Mvav 128,148,52
Ba 128,12,32768
v_add_f32 v28, v28, v112
Mvav 128,152,56
EX 113
Mvav 128,156,60
Ba 132,13,32768
CV 192,112,113
Cvav 132,144,64,36
EX 114
Mvav 132,148,68
Ba 136,14,32768
v_add_f32 v28, v28, v113
Mvav 132,152,72
EX 115
Mvav 132,156,76
Ba 140,15,32768
CV 193,114,115
Cvav 136,144,80,40
EX 116
Mvav 136,148,84
Ta 160,22,0
v_add_f32 v28, v28, v114
Mvav 136,152,88
EX 117
Mvav 136,156,92
Ta 162,22,4096
CV 196,116,117
Cvav 140,144,96,44
EX 118
Mvav 140,148,100
Ta 164,22,8192
v_add_f32 v28, v28, v115
Mvav 140,152,104
s_waitcnt lgkmcnt(7)
EX 119
Mvav 140,156,108
Ta 166,22,12288
CV 197,118,119
Maav 64,176,144
EX 120
Maav 64,180,160
Ta 168,23,0
v_add_f32 v29, v29, v116
Maav 68,176,148
EX 121
Maav 68,180,164
Ta 170,23,4096
CV 200,120,121
Maav 72,176,152
EX 122
Maav 72,180,168
Ta 172,23,8192
v_add_f32 v29, v29, v117
Maav 76,176,156
EX 123
Maav 76,180,172
Ta 174,23,12288
CV 201,122,123
Maav 80,184,144
EX 124
Maav 80,188,160
EX 125
Maav 84,184,148
CV 204,124,125
v_add_f32 v29, v29, v118
Maav 84,188,164
global_load_lds_dwordx4 v8, s[36:37] offset:2048
Maav 88,184,152
EX 126
Maav 88,188,168
s_waitcnt lgkmcnt(8)
EX 127
Maav 92,184,156
CV 205,126,127
v_add_f32 v29, v29, v119
Maav 92,188,172
global_load_lds_dwordx4 v9, s[36:37] offset:3072
Cvav 112,128,48,32
EX 128
Mvav 112,132,52
Ba 144,12,36864
v_add_f32 v28, v28, v128
Mvav 112,136,56
EX 129
Mvav 112,140,60
Ba 148,13,36864
CV 194,128,129
Cvav 116,128,64,36
s_add_u32 s36, s36, 16384
EX 130
Mvav 116,132,68
Ba 152,14,36864
v_add_f32 v28, v28, v129
Mvav 116,136,72
s_addc_u32 s37, s37, 0
EX 131
Mvav 116,140,76
Ba 156,15,36864
CV 195,130,131
Cvav 120,128,80,40
EX 132
Mvav 120,132,84
Ta 176,16,16384
v_add_f32 v28, v28, v130
Mvav 120,136,88
EX 133
Mvav 120,140,92
Ta 178,16,20480
CV 198,132,133
Cvav 124,128,96,44
EX 134
Mvav 124,132,100
Ta 180,16,24576
v_add_f32 v28, v28, v131
Mvav 124,136,104
s_waitcnt lgkmcnt(7)
EX 135
Mvav 124,140,108
Ta 182,16,28672
CV 199,134,135
Maav 96,160,144
EX 136
Maav 96,164,160
Ta 184,17,16384
v_add_f32 v29, v29, v132
Maav 100,160,148
EX 137
Maav 100,164,164
Ta 186,17,20480
CV 202,136,137
Maav 104,160,152
EX 138
Maav 104,164,168
Ta 188,17,24576
v_add_f32 v29, v29, v133
Maav 108,160,156
EX 139
Maav 108,164,172
Ta 190,17,28672
CV 203,138,139
Maav 112,168,144
s_add_u32 m0, s42, 49152
EX 140
Maav 112,172,160
EX 141
Maav 116,168,148
CV 206,140,141
v_add_f32 v29, v29, v134
Maav 116,172,164
)ASM"
R"ASM(
global_load_lds_dwordx4 v10, s[38:39] offset:0
Maav 120,168,152
EX 142
Maav 120,172,168
s_waitcnt lgkmcnt(8)
EX 143
Maav 124,168,156
CV 207,142,143
v_add_f32 v29, v29, v135
Maav 124,172,172
global_load_lds_dwordx4 v11, s[38:39] offset:1024
Cvav 128,144,48,32
EX 112
Mvav 128,148,52
Ba 128,12,40960
v_add_f32 v28, v28, v112
Mvav 128,152,56
EX 113
Mvav 128,156,60
Ba 132,13,40960
CV 144,112,113
Cvav 132,144,64,36
EX 114
Mvav 132,148,68
Ba 136,14,40960
v_add_f32 v28, v28, v113
Mvav 132,152,72
EX 115
Mvav 132,156,76
Ba 140,15,40960
CV 145,114,115
Cvav 136,144,80,40
EX 116
Mvav 136,148,84
Ta 160,18,16384
v_add_f32 v28, v28, v114
Mvav 136,152,88
EX 117
Mvav 136,156,92
Ta 162,18,20480
CV 148,116,117
Cvav 140,144,96,44
EX 118
Mvav 140,148,100
Ta 164,18,24576
v_add_f32 v28, v28, v115
Mvav 140,152,104
s_waitcnt lgkmcnt(7)
EX 119
Mvav 140,156,108
Ta 166,18,28672
CV 149,118,119
Maav 0,176,176
EX 120
Maav 0,180,192
Ta 168,19,16384
v_add_f32 v29, v29, v116
Maav 4,176,180
EX 121
Maav 4,180,196
Ta 170,19,20480
CV 152,120,121
Maav 8,176,184
EX 122
Maav 8,180,200
Ta 172,19,24576
v_add_f32 v29, v29, v117
Maav 12,176,188
EX 123
Maav 12,180,204
Ta 174,19,28672
CV 153,122,123
Maav 16,184,176
EX 124
Maav 16,188,192
EX 125
Maav 20,184,180
CV 156,124,125
v_add_f32 v29, v29, v118
Maav 20,188,196
global_load_lds_dwordx4 v10, s[38:39] offset:2048
Maav 24,184,184
EX 126
Maav 24,188,200
s_waitcnt lgkmcnt(8)
EX 127
Maav 28,184,188
CV 157,126,127
v_add_f32 v29, v29, v119
Maav 28,188,204
global_load_lds_dwordx4 v11, s[38:39] offset:3072
s_cmp_lg_u32 s45, 0
s_cbranch_scc1 LloopB_%=
Cvav 112,128,48,32
s_waitcnt vmcnt(4)
EX 128
Mvav 112,132,52
ds_write_b32 v2, v4 offset:0
Mvav 112,136,56
Ba 144,12,45056
v_add_f32 v28, v28, v128
Mvav 112,140,60
s_add_u32 s44, s44, 1
EX 129
Cvav 116,128,64,36
Ba 148,13,45056
CV 146,128,129
Mvav 116,132,68
EX 130
Mvav 116,136,72
Ba 152,14,45056
v_add_f32 v28, v28, v129
Mvav 116,140,76
EX 131
Cvav 120,128,80,40
Ba 156,15,45056
CV 147,130,131
Mvav 120,132,84
EX 132
Mvav 120,136,88
Ta 176,20,16384
v_add_f32 v28, v28, v130
Mvav 120,140,92
EX 133
Cvav 124,128,96,44
Ta 178,20,20480
CV 150,132,133
Mvav 124,132,100
EX 134
Mvav 124,136,104
Ta 180,20,24576
v_add_f32 v28, v28, v131
Mvav 124,140,108
s_waitcnt lgkmcnt(8)
EX 135
Maav 32,160,176
Ta 182,20,28672
CV 151,134,135
Maav 32,164,192
EX 136
Maav 36,160,180
Ta 184,21,16384
v_add_f32 v29, v29, v132
Maav 36,164,196
EX 137
Maav 40,160,184
Ta 186,21,20480
CV 154,136,137
Maav 40,164,200
ds_read_b32 v5, v3 offset:0
v_add_u32 v4, 1, v4
Maav 44,160,188
EX 138
Maav 44,164,204
Ta 188,21,24576
v_add_f32 v29, v29, v133
Maav 48,168,176
EX 139
Maav 48,172,192
Ta 190,21,28672
CV 155,138,139
Maav 52,168,180
EX 140
Maav 52,172,196
EX 141
Maav 56,168,184
s_waitcnt lgkmcnt(2)
v_cmp_gt_u32 vcc, s44, v5
Maav 56,172,200
CV 158,140,141
v_add_f32 v29, v29, v134
Maav 60,168,188
EX 142
Maav 60,172,204
EX 143
Maav 200,208,184
CV 159,142,143
v_add_f32 v29, v29, v135
Maav 200,208,200
s_cbranch_vccnz Lspin222_%=
Lback222_%=:
Maav 204,208,188
Maav 204,208,204
Cvav 128,144,48,32
EX 112
s_getpc_b64 s[54:55]
Lpc2_%=:
s_lshr_b32 s50, %1, 3
s_lshl_b32 s50, s50, 11
Mvav 128,148,52
Ba 128,12,49152
v_add_f32 v28, v28, v112
v_and_b32 v236, 31, v1
Mvav 128,152,56
EX 113
v_lshlrev_b32 v236, 6, v236
Mvav 128,156,60
Ba 132,13,49152
CV 160,112,113
v_add_u32 v236, s50, v236
)ASM"
R"ASM(
Cvav 132,144,64,36
EX 114
v_min_u32 v236, Ldone_%=-Lpc2_%=-4, v236
Mvav 132,148,68
Ba 136,14,49152
v_add_f32 v28, v28, v113
global_load_dword a255, v236, s[54:55]
Mvav 132,152,72
EX 115
Mvav 132,156,76
Ba 140,15,49152
CV 161,114,115
Cvav 136,144,80,40
EX 116
Mvav 136,148,84
Ta 160,22,16384
v_add_f32 v28, v28, v114
Mvav 136,152,88
EX 117
Mvav 136,156,92
Ta 162,22,20480
CV 164,116,117
Cvav 140,144,96,44
EX 118
Mvav 140,148,100
Ta 164,22,24576
v_add_f32 v28, v28, v115
Mvav 140,152,104
s_waitcnt lgkmcnt(7)
EX 119
Mvav 140,156,108
Ta 166,22,28672
CV 165,118,119
Maav 64,176,176
EX 120
Maav 64,180,192
Ta 168,23,16384
v_add_f32 v29, v29, v116
Maav 68,176,180
EX 121
Maav 68,180,196
Ta 170,23,20480
CV 168,120,121
Maav 72,176,184
EX 122
Maav 72,180,200
Ta 172,23,24576
v_add_f32 v29, v29, v117
Maav 76,176,188
EX 123
Maav 76,180,204
Ta 174,23,28672
CV 169,122,123
Maav 80,184,176
EX 124
Maav 80,188,192
EX 125
Maav 84,184,180
CV 172,124,125
v_add_f32 v29, v29, v118
s_mul_i32 s50, s86, 352
s_add_u32 s50, s50, 128
s_min_u32 s50, s50, s87
s_lshl_b32 s50, s50, 10
s_lshl_b32 s51, s86, 6
s_xor_b32 s51, s51, 128
s_and_b32 s51, s51, 128
s_add_u32 s50, s50, s51
s_add_u32 s60, s26, s50
s_addc_u32 s61, s27, 0
s_add_u32 m0, s34, 0
Maav 84,188,196
global_load_lds_dwordx4 v244, s[60:61] offset:0
global_load_lds_dwordx4 v245, s[60:61] offset:1024
Maav 88,184,184
EX 126
Maav 88,188,200
s_waitcnt lgkmcnt(8)
EX 127
Maav 92,184,188
CV 173,126,127
v_add_f32 v29, v29, v119
Maav 92,188,204
global_load_lds_dwordx4 v246, s[60:61] offset:2048
global_load_lds_dwordx4 v247, s[60:61] offset:3072
Cvav 112,128,48,32
EX 128
Mvav 112,132,52
Ba 144,12,53248
v_add_f32 v28, v28, v128
Mvav 112,136,56
EX 129
Mvav 112,140,60
Ba 148,13,53248
CV 162,128,129
Cvav 116,128,64,36
EX 130
Mvav 116,132,68
Ba 152,14,53248
v_add_f32 v28, v28, v129
Mvav 116,136,72
EX 131
Mvav 116,140,76
Ba 156,15,53248
CV 163,130,131
Cvav 120,128,80,40
EX 132
Mvav 120,132,84
Ta 176,16,32768
v_add_f32 v28, v28, v130
Mvav 120,136,88
EX 133
Mvav 120,140,92
Ta 178,16,36864
CV 166,132,133
Cvav 124,128,96,44
EX 134
Mvav 124,132,100
Ta 180,16,40960
v_add_f32 v28, v28, v131
Mvav 124,136,104
s_waitcnt lgkmcnt(7)
EX 135
Mvav 124,140,108
Ta 182,16,45056
CV 167,134,135
Maav 96,160,176
EX 136
Maav 96,164,192
Ta 184,17,32768
v_add_f32 v29, v29, v132
Maav 100,160,180
EX 137
Maav 100,164,196
Ta 186,17,36864
CV 170,136,137
Maav 104,160,184
EX 138
Maav 104,164,200
Ta 188,17,40960
v_add_f32 v29, v29, v133
Maav 108,160,188
EX 139
Maav 108,164,204
Ta 190,17,45056
CV 171,138,139
Maav 112,168,176
EX 140
Maav 112,172,192
EX 141
Maav 116,168,180
CV 174,140,141
v_add_f32 v29, v29, v134
s_mul_i32 s50, s86, 352
s_add_u32 s50, s50, 256
s_min_u32 s50, s50, s87
s_lshl_b32 s50, s50, 10
s_lshl_b32 s51, s86, 6
s_xor_b32 s51, s51, 128
s_and_b32 s51, s51, 128
s_add_u32 s50, s50, s51
s_add_u32 s60, s26, s50
s_addc_u32 s61, s27, 0
s_add_u32 m0, s34, 16384
Maav 116,172,196
global_load_lds_dwordx4 v244, s[60:61] offset:0
global_load_lds_dwordx4 v245, s[60:61] offset:1024
Maav 120,168,184
EX 142
Maav 120,172,200
s_waitcnt lgkmcnt(8)
EX 143
Maav 124,168,188
CV 175,142,143
v_add_f32 v29, v29, v135
Maav 124,172,204
global_load_lds_dwordx4 v246, s[60:61] offset:2048
global_load_lds_dwordx4 v247, s[60:61] offset:3072
Cvav 128,144,48,32
EX 112
Mvav 128,148,52
Ba 128,12,57344
v_add_f32 v28, v28, v112
Mvav 128,152,56
EX 113
Mvav 128,156,60
Ba 132,13,57344
CV 176,112,113
Cvav 132,144,64,36
EX 114
Mvav 132,148,68
Ba 136,14,57344
v_add_f32 v28, v28, v113
Mvav 132,152,72
EX 115
Mvav 132,156,76
Ba 140,15,57344
CV 177,114,115
Cvav 136,144,80,40
EX 116
)ASM"
R"ASM(
Mvav 136,148,84
Ta 160,18,32768
v_add_f32 v28, v28, v114
Mvav 136,152,88
EX 117
Mvav 136,156,92
Ta 162,18,36864
CV 180,116,117
Cvav 140,144,96,44
EX 118
Mvav 140,148,100
Ta 164,18,40960
v_add_f32 v28, v28, v115
Mvav 140,152,104
s_waitcnt lgkmcnt(7)
EX 119
Mvav 140,156,108
Ta 166,18,45056
CV 181,118,119
Maav 0,176,144
EX 120
Maav 0,180,160
Ta 168,19,32768
v_add_f32 v29, v29, v116
Maav 4,176,148
EX 121
Maav 4,180,164
Ta 170,19,36864
CV 184,120,121
Maav 8,176,152
EX 122
Maav 8,180,168
Ta 172,19,40960
v_add_f32 v29, v29, v117
Maav 12,176,156
EX 123
Maav 12,180,172
Ta 174,19,45056
CV 185,122,123
Maav 16,184,144
EX 124
Maav 16,188,160
EX 125
Maav 20,184,148
CV 188,124,125
v_add_f32 v29, v29, v118
s_mul_i32 s50, s86, 352
s_add_u32 s50, s50, 0
s_min_u32 s50, s50, s87
s_lshl_b32 s50, s50, 10
s_lshl_b32 s51, s86, 6
s_xor_b32 s51, s51, 0
s_and_b32 s51, s51, 128
s_add_u32 s50, s50, s51
s_add_u32 s60, s26, s50
s_addc_u32 s61, s27, 0
s_add_u32 m0, s34, 65536
Maav 20,188,164
global_load_lds_dwordx4 v244, s[60:61] offset:0
global_load_lds_dwordx4 v245, s[60:61] offset:1024
Maav 24,184,152
EX 126
Maav 24,188,168
s_waitcnt lgkmcnt(8)
EX 127
Maav 28,184,156
CV 189,126,127
v_add_f32 v29, v29, v119
Maav 28,188,172
global_load_lds_dwordx4 v246, s[60:61] offset:2048
global_load_lds_dwordx4 v247, s[60:61] offset:3072
Cvav 112,128,48,32
s_waitcnt vmcnt(13)
EX 128
Mvav 112,132,52
ds_write_b32 v2, v4 offset:0
Mvav 112,136,56
Ba 144,12,61440
v_add_f32 v28, v28, v128
Mvav 112,140,60
s_add_u32 s44, s44, 1
EX 129
Cvav 116,128,64,36
Ba 148,13,61440
CV 178,128,129
Mvav 116,132,68
EX 130
Mvav 116,136,72
Ba 152,14,61440
v_add_f32 v28, v28, v129
Mvav 116,140,76
EX 131
Cvav 120,128,80,40
Ba 156,15,61440
CV 179,130,131
Mvav 120,132,84
EX 132
Mvav 120,136,88
Ta 176,20,32768
v_add_f32 v28, v28, v130
Mvav 120,140,92
EX 133
Cvav 124,128,96,44
Ta 178,20,36864
CV 182,132,133
Mvav 124,132,100
EX 134
Mvav 124,136,104
Ta 180,20,40960
v_add_f32 v28, v28, v131
Mvav 124,140,108
s_waitcnt lgkmcnt(8)
EX 135
Maav 32,160,144
Ta 182,20,45056
CV 183,134,135
Maav 32,164,160
EX 136
Maav 36,160,148
Ta 184,21,32768
v_add_f32 v29, v29, v132
Maav 36,164,164
EX 137
Maav 40,160,152
Ta 186,21,36864
CV 186,136,137
Maav 40,164,168
ds_read_b32 v5, v3 offset:0
v_add_u32 v4, 1, v4
Maav 44,160,156
EX 138
Maav 44,164,172
Ta 188,21,40960
v_add_f32 v29, v29, v133
Maav 48,168,144
EX 139
Maav 48,172,160
Ta 190,21,45056
CV 187,138,139
Maav 52,168,148
EX 140
Maav 52,172,164
EX 141
Maav 56,168,152
s_waitcnt lgkmcnt(2)
v_cmp_gt_u32 vcc, s44, v5
Maav 56,172,168
CV 190,140,141
v_add_f32 v29, v29, v134
Maav 60,168,156
EX 142
Maav 60,172,172
EX 143
Maav 200,208,152
CV 191,142,143
v_add_f32 v29, v29, v135
Maav 200,208,168
s_cbranch_vccnz Lspin226_%=
Lback226_%=:
Maav 204,208,156
global_load_dwordx4 v[252:255], v24, s[62:63] offset:2048 sc1
Maav 204,208,172
global_load_dwordx4 v[236:239], v248, s[62:63] offset:2048 sc1
Cvav 128,144,48,32
EX 112
Mvav 128,148,52
v_add_f32 v28, v28, v112
global_load_dwordx4 a[128:131], v24, s[62:63] offset:3072 sc1
Mvav 128,152,56
EX 113
Mvav 128,156,60
CV 192,112,113
global_load_dwordx4 a[132:135], v248, s[62:63] offset:3072 sc1
Cvav 132,144,64,36
EX 114
Mvav 132,148,68
v_add_f32 v28, v28, v113
s_add_u32 s62, s84, 8192
s_addc_u32 s63, s85, 0
global_load_dwordx4 a[136:139], v24, s[62:63] offset:0 sc1
Mvav 132,152,72
EX 115
Mvav 132,156,76
CV 193,114,115
global_load_dwordx4 a[140:143], v248, s[62:63] offset:0 sc1
Cvav 136,144,80,40
EX 116
Mvav 136,148,84
Ta 160,22,32768
v_add_f32 v28, v28, v114
Mvav 136,152,88
EX 117
Mvav 136,156,92
Ta 162,22,36864
CV 196,116,117
Cvav 140,144,96,44
EX 118
Mvav 140,148,100
Ta 164,22,40960
v_add_f32 v28, v28, v115
)ASM"
R"ASM(
Mvav 140,152,104
s_waitcnt lgkmcnt(3)
EX 119
Mvav 140,156,108
Ta 166,22,45056
CV 197,118,119
Maav 64,176,144
EX 120
Maav 64,180,160
Ta 168,23,32768
v_add_f32 v29, v29, v116
Maav 68,176,148
EX 121
Maav 68,180,164
Ta 170,23,36864
CV 200,120,121
Maav 72,176,152
EX 122
Maav 72,180,168
Ta 172,23,40960
v_add_f32 v29, v29, v117
Maav 76,176,156
EX 123
Maav 76,180,172
Ta 174,23,45056
CV 201,122,123
Maav 80,184,144
EX 124
Maav 80,188,160
EX 125
Maav 84,184,148
CV 204,124,125
v_add_f32 v29, v29, v118
s_mul_i32 s50, s86, 352
s_add_u32 s50, s50, 128
s_min_u32 s50, s50, s87
s_lshl_b32 s50, s50, 10
s_lshl_b32 s51, s86, 6
s_xor_b32 s51, s51, 0
s_and_b32 s51, s51, 128
s_add_u32 s50, s50, s51
s_add_u32 s60, s26, s50
s_addc_u32 s61, s27, 0
s_add_u32 m0, s34, 32768
Maav 84,188,164
global_load_lds_dwordx4 v244, s[60:61] offset:0
global_load_lds_dwordx4 v245, s[60:61] offset:1024
Maav 88,184,152
EX 126
Maav 88,188,168
EX 127
Maav 92,184,156
CV 205,126,127
v_add_f32 v29, v29, v119
Maav 92,188,172
global_load_lds_dwordx4 v246, s[60:61] offset:2048
global_load_lds_dwordx4 v247, s[60:61] offset:3072
EX 128
s_nop 0
v_add_f32 v28, v28, v128
global_load_dwordx4 a[144:147], v24, s[62:63] offset:1024 sc1
EX 129
s_nop 0
CV 194,128,129
global_load_dwordx4 a[148:151], v248, s[62:63] offset:1024 sc1
EX 130
v_add_f32 v28, v28, v129
global_load_dwordx4 a[152:155], v24, s[62:63] offset:2048 sc1
EX 131
s_nop 0
CV 195,130,131
global_load_dwordx4 a[156:159], v248, s[62:63] offset:2048 sc1
EX 132
Ta 176,16,49152
v_add_f32 v28, v28, v130
EX 133
Ta 178,16,53248
CV 198,132,133
EX 134
Ta 180,16,57344
v_add_f32 v28, v28, v131
s_waitcnt lgkmcnt(3)
EX 135
Ta 182,16,61440
CV 199,134,135
Maav 96,160,144
EX 136
Maav 96,164,160
Ta 184,17,49152
v_add_f32 v29, v29, v132
Maav 100,160,148
EX 137
Maav 100,164,164
Ta 186,17,53248
CV 202,136,137
Maav 104,160,152
EX 138
Maav 104,164,168
Ta 188,17,57344
v_add_f32 v29, v29, v133
Maav 108,160,156
EX 139
Maav 108,164,172
Ta 190,17,61440
CV 203,138,139
Maav 112,168,144
EX 140
Maav 112,172,160
EX 141
Maav 116,168,148
CV 206,140,141
v_add_f32 v29, v29, v134
s_mul_i32 s50, s86, 352
s_add_u32 s50, s50, 256
s_min_u32 s50, s50, s87
s_lshl_b32 s50, s50, 10
s_lshl_b32 s51, s86, 6
s_xor_b32 s51, s51, 0
s_and_b32 s51, s51, 128
s_add_u32 s50, s50, s51
s_add_u32 s60, s26, s50
s_addc_u32 s61, s27, 0
s_add_u32 m0, s34, 81920
Maav 116,172,164
global_load_lds_dwordx4 v244, s[60:61] offset:0
global_load_lds_dwordx4 v245, s[60:61] offset:1024
Maav 120,168,152
EX 142
Maav 120,172,168
EX 143
Maav 124,168,156
CV 207,142,143
v_add_f32 v29, v29, v135
Maav 124,172,172
global_load_lds_dwordx4 v246, s[60:61] offset:2048
global_load_lds_dwordx4 v247, s[60:61] offset:3072
Ta 160,18,49152
Ta 162,18,53248
Maav 200,208,184
Maav 200,208,200
Ta 164,18,57344
Maav 204,208,188
s_waitcnt lgkmcnt(3)
Maav 204,208,204
Ta 166,18,61440
Maav 0,176,176
Maav 0,180,192
Ta 168,19,49152
Maav 4,176,180
Maav 4,180,196
Ta 170,19,53248
Maav 8,176,184
Maav 8,180,200
Ta 172,19,57344
Maav 12,176,188
Maav 12,180,204
Ta 174,19,61440
Maav 16,184,176
Maav 16,188,192
Maav 20,184,180
Maav 20,188,196
Maav 24,184,184
Maav 24,188,200
Maav 28,184,188
Maav 28,188,204
Ta 176,20,49152
Ta 178,20,53248
Ta 180,20,57344
s_waitcnt lgkmcnt(3)
Ta 182,20,61440
Maav 32,160,176
v_mov_b32 v33, v28
v_mov_b32 v37, v29
v_accvgpr_read_b32 v41, a200
v_permlane32_swap_b32 v28, v33
v_permlane32_swap_b32 v29, v37
v_add_f32 v28, v28, v33
v_add_f32 v29, v29, v37
Maav 32,164,192
Ta 184,21,49152
v_mov_b32 v33, v28
v_mov_b32 v37, v29
v_accvgpr_read_b32 v45, a204
v_permlane16_swap_b32 v28, v33
v_permlane16_swap_b32 v29, v37
v_add_f32 v33, v28, v33
v_add_f32 v37, v29, v37
Maav 36,160,180
v_rcp_f32 v28, v33
v_rcp_f32 v29, v37
v_rcp_f32 v30, v41
v_rcp_f32 v31, v45
v_accvgpr_read_b32 v208, a0
v_accvgpr_read_b32 v209, a1
v_accvgpr_read_b32 v210, a2
Maav 36,164,196
Ta 186,21,53248
v_accvgpr_read_b32 v211, a3
v_accvgpr_read_b32 v212, a16
v_accvgpr_read_b32 v213, a17
)ASM"
R"ASM(
v_accvgpr_read_b32 v214, a18
v_accvgpr_read_b32 v215, a19
v_mul_f32 v208, v208, v28
v_mul_f32 v209, v209, v28
Maav 40,160,184
v_mul_f32 v210, v210, v28
v_mul_f32 v211, v211, v28
v_mul_f32 v212, v212, v28
v_mul_f32 v213, v213, v28
v_mul_f32 v214, v214, v28
v_mul_f32 v215, v215, v28
CV 216,208,209
Maav 40,164,200
Ta 188,21,57344
CV 217,210,211
CV 218,212,213
CV 219,214,215
global_store_dwordx4 v24, v[216:219], s[48:49] offset:0 sc1
v_accvgpr_read_b32 v224, a4
v_accvgpr_read_b32 v225, a5
v_accvgpr_read_b32 v226, a6
Maav 44,160,188
v_accvgpr_read_b32 v227, a7
v_accvgpr_read_b32 v228, a20
v_accvgpr_read_b32 v229, a21
v_accvgpr_read_b32 v230, a22
v_accvgpr_read_b32 v231, a23
v_mul_f32 v224, v224, v29
v_mul_f32 v225, v225, v29
Maav 44,164,204
Ta 190,21,61440
v_mul_f32 v226, v226, v29
v_mul_f32 v227, v227, v29
v_mul_f32 v228, v228, v29
v_mul_f32 v229, v229, v29
v_mul_f32 v230, v230, v29
v_mul_f32 v231, v231, v29
CV 232,224,225
Maav 48,168,176
CV 233,226,227
CV 234,228,229
CV 235,230,231
global_store_dwordx4 v25, v[232:235], s[48:49] offset:0 sc1
v_accvgpr_read_b32 v208, a8
v_accvgpr_read_b32 v209, a9
v_accvgpr_read_b32 v210, a10
Maav 48,172,192
v_accvgpr_read_b32 v211, a11
v_accvgpr_read_b32 v212, a24
v_accvgpr_read_b32 v213, a25
v_accvgpr_read_b32 v214, a26
v_accvgpr_read_b32 v215, a27
v_mul_f32 v208, v208, v30
v_mul_f32 v209, v209, v30
Maav 52,168,180
v_mul_f32 v210, v210, v30
v_mul_f32 v211, v211, v30
v_mul_f32 v212, v212, v30
v_mul_f32 v213, v213, v30
v_mul_f32 v214, v214, v30
v_mul_f32 v215, v215, v30
CV 216,208,209
Maav 52,172,196
CV 217,210,211
CV 218,212,213
CV 219,214,215
global_store_dwordx4 v26, v[216:219], s[48:49] offset:0 sc1
v_accvgpr_read_b32 v224, a12
v_accvgpr_read_b32 v225, a13
v_accvgpr_read_b32 v226, a14
Maav 56,168,184
v_accvgpr_read_b32 v227, a15
v_accvgpr_read_b32 v228, a28
v_accvgpr_read_b32 v229, a29
v_accvgpr_read_b32 v230, a30
v_accvgpr_read_b32 v231, a31
v_mul_f32 v224, v224, v31
v_mul_f32 v225, v225, v31
Maav 56,172,200
v_mul_f32 v226, v226, v31
v_mul_f32 v227, v227, v31
v_mul_f32 v228, v228, v31
v_mul_f32 v229, v229, v31
v_mul_f32 v230, v230, v31
v_mul_f32 v231, v231, v31
CV 232,224,225
Maav 60,168,188
CV 233,226,227
CV 234,228,229
CV 235,230,231
global_store_dwordx4 v27, v[232:235], s[48:49] offset:0 sc1
Maav 60,172,204
Ta 160,22,49152
Ta 162,22,53248
Ta 164,22,57344
s_waitcnt lgkmcnt(3)
Ta 166,22,61440
Maav 64,176,176
v_accvgpr_read_b32 v208, a32
v_accvgpr_read_b32 v209, a33
v_accvgpr_read_b32 v210, a34
v_accvgpr_read_b32 v211, a35
v_accvgpr_read_b32 v212, a48
v_accvgpr_read_b32 v213, a49
Maav 64,180,192
Ta 168,23,49152
v_accvgpr_read_b32 v214, a50
v_accvgpr_read_b32 v215, a51
v_mul_f32 v208, v208, v28
v_mul_f32 v209, v209, v28
v_mul_f32 v210, v210, v28
v_mul_f32 v211, v211, v28
Maav 68,176,180
v_mul_f32 v212, v212, v28
v_mul_f32 v213, v213, v28
v_mul_f32 v214, v214, v28
v_mul_f32 v215, v215, v28
CV 216,208,209
CV 217,210,211
Maav 68,180,196
Ta 170,23,53248
CV 218,212,213
CV 219,214,215
global_store_dwordx4 v24, v[216:219], s[48:49] offset:1024 sc1
v_accvgpr_read_b32 v224, a36
v_accvgpr_read_b32 v225, a37
v_accvgpr_read_b32 v226, a38
Maav 72,176,184
v_accvgpr_read_b32 v227, a39
v_accvgpr_read_b32 v228, a52
v_accvgpr_read_b32 v229, a53
v_accvgpr_read_b32 v230, a54
v_accvgpr_read_b32 v231, a55
v_mul_f32 v224, v224, v29
Maav 72,180,200
Ta 172,23,57344
v_mul_f32 v225, v225, v29
v_mul_f32 v226, v226, v29
v_mul_f32 v227, v227, v29
v_mul_f32 v228, v228, v29
v_mul_f32 v229, v229, v29
v_mul_f32 v230, v230, v29
Maav 76,176,188
v_mul_f32 v231, v231, v29
CV 232,224,225
CV 233,226,227
CV 234,228,229
CV 235,230,231
global_store_dwordx4 v25, v[232:235], s[48:49] offset:1024 sc1
Maav 76,180,204
Ta 174,23,61440
s_waitcnt vmcnt(6)
v_add_f32 v222, v33, v37
v_add_f32 v223, v41, v45
v_add_f32 v222, v222, v223
s_mov_b32 s50, 1400897536
v_cmp_nge_f32 vcc, s50, v222
s_nop 2
s_cmp_lg_u64 vcc, 0
s_cselect_b32 s50, 1, 0
v_mov_b32 v222, s50
ds_write_b32 v2, v222 offset:1024
ds_write_b32 v2, v4 offset:0
s_add_u32 s44, s44, 1
v_add_u32 v4, 1, v4
v_accvgpr_read_b32 v208, a40
v_accvgpr_read_b32 v209, a41
v_accvgpr_read_b32 v210, a42
v_accvgpr_read_b32 v211, a43
Maav 80,184,176
v_accvgpr_read_b32 v212, a56
v_accvgpr_read_b32 v213, a57
v_accvgpr_read_b32 v214, a58
v_accvgpr_read_b32 v215, a59
v_mul_f32 v208, v208, v30
v_mul_f32 v209, v209, v30
Maav 80,188,192
v_mul_f32 v210, v210, v30
v_mul_f32 v211, v211, v30
v_mul_f32 v212, v212, v30
v_mul_f32 v213, v213, v30
v_mul_f32 v214, v214, v30
v_mul_f32 v215, v215, v30
Maav 84,184,180
CV 216,208,209
CV 217,210,211
CV 218,212,213
CV 219,214,215
global_store_dwordx4 v26, v[216:219], s[48:49] offset:1024 sc1
v_accvgpr_read_b32 v224, a44
Maav 84,188,196
v_accvgpr_read_b32 v225, a45
v_accvgpr_read_b32 v226, a46
v_accvgpr_read_b32 v227, a47
v_accvgpr_read_b32 v228, a60
v_accvgpr_read_b32 v229, a61
v_accvgpr_read_b32 v230, a62
Maav 88,184,184
v_accvgpr_read_b32 v231, a63
v_mul_f32 v224, v224, v31
)ASM"
R"ASM(
v_mul_f32 v225, v225, v31
v_mul_f32 v226, v226, v31
v_mul_f32 v227, v227, v31
v_mul_f32 v228, v228, v31
Maav 88,188,200
v_mul_f32 v229, v229, v31
v_mul_f32 v230, v230, v31
v_mul_f32 v231, v231, v31
CV 232,224,225
CV 233,226,227
CV 234,228,229
Maav 92,184,188
CV 235,230,231
global_store_dwordx4 v27, v[232:235], s[48:49] offset:1024 sc1
Maav 92,188,204
s_waitcnt lgkmcnt(2)
Maav 96,160,176
ds_read_b32 v5, v3 offset:0
v_accvgpr_read_b32 v208, a64
v_accvgpr_read_b32 v209, a65
v_accvgpr_read_b32 v210, a66
v_accvgpr_read_b32 v211, a67
v_accvgpr_read_b32 v212, a80
Maav 96,164,192
v_accvgpr_read_b32 v213, a81
v_accvgpr_read_b32 v214, a82
v_accvgpr_read_b32 v215, a83
v_mul_f32 v208, v208, v28
v_mul_f32 v209, v209, v28
v_mul_f32 v210, v210, v28
Maav 100,160,180
v_mul_f32 v211, v211, v28
v_mul_f32 v212, v212, v28
v_mul_f32 v213, v213, v28
v_mul_f32 v214, v214, v28
v_mul_f32 v215, v215, v28
CV 216,208,209
Maav 100,164,196
CV 217,210,211
CV 218,212,213
CV 219,214,215
global_store_dwordx4 v24, v[216:219], s[48:49] offset:2048 sc1
s_waitcnt lgkmcnt(0)
v_cmp_gt_u32 vcc, s44, v5
s_cbranch_vccnz Lspin400_%=
Lback400_%=:
ds_read_b32 v222, v3 offset:1024
v_accvgpr_read_b32 v224, a68
Maav 104,160,184
v_accvgpr_read_b32 v225, a69
v_accvgpr_read_b32 v226, a70
v_accvgpr_read_b32 v227, a71
v_accvgpr_read_b32 v228, a84
v_accvgpr_read_b32 v229, a85
v_accvgpr_read_b32 v230, a86
Maav 104,164,200
v_accvgpr_read_b32 v231, a87
v_mul_f32 v224, v224, v29
v_mul_f32 v225, v225, v29
v_mul_f32 v226, v226, v29
v_mul_f32 v227, v227, v29
v_mul_f32 v228, v228, v29
Maav 108,160,188
v_mul_f32 v229, v229, v29
v_mul_f32 v230, v230, v29
v_mul_f32 v231, v231, v29
CV 232,224,225
CV 233,226,227
CV 234,228,229
Maav 108,164,204
CV 235,230,231
global_store_dwordx4 v25, v[232:235], s[48:49] offset:2048 sc1
s_waitcnt lgkmcnt(0)
v_cmp_ne_u32 vcc, 0, v222
s_cbranch_vccnz Lredo_%=
Lnoredo_%=:
v_accvgpr_read_b32 v208, a72
v_accvgpr_read_b32 v209, a73
v_accvgpr_read_b32 v210, a74
Maav 112,168,176
v_accvgpr_read_b32 v211, a75
v_accvgpr_read_b32 v212, a88
v_accvgpr_read_b32 v213, a89
v_accvgpr_read_b32 v214, a90
v_accvgpr_read_b32 v215, a91
v_mul_f32 v208, v208, v30
Maav 112,172,192
v_mul_f32 v209, v209, v30
v_mul_f32 v210, v210, v30
v_mul_f32 v211, v211, v30
v_mul_f32 v212, v212, v30
v_mul_f32 v213, v213, v30
v_mul_f32 v214, v214, v30
Maav 116,168,180
v_mul_f32 v215, v215, v30
CV 216,208,209
CV 217,210,211
CV 218,212,213
CV 219,214,215
global_store_dwordx4 v26, v[216:219], s[48:49] offset:2048 sc1
Maav 116,172,196
v_accvgpr_read_b32 v224, a76
v_accvgpr_read_b32 v225, a77
v_accvgpr_read_b32 v226, a78
v_accvgpr_read_b32 v227, a79
v_accvgpr_read_b32 v228, a92
v_accvgpr_read_b32 v229, a93
Maav 120,168,184
v_accvgpr_read_b32 v230, a94
v_accvgpr_read_b32 v231, a95
v_mul_f32 v224, v224, v31
v_mul_f32 v225, v225, v31
v_mul_f32 v226, v226, v31
v_mul_f32 v227, v227, v31
Maav 120,172,200
v_mul_f32 v228, v228, v31
v_mul_f32 v229, v229, v31
v_mul_f32 v230, v230, v31
v_mul_f32 v231, v231, v31
CV 232,224,225
CV 233,226,227
Maav 124,168,188
CV 234,228,229
CV 235,230,231
global_store_dwordx4 v27, v[232:235], s[48:49] offset:2048 sc1
Maav 124,172,204
v_accvgpr_read_b32 v208, a96
v_accvgpr_read_b32 v209, a97
v_accvgpr_read_b32 v210, a98
v_accvgpr_read_b32 v211, a99
v_accvgpr_read_b32 v212, a112
v_accvgpr_read_b32 v213, a113
v_accvgpr_read_b32 v214, a114
v_accvgpr_read_b32 v215, a115
v_mul_f32 v208, v208, v28
v_mul_f32 v209, v209, v28
v_mul_f32 v210, v210, v28
v_mul_f32 v211, v211, v28
v_mul_f32 v212, v212, v28
v_mul_f32 v213, v213, v28
v_mul_f32 v214, v214, v28
v_mul_f32 v215, v215, v28
CV 216,208,209
CV 217,210,211
CV 218,212,213
CV 219,214,215
global_store_dwordx4 v24, v[216:219], s[48:49] offset:3072 sc1
v_accvgpr_read_b32 v224, a100
v_accvgpr_read_b32 v225, a101
v_accvgpr_read_b32 v226, a102
v_accvgpr_read_b32 v227, a103
v_accvgpr_read_b32 v228, a116
v_accvgpr_read_b32 v229, a117
v_accvgpr_read_b32 v230, a118
v_accvgpr_read_b32 v231, a119
v_mul_f32 v224, v224, v29
v_mul_f32 v225, v225, v29
v_mul_f32 v226, v226, v29
v_mul_f32 v227, v227, v29
v_mul_f32 v228, v228, v29
v_mul_f32 v229, v229, v29
v_mul_f32 v230, v230, v29
v_mul_f32 v231, v231, v29
CV 232,224,225
CV 233,226,227
CV 234,228,229
CV 235,230,231
global_store_dwordx4 v25, v[232:235], s[48:49] offset:3072 sc1
v_accvgpr_read_b32 v208, a104
v_accvgpr_read_b32 v209, a105
v_accvgpr_read_b32 v210, a106
v_accvgpr_read_b32 v211, a107
v_accvgpr_read_b32 v212, a120
v_accvgpr_read_b32 v213, a121
v_accvgpr_read_b32 v214, a122
v_accvgpr_read_b32 v215, a123
v_mul_f32 v208, v208, v30
v_mul_f32 v209, v209, v30
v_mul_f32 v210, v210, v30
v_mul_f32 v211, v211, v30
v_mul_f32 v212, v212, v30
v_mul_f32 v213, v213, v30
v_mul_f32 v214, v214, v30
v_mul_f32 v215, v215, v30
CV 216,208,209
CV 217,210,211
CV 218,212,213
CV 219,214,215
global_store_dwordx4 v26, v[216:219], s[48:49] offset:3072 sc1
v_accvgpr_read_b32 v224, a108
v_accvgpr_read_b32 v225, a109
v_accvgpr_read_b32 v226, a110
v_accvgpr_read_b32 v227, a111
v_accvgpr_read_b32 v228, a124
v_accvgpr_read_b32 v229, a125
v_accvgpr_read_b32 v230, a126
v_accvgpr_read_b32 v231, a127
v_mul_f32 v224, v224, v31
v_mul_f32 v225, v225, v31
v_mul_f32 v226, v226, v31
)ASM"
R"ASM(
v_mul_f32 v227, v227, v31
v_mul_f32 v228, v228, v31
v_mul_f32 v229, v229, v31
v_mul_f32 v230, v230, v31
v_mul_f32 v231, v231, v31
CV 232,224,225
CV 233,226,227
CV 234,228,229
CV 235,230,231
global_store_dwordx4 v27, v[232:235], s[48:49] offset:3072 sc1
s_bitcmp0_b32 s40, 2
s_cbranch_scc1 Leven_%=
s_cmp_lg_u32 s86, 3
s_cbranch_scc1 Lnz7_%=
v_accvgpr_write_b32 a152, 0
v_accvgpr_write_b32 a153, 0
v_accvgpr_write_b32 a154, 0
v_accvgpr_write_b32 a155, 0
v_accvgpr_write_b32 a156, 0
v_accvgpr_write_b32 a157, 0
v_accvgpr_write_b32 a158, 0
v_accvgpr_write_b32 a159, 0
Lnz7_%=:
v_add_u32 v240, 131072, v240
Tv 112,240,0
Tv 114,240,2048
Tv 116,240,4096
Tv 118,240,6144
Tv 120,240,8192
Tv 122,240,10240
Tv 124,240,12288
Tv 126,240,14336
v_add_u32 v241, 131072, v241
Tv 128,241,0
Tv 130,241,2048
Tv 132,241,4096
Tv 134,241,6144
Tv 136,241,8192
Tv 138,241,10240
Tv 140,241,12288
Tv 142,241,14336
v_add_u32 v242, 131072, v242
Tv 144,242,0
Tv 146,242,2048
Tv 148,242,4096
Tv 150,242,6144
Tv 152,242,8192
Tv 154,242,10240
Tv 156,242,12288
Tv 158,242,14336
s_waitcnt lgkmcnt(15)
Zvva 80,112,212
v_add_u32 v243, 131072, v243
Tv 160,243,0
Tv 162,243,2048
Mvva 80,116,220
Mvva 80,120,228
Tv 164,243,4096
Tv 166,243,6144
Mvva 80,124,236
ds_write_b32 v2, v4 offset:0
v_add_u32 v4, 1, v4
Zvva 84,112,216
Tv 168,243,8192
Mvva 84,116,224
Tv 170,243,10240
Tv 172,243,12288
Mvva 84,120,232
Tv 174,243,14336
Mvva 84,124,240
s_waitcnt lgkmcnt(15)
v_add_u32 v240, 4294836224, v240
Zvva 88,128,212
Tv 112,240,0
Tv 114,240,2048
Mvva 88,132,220
Tv 116,240,4096
Mvva 88,136,228
Tv 118,240,6144
Mvva 88,140,236
Tv 120,240,8192
Zvva 92,128,216
Tv 122,240,10240
Mvva 92,132,224
Tv 124,240,12288
Mvva 92,136,232
Tv 126,240,14336
Mvva 92,140,240
s_waitcnt lgkmcnt(15)
v_add_u32 v241, 4294836224, v241
Zvva 96,144,212
Tv 128,241,0
Tv 130,241,2048
Mvva 96,148,220
Tv 132,241,4096
Mvva 96,152,228
Tv 134,241,6144
Mvva 96,156,236
Tv 136,241,8192
Zvva 100,144,216
Tv 138,241,10240
Mvva 100,148,224
Tv 140,241,12288
Mvva 100,152,232
Tv 142,241,14336
Mvva 100,156,240
s_waitcnt lgkmcnt(15)
v_add_u32 v242, 4294836224, v242
Zvva 104,160,212
Tv 144,242,0
Mvva 104,164,220
Tv 146,242,2048
Mvva 104,168,228
Tv 148,242,4096
Mvva 104,172,236
Tv 150,242,6144
Zvva 108,160,216
Tv 152,242,8192
Mvva 108,164,224
Tv 154,242,10240
Mvva 108,168,232
Tv 156,242,12288
Mvva 108,172,240
Tv 158,242,14336
s_waitcnt lgkmcnt(15)
Mvva 80,112,244
v_add_u32 v243, 4294836224, v243
Tv 160,243,0
Tv 162,243,2048
Mvva 80,116,192
ds_read_b32 v5, v3 offset:0
s_add_u32 s44, s44, 1
Mvvv 80,120,252
Tv 164,243,4096
Tv 166,243,6144
Mvva 80,124,128
ds_write_b32 v2, v4 offset:0
v_add_u32 v4, 1, v4
Mvva 84,112,248
Tv 168,243,8192
Mvva 84,116,196
Tv 170,243,10240
Tv 172,243,12288
Mvvv 84,120,236
Tv 174,243,14336
Mvva 84,124,132
s_waitcnt lgkmcnt(15)
s_waitcnt lgkmcnt(7)
v_cmp_gt_u32 vcc, s44, v5
s_cbranch_vccnz Lspin1701_%=
Lback1701_%=:
Mvva 88,128,244
Tv 112,240,16384
Mvva 88,132,192
Tv 114,240,18432
Mvvv 88,136,252
Tv 116,240,20480
Mvva 88,140,128
Tv 118,240,22528
Mvva 92,128,248
Tv 120,240,24576
Mvva 92,132,196
Tv 122,240,26624
Mvvv 92,136,236
Mvva 92,140,132
Mvva 96,144,244
Tv 128,241,16384
Mvva 96,148,192
Tv 130,241,18432
Mvvv 96,152,252
Tv 132,241,20480
Mvva 96,156,128
Tv 134,241,22528
Mvva 100,144,248
Tv 136,241,24576
Mvva 100,148,196
Tv 138,241,26624
Mvvv 100,152,236
Mvva 100,156,132
s_waitcnt lgkmcnt(12)
Mvva 104,160,244
Tv 144,242,16384
Mvva 104,164,192
Tv 146,242,18432
Mvvv 104,168,252
Tv 148,242,20480
Mvva 104,172,128
Mvva 108,160,248
Tv 150,242,22528
Mvva 108,164,196
Tv 152,242,24576
Mvvv 108,168,236
Tv 154,242,26624
Mvva 108,172,132
s_waitcnt vmcnt(0)
s_mov_b32 s72, 0
s_call_b64 s[76:77], Lnotify_%=
s_waitcnt lgkmcnt(12)
Mvva 80,112,136
Tv 160,243,16384
)ASM"
R"ASM(
Tv 162,243,18432
Mvva 80,116,144
ds_read_b32 v5, v3 offset:0
s_add_u32 s44, s44, 1
Mvva 80,120,152
Tv 164,243,20480
Tv 166,243,22528
Mvva 84,112,140
ds_write_b32 v2, v4 offset:0
v_add_u32 v4, 1, v4
Mvva 84,116,148
Tv 168,243,24576
Tv 170,243,26624
Mvva 84,120,156
s_waitcnt lgkmcnt(14)
s_waitcnt lgkmcnt(5)
v_cmp_gt_u32 vcc, s44, v5
s_cbranch_vccnz Lspin1702_%=
Lback1702_%=:
v_add_u32 v242, 65536, v242
Mvva 88,128,136
Tv 112,242,0
Tv 114,242,2048
Tv 116,242,4096
Mvva 88,132,144
s_xor_b32 s50, s89, 0
s_lshl_b32 s50, s50, 17
s_lshl_b32 s51, s86, 6
s_xor_b32 s51, s51, 128
s_and_b32 s51, s51, 128
s_add_u32 s50, s50, s51
s_add_u32 s60, s24, s50
s_addc_u32 s61, s25, 0
s_add_u32 m0, s34, 49152
s_nop 0
global_load_lds_dwordx4 v244, s[60:61] offset:0
Mvva 88,136,152
Tv 118,242,6144
Tv 120,242,8192
Tv 122,242,10240
Mvva 92,128,140
global_load_lds_dwordx4 v245, s[60:61] offset:1024
Mvva 92,132,148
Tv 124,242,12288
Tv 126,242,14336
Mvva 92,136,156
global_load_lds_dwordx4 v246, s[60:61] offset:2048
v_add_u32 v243, 65536, v243
Mvva 96,144,136
Tv 128,243,0
Tv 130,243,2048
Tv 132,243,4096
Mvva 96,148,144
global_load_lds_dwordx4 v247, s[60:61] offset:3072
Mvva 96,152,152
Tv 134,243,6144
Tv 136,243,8192
Tv 138,243,10240
Mvva 100,144,140
s_xor_b32 s50, s89, 0
s_lshl_b32 s50, s50, 17
s_lshl_b32 s51, s86, 6
s_xor_b32 s51, s51, 0
s_and_b32 s51, s51, 128
s_add_u32 s50, s50, s51
s_add_u32 s60, s24, s50
s_addc_u32 s61, s25, 0
s_add_u32 m0, s34, 98304
s_nop 0
global_load_lds_dwordx4 v244, s[60:61] offset:0
Mvva 100,148,148
Tv 140,243,12288
Tv 142,243,14336
Mvva 100,152,156
s_waitcnt lgkmcnt(15)
v_add_u32 v242, 4294901760, v242
Mvva 104,160,136
Tv 144,242,32768
Tv 146,242,34816
Tv 148,242,36864
Mvva 104,164,144
global_load_lds_dwordx4 v245, s[60:61] offset:1024
Mvva 104,168,152
Tv 150,242,38912
Tv 152,242,40960
Tv 154,242,43008
Mvva 108,160,140
global_load_lds_dwordx4 v246, s[60:61] offset:2048
Mvva 108,164,148
Tv 156,242,45056
Tv 158,242,47104
Mvva 108,168,156
global_load_lds_dwordx4 v247, s[60:61] offset:3072
s_waitcnt lgkmcnt(15)
Zvva 64,112,212
v_add_u32 v243, 4294901760, v243
Tv 160,243,32768
Tv 162,243,34816
Mvva 64,116,220
ds_read_b32 v5, v3 offset:0
s_add_u32 s44, s44, 1
Mvva 64,120,228
Tv 164,243,36864
Tv 166,243,38912
Mvva 64,124,236
ds_write_b32 v2, v4 offset:0
v_add_u32 v4, 1, v4
Zvva 68,112,216
Tv 168,243,40960
Mvva 68,116,224
Tv 170,243,43008
Tv 172,243,45056
Mvva 68,120,232
Tv 174,243,47104
Mvva 68,124,240
s_waitcnt lgkmcnt(15)
s_waitcnt lgkmcnt(7)
v_cmp_gt_u32 vcc, s44, v5
s_cbranch_vccnz Lspin1703_%=
Lback1703_%=:
v_add_u32 v242, 65536, v242
Zvva 72,128,212
Tv 112,242,16384
Tv 114,242,18432
Mvva 72,132,220
s_xor_b32 s50, s86, 2
s_lshl_b32 s50, s50, 16
s_add_u32 s50, s50, 4096
s_add_u32 s62, s82, s50
s_addc_u32 s63, s83, 0
CV 80,80,81
CV 81,82,83
CV 82,84,85
CV 83,86,87
global_store_dwordx4 v24, v[80:83], s[62:63] offset:2048 sc1
Mvva 72,136,228
Tv 116,242,20480
Mvva 72,140,236
CV 88,88,89
CV 89,90,91
CV 90,92,93
CV 91,94,95
global_store_dwordx4 v24, v[88:91], s[62:63] offset:3072 sc1
Zvva 76,128,216
Tv 118,242,22528
Tv 120,242,24576
Mvva 76,132,224
s_xor_b32 s50, s86, 3
s_lshl_b32 s50, s50, 16
s_add_u32 s50, s50, 8192
s_add_u32 s62, s82, s50
s_addc_u32 s63, s83, 0
CV 96,96,97
CV 97,98,99
CV 98,100,101
CV 99,102,103
global_store_dwordx4 v24, v[96:99], s[62:63] offset:2048 sc1
Mvva 76,136,232
Tv 122,242,26624
Mvva 76,140,240
CV 104,104,105
CV 105,106,107
CV 106,108,109
CV 107,110,111
global_store_dwordx4 v24, v[104:107], s[62:63] offset:3072 sc1
v_add_u32 v243, 65536, v243
Mvva 64,144,244
Tv 128,243,16384
Tv 130,243,18432
Mvva 64,148,192
s_xor_b32 s50, s89, 1
s_lshl_b32 s50, s50, 17
s_lshl_b32 s51, s86, 6
s_xor_b32 s51, s51, 128
s_and_b32 s51, s51, 128
s_add_u32 s50, s50, s51
s_add_u32 s60, s24, s50
s_addc_u32 s61, s25, 0
s_add_u32 m0, s34, 114688
s_nop 0
global_load_lds_dwordx4 v244, s[60:61] offset:0
Mvvv 64,152,252
Tv 132,243,20480
Mvva 64,156,128
global_load_lds_dwordx4 v245, s[60:61] offset:1024
Mvva 68,144,248
Tv 134,243,22528
Tv 136,243,24576
Mvva 68,148,196
global_load_lds_dwordx4 v246, s[60:61] offset:2048
Mvvv 68,152,236
Tv 138,243,26624
Mvva 68,156,132
s_waitcnt lgkmcnt(12)
global_load_lds_dwordx4 v247, s[60:61] offset:3072
v_add_u32 v240, 65536, v240
Mvva 72,160,244
Tv 144,240,0
Tv 146,240,2048
Mvva 72,164,192
)ASM"
R"ASM(
s_xor_b32 s50, s89, 1
s_lshl_b32 s50, s50, 17
s_lshl_b32 s51, s86, 6
s_xor_b32 s51, s51, 0
s_and_b32 s51, s51, 128
s_add_u32 s50, s50, s51
s_add_u32 s60, s24, s50
s_addc_u32 s61, s25, 0
s_add_u32 m0, s34, 0
s_nop 0
global_load_lds_dwordx4 v244, s[60:61] offset:0
Mvvv 72,168,252
Tv 148,240,4096
Tv 150,240,6144
Mvva 72,172,128
global_load_lds_dwordx4 v245, s[60:61] offset:1024
Mvva 76,160,248
Tv 152,240,8192
Tv 154,240,10240
Mvva 76,164,196
global_load_lds_dwordx4 v246, s[60:61] offset:2048
Mvvv 76,168,236
Tv 156,240,12288
Tv 158,240,14336
Mvva 76,172,132
global_load_lds_dwordx4 v247, s[60:61] offset:3072
s_waitcnt vmcnt(16)
s_waitcnt lgkmcnt(14)
Mvva 64,112,136
v_add_u32 v241, 65536, v241
Tv 160,241,0
Tv 162,241,2048
Tv 164,241,4096
Mvva 64,116,144
ds_read_b32 v5, v3 offset:0
s_add_u32 s44, s44, 1
Mvva 64,120,152
Tv 166,241,6144
Tv 168,241,8192
Tv 170,241,10240
Mvva 68,112,140
ds_write_b32 v2, v4 offset:0
v_add_u32 v4, 1, v4
Mvva 68,116,148
Tv 172,241,12288
Tv 174,241,14336
Mvva 68,120,156
s_waitcnt lgkmcnt(15)
s_waitcnt lgkmcnt(6)
v_cmp_gt_u32 vcc, s44, v5
s_cbranch_vccnz Lspin1704_%=
Lback1704_%=:
v_add_u32 v240, 4294901760, v240
Mvva 72,128,136
Tv 112,240,32768
Tv 114,240,34816
Mvva 72,132,144
Tv 116,240,36864
Tv 118,240,38912
Mvva 72,136,152
Tv 120,240,40960
Mvva 76,128,140
Tv 122,240,43008
Tv 124,240,45056
Mvva 76,132,148
Tv 126,240,47104
Mvva 76,136,156
v_add_u32 v241, 4294901760, v241
Zvva 48,144,212
Tv 128,241,32768
Tv 130,241,34816
Mvva 48,148,220
Tv 132,241,36864
Mvva 48,152,228
Tv 134,241,38912
Mvva 48,156,236
Tv 136,241,40960
Tv 138,241,43008
Zvva 52,144,216
Tv 140,241,45056
Mvva 52,148,224
s_xor_b32 s50, s86, 1
s_lshl_b32 s50, s50, 16
s_add_u32 s50, s50, 0
s_add_u32 s62, s82, s50
s_addc_u32 s63, s83, 0
CV 64,64,65
CV 65,66,67
CV 66,68,69
CV 67,70,71
global_store_dwordx4 v24, v[64:67], s[62:63] offset:2048 sc1
Mvva 52,152,232
Tv 142,241,47104
Mvva 52,156,240
s_waitcnt lgkmcnt(15)
CV 72,72,73
CV 73,74,75
CV 74,76,77
CV 75,78,79
global_store_dwordx4 v24, v[72:75], s[62:63] offset:3072 sc1
v_add_u32 v240, 65536, v240
Zvva 56,160,212
Tv 144,240,16384
Mvva 56,164,220
Tv 146,240,18432
Mvva 56,168,228
Tv 148,240,20480
Mvva 56,172,236
Zvva 60,160,216
Tv 150,240,22528
Mvva 60,164,224
Tv 152,240,24576
Mvva 60,168,232
Tv 154,240,26624
Mvva 60,172,240
s_mov_b32 s70, 0
s_and_b32 s50, s40, 28
s_lshl_b32 s72, 15, s50
s_andn2_b32 s50, s72, s70
s_cbranch_scc0 Lrdya7_%=
s_call_b64 s[76:77], Lwspin0_%=
Lrdya7_%=:
s_xor_b32 s50, s89, 0
s_lshl_b32 s50, s50, 12
s_add_u32 s62, s80, s50
s_addc_u32 s63, s81, 0
global_load_dwordx4 a[0:3], v24, s[62:63] offset:0 sc1
global_load_dwordx4 a[4:7], v25, s[62:63] offset:0 sc1
global_load_dwordx4 a[8:11], v24, s[62:63] offset:1024 sc1
global_load_dwordx4 a[12:15], v25, s[62:63] offset:1024 sc1
global_load_dwordx4 a[16:19], v24, s[62:63] offset:2048 sc1
global_load_dwordx4 a[20:23], v25, s[62:63] offset:2048 sc1
global_load_dwordx4 a[24:27], v24, s[62:63] offset:3072 sc1
global_load_dwordx4 a[28:31], v25, s[62:63] offset:3072 sc1
s_xor_b32 s50, s89, 1
s_lshl_b32 s50, s50, 12
s_add_u32 s62, s80, s50
s_addc_u32 s63, s81, 0
global_load_dwordx4 a[32:35], v24, s[62:63] offset:0 sc1
global_load_dwordx4 a[36:39], v25, s[62:63] offset:0 sc1
global_load_dwordx4 a[40:43], v24, s[62:63] offset:1024 sc1
global_load_dwordx4 a[44:47], v25, s[62:63] offset:1024 sc1
global_load_dwordx4 a[48:51], v24, s[62:63] offset:2048 sc1
global_load_dwordx4 a[52:55], v25, s[62:63] offset:2048 sc1
global_load_dwordx4 a[56:59], v24, s[62:63] offset:3072 sc1
global_load_dwordx4 a[60:63], v25, s[62:63] offset:3072 sc1
s_waitcnt vmcnt(30)
s_waitcnt lgkmcnt(14)
Mvva 48,112,244
v_add_u32 v241, 65536, v241
Tv 160,241,16384
Tv 162,241,18432
Mvva 48,116,192
ds_read_b32 v5, v3 offset:0
s_add_u32 s44, s44, 1
Mvvv 48,120,252
Tv 164,241,20480
Mvva 48,124,128
ds_write_b32 v2, v4 offset:0
v_add_u32 v4, 1, v4
Mvva 52,112,248
Tv 166,241,22528
Mvva 52,116,196
Tv 168,241,24576
Mvvv 52,120,236
Tv 170,241,26624
Mvva 52,124,132
s_waitcnt lgkmcnt(14)
s_waitcnt lgkmcnt(5)
v_cmp_gt_u32 vcc, s44, v5
s_cbranch_vccnz Lspin1705_%=
Lback1705_%=:
v_add_u32 v240, 4294901760, v240
Mvva 56,128,244
Tv 112,240,49152
Tv 114,240,51200
Mvva 56,132,192
s_xor_b32 s50, s89, 2
s_lshl_b32 s50, s50, 17
s_lshl_b32 s51, s86, 6
s_xor_b32 s51, s51, 128
s_and_b32 s51, s51, 128
s_add_u32 s50, s50, s51
s_add_u32 s60, s24, s50
s_addc_u32 s61, s25, 0
s_add_u32 m0, s34, 16384
s_nop 0
global_load_lds_dwordx4 v244, s[60:61] offset:0
Mvvv 56,136,252
Tv 116,240,53248
Tv 118,240,55296
Mvva 56,140,128
global_load_lds_dwordx4 v245, s[60:61] offset:1024
Mvva 60,128,248
Tv 120,240,57344
Tv 122,240,59392
Mvva 60,132,196
global_load_lds_dwordx4 v246, s[60:61] offset:2048
Mvvv 60,136,236
Tv 124,240,61440
)ASM"
R"ASM(
Tv 126,240,63488
Mvva 60,140,132
v_add_u32 v241, 4294901760, v241
Mvva 48,144,136
Tv 128,241,49152
Tv 130,241,51200
Mvva 48,148,144
global_load_lds_dwordx4 v247, s[60:61] offset:3072
Mvva 48,152,152
Tv 132,241,53248
Tv 134,241,55296
Mvva 52,144,140
Tv 136,241,57344
Tv 138,241,59392
Mvva 52,148,148
Tv 140,241,61440
Tv 142,241,63488
Mvva 52,152,156
s_waitcnt lgkmcnt(15)
v_add_u32 v242, 4294901760, v242
Mvva 56,160,136
Tv 144,242,49152
Tv 146,242,51200
Mvva 56,164,144
Tv 148,242,53248
Mvva 56,168,152
Tv 150,242,55296
Mvva 60,160,140
Tv 152,242,57344
Tv 154,242,59392
Mvva 60,164,148
Tv 156,242,61440
Mvva 60,168,156
Tv 158,242,63488
s_waitcnt vmcnt(12)
s_waitcnt lgkmcnt(15)
Zvva 80,112,0
v_add_u32 v243, 4294901760, v243
Tv 160,243,49152
Tv 162,243,51200
Mvva 80,116,8
ds_read_b32 v5, v3 offset:0
s_add_u32 s44, s44, 1
Mvva 80,120,16
Tv 164,243,53248
Tv 166,243,55296
Mvva 80,124,24
ds_write_b32 v2, v4 offset:0
v_add_u32 v4, 1, v4
Zvva 84,112,4
Tv 168,243,57344
v_mov_b32 v32, v48
v_mov_b32 v33, v49
Mvva 84,116,12
Tv 170,243,59392
Tv 172,243,61440
Mvva 84,120,20
Tv 174,243,63488
v_mov_b32 v34, v50
v_mov_b32 v35, v51
Mvva 84,124,28
s_waitcnt lgkmcnt(15)
s_waitcnt lgkmcnt(7)
v_cmp_gt_u32 vcc, s44, v5
s_cbranch_vccnz Lspin1706_%=
Lback1706_%=:
v_add_u32 v240, 65536, v240
v_mov_b32 v36, v52
v_mov_b32 v37, v53
Zvva 88,128,0
Tv 112,240,32768
Tv 114,240,34816
Mvva 88,132,8
s_xor_b32 s50, s89, 2
s_lshl_b32 s50, s50, 17
s_lshl_b32 s51, s86, 6
s_xor_b32 s51, s51, 0
s_and_b32 s51, s51, 128
s_add_u32 s50, s50, s51
s_add_u32 s60, s24, s50
s_addc_u32 s61, s25, 0
s_add_u32 m0, s34, 65536
s_nop 0
global_load_lds_dwordx4 v244, s[60:61] offset:0
Mvva 88,136,16
Tv 116,240,36864
Tv 118,240,38912
Mvva 88,140,24
global_load_lds_dwordx4 v245, s[60:61] offset:1024
Zvva 92,128,4
Tv 120,240,40960
Tv 122,240,43008
Mvva 92,132,12
global_load_lds_dwordx4 v246, s[60:61] offset:2048
Mvva 92,136,20
Tv 124,240,45056
Tv 126,240,47104
Mvva 92,140,28
v_add_u32 v241, 65536, v241
v_mov_b32 v38, v54
v_mov_b32 v39, v55
Zvva 96,144,0
Tv 128,241,32768
Tv 130,241,34816
Mvva 96,148,8
global_load_lds_dwordx4 v247, s[60:61] offset:3072
Mvva 96,152,16
Tv 132,241,36864
Tv 134,241,38912
Mvva 96,156,24
s_xor_b32 s50, s89, 2
s_lshl_b32 s50, s50, 12
s_add_u32 s62, s80, s50
s_addc_u32 s63, s81, 0
global_load_dwordx4 a[64:67], v24, s[62:63] offset:0 sc1
Zvva 100,144,4
Tv 136,241,40960
Tv 138,241,43008
Mvva 100,148,12
global_load_dwordx4 a[68:71], v25, s[62:63] offset:0 sc1
Mvva 100,152,20
Tv 140,241,45056
Tv 142,241,47104
Mvva 100,156,28
s_waitcnt lgkmcnt(15)
v_add_u32 v242, 65536, v242
v_mov_b32 v40, v56
v_mov_b32 v41, v57
Zvva 104,160,0
Tv 144,242,32768
Tv 146,242,34816
Mvva 104,164,8
global_load_dwordx4 a[72:75], v24, s[62:63] offset:1024 sc1
Mvva 104,168,16
Tv 148,242,36864
v_mov_b32 v42, v58
v_mov_b32 v43, v59
Mvva 104,172,24
global_load_dwordx4 a[76:79], v25, s[62:63] offset:1024 sc1
Zvva 108,160,4
Tv 150,242,38912
v_mov_b32 v44, v60
v_mov_b32 v45, v61
Mvva 108,164,12
Tv 152,242,40960
Tv 154,242,43008
Mvva 108,168,20
Tv 156,242,45056
v_mov_b32 v46, v62
v_mov_b32 v47, v63
Mvva 108,172,28
Tv 158,242,47104
s_waitcnt lgkmcnt(15)
Zvva 48,112,0
v_add_u32 v243, 65536, v243
Tv 160,243,32768
Tv 162,243,34816
Mvva 48,116,8
ds_read_b32 v5, v3 offset:0
s_add_u32 s44, s44, 1
Mvva 48,120,16
Tv 164,243,36864
Tv 166,243,38912
Mvva 48,124,24
ds_write_b32 v2, v4 offset:0
v_add_u32 v4, 1, v4
Zvva 52,112,4
Tv 168,243,40960
Mvva 52,116,12
Tv 170,243,43008
Tv 172,243,45056
Mvva 52,120,20
Tv 174,243,47104
Mvva 52,124,28
s_waitcnt lgkmcnt(15)
s_waitcnt lgkmcnt(7)
v_cmp_gt_u32 vcc, s44, v5
s_cbranch_vccnz Lspin1707_%=
Lback1707_%=:
Zvva 56,128,0
Tv 112,240,49152
Tv 114,240,51200
Mvva 56,132,8
global_load_dwordx4 a[80:83], v24, s[62:63] offset:2048 sc1
Mvva 56,136,16
Tv 116,240,53248
Tv 118,240,55296
Mvva 56,140,24
global_load_dwordx4 a[84:87], v25, s[62:63] offset:2048 sc1
Zvva 60,128,4
Tv 120,240,57344
Tv 122,240,59392
Mvva 60,132,12
global_load_dwordx4 a[88:91], v24, s[62:63] offset:3072 sc1
Mvva 60,136,20
Tv 124,240,61440
Tv 126,240,63488
Mvva 60,140,28
Zvva 64,144,0
Tv 128,241,49152
)ASM"
R"ASM(
Tv 130,241,51200
Mvva 64,148,8
global_load_dwordx4 a[92:95], v25, s[62:63] offset:3072 sc1
Mvva 64,152,16
Tv 132,241,53248
Tv 134,241,55296
Mvva 64,156,24
s_xor_b32 s50, s89, 3
s_lshl_b32 s50, s50, 17
s_lshl_b32 s51, s86, 6
s_xor_b32 s51, s51, 128
s_and_b32 s51, s51, 128
s_add_u32 s50, s50, s51
s_add_u32 s60, s24, s50
s_addc_u32 s61, s25, 0
s_add_u32 m0, s34, 32768
s_nop 0
global_load_lds_dwordx4 v244, s[60:61] offset:0
Zvva 68,144,4
Tv 136,241,57344
Tv 138,241,59392
Mvva 68,148,12
global_load_lds_dwordx4 v245, s[60:61] offset:1024
Mvva 68,152,20
Tv 140,241,61440
Tv 142,241,63488
Mvva 68,156,28
s_waitcnt lgkmcnt(15)
Zvva 72,160,0
Tv 144,242,49152
Tv 146,242,51200
Mvva 72,164,8
global_load_lds_dwordx4 v246, s[60:61] offset:2048
Mvva 72,168,16
Tv 148,242,53248
Mvva 72,172,24
global_load_lds_dwordx4 v247, s[60:61] offset:3072
Zvva 76,160,4
Tv 150,242,55296
Mvva 76,164,12
Tv 152,242,57344
Tv 154,242,59392
Mvva 76,168,20
Tv 156,242,61440
Mvva 76,172,28
Tv 158,242,63488
s_waitcnt vmcnt(16)
s_waitcnt lgkmcnt(15)
Mvva 80,112,32
Tv 160,243,49152
Tv 162,243,51200
Mvva 80,116,40
ds_read_b32 v5, v3 offset:0
s_add_u32 s44, s44, 1
Mvva 80,120,48
Tv 164,243,53248
Tv 166,243,55296
Mvva 80,124,56
ds_write_b32 v2, v4 offset:0
v_add_u32 v4, 1, v4
Mvva 84,112,36
Tv 168,243,57344
Mvva 84,116,44
Tv 170,243,59392
Tv 172,243,61440
Mvva 84,120,52
Tv 174,243,63488
Mvva 84,124,60
s_waitcnt lgkmcnt(15)
s_waitcnt lgkmcnt(7)
v_cmp_gt_u32 vcc, s44, v5
s_cbranch_vccnz Lspin1708_%=
Lback1708_%=:
v_add_u32 v240, 4294901760, v240
Mvva 88,128,32
Tv 112,240,0
Tv 114,240,2048
Mvva 88,132,40
s_and_b32 s50, s40, 28
s_xor_b32 s50, s50, 16
s_lshl_b64 exec, 15, s50
global_load_dword v221, v220, s[28:29] sc1
s_mov_b64 exec, -1
Mvva 88,136,48
Tv 116,240,4096
Tv 118,240,6144
Mvva 88,140,56
s_xor_b32 s50, s89, 3
s_lshl_b32 s50, s50, 17
s_lshl_b32 s51, s86, 6
s_xor_b32 s51, s51, 0
s_and_b32 s51, s51, 128
s_add_u32 s50, s50, s51
s_add_u32 s60, s24, s50
s_addc_u32 s61, s25, 0
s_add_u32 m0, s34, 81920
s_nop 0
global_load_lds_dwordx4 v244, s[60:61] offset:0
Mvva 92,128,36
Tv 120,240,8192
Tv 122,240,10240
Mvva 92,132,44
global_load_lds_dwordx4 v245, s[60:61] offset:1024
Mvva 92,136,52
Tv 124,240,12288
Tv 126,240,14336
Mvva 92,140,60
v_add_u32 v241, 4294901760, v241
Mvva 96,144,32
Tv 128,241,0
Tv 130,241,2048
Mvva 96,148,40
global_load_lds_dwordx4 v246, s[60:61] offset:2048
Mvva 96,152,48
Tv 132,241,4096
Tv 134,241,6144
Mvva 96,156,56
global_load_lds_dwordx4 v247, s[60:61] offset:3072
Mvva 100,144,36
Tv 136,241,8192
Tv 138,241,10240
Mvva 100,148,44
s_xor_b32 s50, s89, 3
s_lshl_b32 s50, s50, 12
s_add_u32 s62, s80, s50
s_addc_u32 s63, s81, 0
global_load_dwordx4 a[96:99], v24, s[62:63] offset:0 sc1
Mvva 100,152,52
Tv 140,241,12288
Tv 142,241,14336
Mvva 100,156,60
s_waitcnt lgkmcnt(15)
v_add_u32 v242, 4294901760, v242
Mvva 104,160,32
Tv 144,242,0
Tv 146,242,2048
Mvva 104,164,40
global_load_dwordx4 a[100:103], v25, s[62:63] offset:0 sc1
Mvva 104,168,48
Tv 148,242,4096
Tv 150,242,6144
Mvva 104,172,56
global_load_dwordx4 a[104:107], v24, s[62:63] offset:1024 sc1
Mvva 108,160,36
Tv 152,242,8192
Mvva 108,164,44
global_load_dwordx4 a[108:111], v25, s[62:63] offset:1024 sc1
Mvva 108,168,52
Tv 154,242,10240
Tv 156,242,12288
Mvva 108,172,60
Tv 158,242,14336
s_waitcnt vmcnt(21)
s_waitcnt lgkmcnt(15)
Mvva 48,112,32
v_add_u32 v243, 4294901760, v243
Tv 160,243,0
Tv 162,243,2048
Mvva 48,116,40
ds_read_b32 v5, v3 offset:0
s_add_u32 s44, s44, 1
Mvva 48,120,48
Tv 164,243,4096
Tv 166,243,6144
Mvva 48,124,56
ds_write_b32 v2, v4 offset:0
v_add_u32 v4, 1, v4
Mvva 52,112,36
Tv 168,243,8192
Mvva 52,116,44
Tv 170,243,10240
Tv 172,243,12288
Mvva 52,120,52
Tv 174,243,14336
Mvva 52,124,60
s_waitcnt lgkmcnt(15)
s_waitcnt lgkmcnt(7)
v_cmp_gt_u32 vcc, s44, v5
s_cbranch_vccnz Lspin1709_%=
Lback1709_%=:
Mvva 56,128,32
Tv 112,240,16384
Tv 114,240,18432
Mvva 56,132,40
global_load_dwordx4 a[112:115], v24, s[62:63] offset:2048 sc1
Mvva 56,136,48
Tv 116,240,20480
Tv 118,240,22528
Mvva 56,140,56
global_load_dwordx4 a[116:119], v25, s[62:63] offset:2048 sc1
Mvva 60,128,36
Tv 120,240,24576
Tv 122,240,26624
Mvva 60,132,44
global_load_dwordx4 a[120:123], v24, s[62:63] offset:3072 sc1
Mvva 60,136,52
Tv 124,240,28672
Tv 126,240,30720
Mvva 60,140,60
Mvva 64,144,32
)ASM"
R"ASM(
Tv 128,241,16384
Tv 130,241,18432
Mvva 64,148,40
global_load_dwordx4 a[124:127], v25, s[62:63] offset:3072 sc1
Mvva 64,152,48
Tv 132,241,20480
Tv 134,241,22528
Mvva 64,156,56
s_xor_b32 s50, s89, 16
s_lshl_b32 s50, s50, 17
s_lshl_b32 s51, s86, 6
s_xor_b32 s51, s51, 128
s_and_b32 s51, s51, 128
s_add_u32 s50, s50, s51
s_add_u32 s60, s24, s50
s_addc_u32 s61, s25, 0
s_add_u32 m0, s34, 49152
s_nop 0
global_load_lds_dwordx4 v244, s[60:61] offset:0
Mvva 68,144,36
Tv 136,241,24576
Tv 138,241,26624
Mvva 68,148,44
global_load_lds_dwordx4 v245, s[60:61] offset:1024
Mvva 68,152,52
Tv 140,241,28672
Tv 142,241,30720
Mvva 68,156,60
s_waitcnt lgkmcnt(15)
Mvva 72,160,32
Tv 144,242,16384
Tv 146,242,18432
Mvva 72,164,40
global_load_lds_dwordx4 v246, s[60:61] offset:2048
Mvva 72,168,48
Tv 148,242,20480
Mvva 72,172,56
global_load_lds_dwordx4 v247, s[60:61] offset:3072
Mvva 76,160,36
Tv 150,242,22528
Mvva 76,164,44
Tv 152,242,24576
Tv 154,242,26624
Mvva 76,168,52
Tv 156,242,28672
Mvva 76,172,60
Tv 158,242,30720
s_waitcnt vmcnt(16)
s_waitcnt lgkmcnt(15)
Mvva 80,112,64
Tv 160,243,16384
Tv 162,243,18432
Mvva 80,116,72
ds_read_b32 v5, v3 offset:0
s_add_u32 s44, s44, 1
Mvva 80,120,80
Tv 164,243,20480
Tv 166,243,22528
Mvva 80,124,88
ds_write_b32 v2, v4 offset:0
v_add_u32 v4, 1, v4
Mvva 84,112,68
Tv 168,243,24576
Tv 170,243,26624
Mvva 84,116,76
v_cmp_eq_u32 s[70:71], s47, v221
s_and_b32 s50, s40, 28
s_xor_b32 s50, s50, 16
s_lshl_b32 s72, 15, s50
s_andn2_b32 s50, s72, s70
s_cbranch_scc0 Lrdyb7_10_%=
s_call_b64 s[76:77], Lwait_%=
Lrdyb7_10_%=:
Mvva 84,120,84
Tv 172,243,28672
Tv 174,243,30720
Mvva 84,124,92
s_waitcnt lgkmcnt(15)
s_waitcnt lgkmcnt(7)
v_cmp_gt_u32 vcc, s44, v5
s_cbranch_vccnz Lspin1710_%=
Lback1710_%=:
v_add_u32 v240, 65536, v240
Mvva 88,128,64
Tv 112,240,0
Tv 114,240,2048
Mvva 88,132,72
s_xor_b32 s50, s89, 16
s_lshl_b32 s50, s50, 17
s_lshl_b32 s51, s86, 6
s_xor_b32 s51, s51, 0
s_and_b32 s51, s51, 128
s_add_u32 s50, s50, s51
s_add_u32 s60, s24, s50
s_addc_u32 s61, s25, 0
s_add_u32 m0, s34, 98304
s_nop 0
global_load_lds_dwordx4 v244, s[60:61] offset:0
Mvva 88,136,80
Tv 116,240,4096
Tv 118,240,6144
Mvva 88,140,88
global_load_lds_dwordx4 v245, s[60:61] offset:1024
Mvva 92,128,68
Tv 120,240,8192
Tv 122,240,10240
Mvva 92,132,76
global_load_lds_dwordx4 v246, s[60:61] offset:2048
Mvva 92,136,84
Tv 124,240,12288
Tv 126,240,14336
Mvva 92,140,92
v_add_u32 v241, 65536, v241
Mvva 96,144,64
Tv 128,241,0
Tv 130,241,2048
Mvva 96,148,72
global_load_lds_dwordx4 v247, s[60:61] offset:3072
Mvva 96,152,80
Tv 132,241,4096
Tv 134,241,6144
Mvva 96,156,88
s_xor_b32 s50, s89, 16
s_lshl_b32 s50, s50, 12
s_add_u32 s62, s80, s50
s_addc_u32 s63, s81, 0
global_load_dwordx4 a[128:131], v24, s[62:63] offset:0 sc1
Mvva 100,144,68
Tv 136,241,8192
Tv 138,241,10240
Mvva 100,148,76
global_load_dwordx4 a[132:135], v25, s[62:63] offset:0 sc1
Mvva 100,152,84
Tv 140,241,12288
Tv 142,241,14336
Mvva 100,156,92
s_waitcnt lgkmcnt(15)
v_add_u32 v242, 65536, v242
Mvva 104,160,64
Tv 144,242,0
Tv 146,242,2048
Mvva 104,164,72
global_load_dwordx4 a[136:139], v24, s[62:63] offset:1024 sc1
Mvva 104,168,80
Tv 148,242,4096
Mvva 104,172,88
global_load_dwordx4 a[140:143], v25, s[62:63] offset:1024 sc1
Mvva 108,160,68
Tv 150,242,6144
Mvva 108,164,76
Tv 152,242,8192
Tv 154,242,10240
Mvva 108,168,84
Tv 156,242,12288
Mvva 108,172,92
Tv 158,242,14336
s_waitcnt vmcnt(20)
s_waitcnt lgkmcnt(15)
Mvva 48,112,64
v_add_u32 v243, 65536, v243
Tv 160,243,0
Tv 162,243,2048
Mvva 48,116,72
ds_read_b32 v5, v3 offset:0
s_add_u32 s44, s44, 1
Mvva 48,120,80
Tv 164,243,4096
Tv 166,243,6144
Mvva 48,124,88
ds_write_b32 v2, v4 offset:0
v_add_u32 v4, 1, v4
Mvva 52,112,68
Tv 168,243,8192
Mvva 52,116,76
Tv 170,243,10240
Tv 172,243,12288
Mvva 52,120,84
Tv 174,243,14336
Mvva 52,124,92
s_waitcnt lgkmcnt(15)
s_waitcnt lgkmcnt(7)
v_cmp_gt_u32 vcc, s44, v5
s_cbranch_vccnz Lspin1711_%=
Lback1711_%=:
v_add_u32 v240, 4294901760, v240
Mvva 56,128,64
Tv 112,240,32768
Tv 114,240,34816
Mvva 56,132,72
global_load_dwordx4 a[144:147], v24, s[62:63] offset:2048 sc1
Mvva 56,136,80
Tv 116,240,36864
Tv 118,240,38912
Mvva 56,140,88
global_load_dwordx4 a[148:151], v25, s[62:63] offset:2048 sc1
Mvva 60,128,68
Tv 120,240,40960
Tv 122,240,43008
Mvva 60,132,76
global_load_dwordx4 a[152:155], v24, s[62:63] offset:3072 sc1
)ASM"
R"ASM(
Mvva 60,136,84
Tv 124,240,45056
Tv 126,240,47104
Mvva 60,140,92
global_load_dwordx4 a[156:159], v25, s[62:63] offset:3072 sc1
v_add_u32 v241, 4294901760, v241
Mvva 64,144,64
Tv 128,241,32768
Tv 130,241,34816
Mvva 64,148,72
s_xor_b32 s50, s89, 17
s_lshl_b32 s50, s50, 17
s_lshl_b32 s51, s86, 6
s_xor_b32 s51, s51, 128
s_and_b32 s51, s51, 128
s_add_u32 s50, s50, s51
s_add_u32 s60, s24, s50
s_addc_u32 s61, s25, 0
s_add_u32 m0, s34, 114688
s_nop 0
global_load_lds_dwordx4 v244, s[60:61] offset:0
Mvva 64,152,80
Tv 132,241,36864
Tv 134,241,38912
Mvva 64,156,88
global_load_lds_dwordx4 v245, s[60:61] offset:1024
Mvva 68,144,68
Tv 136,241,40960
Tv 138,241,43008
Mvva 68,148,76
global_load_lds_dwordx4 v246, s[60:61] offset:2048
Mvva 68,152,84
Tv 140,241,45056
Tv 142,241,47104
Mvva 68,156,92
s_waitcnt lgkmcnt(15)
global_load_lds_dwordx4 v247, s[60:61] offset:3072
v_add_u32 v242, 4294901760, v242
Mvva 72,160,64
Tv 144,242,32768
Tv 146,242,34816
Mvva 72,164,72
s_lshl_b32 s50, s86, 16
s_add_u32 s50, s50, 0
s_add_u32 s64, s82, s50
s_addc_u32 s65, s83, 0
global_load_dwordx4 v[176:179], v24, s[64:65] offset:2048 sc1
Mvva 72,168,80
Tv 148,242,36864
Tv 150,242,38912
Mvva 72,172,88
global_load_dwordx4 v[180:183], v24, s[64:65] offset:3072 sc1
Mvva 76,160,68
Tv 152,242,40960
Tv 154,242,43008
Mvva 76,164,76
s_lshl_b32 s50, s86, 16
s_add_u32 s50, s50, 4096
s_add_u32 s64, s82, s50
s_addc_u32 s65, s83, 0
global_load_dwordx4 v[184:187], v24, s[64:65] offset:2048 sc1
Mvva 76,168,84
Tv 156,242,45056
Tv 158,242,47104
Mvva 76,172,92
global_load_dwordx4 v[188:191], v24, s[64:65] offset:3072 sc1
s_waitcnt vmcnt(20)
s_waitcnt lgkmcnt(15)
Mvva 80,112,96
v_add_u32 v243, 4294901760, v243
Tv 160,243,32768
Tv 162,243,34816
Mvva 80,116,104
ds_read_b32 v5, v3 offset:0
s_add_u32 s44, s44, 1
Mvva 80,120,112
Tv 164,243,36864
Tv 166,243,38912
Mvva 80,124,120
ds_write_b32 v2, v4 offset:0
v_add_u32 v4, 1, v4
Mvva 84,112,100
Tv 168,243,40960
Mvva 84,116,108
Tv 170,243,43008
Tv 172,243,45056
Mvva 84,120,116
Tv 174,243,47104
Mvva 84,124,124
s_waitcnt lgkmcnt(15)
s_waitcnt lgkmcnt(7)
v_cmp_gt_u32 vcc, s44, v5
s_cbranch_vccnz Lspin1712_%=
Lback1712_%=:
v_add_u32 v240, 65536, v240
Mvva 88,128,96
Tv 112,240,16384
Tv 114,240,18432
Mvva 88,132,104
s_xor_b32 s50, s89, 18
s_lshl_b32 s50, s50, 17
s_lshl_b32 s51, s86, 6
s_xor_b32 s51, s51, 128
s_and_b32 s51, s51, 128
s_add_u32 s50, s50, s51
s_add_u32 s60, s24, s50
s_addc_u32 s61, s25, 0
s_add_u32 m0, s34, 0
s_nop 0
global_load_lds_dwordx4 v244, s[60:61] offset:0
Mvva 88,136,112
Tv 116,240,20480
Tv 118,240,22528
Mvva 88,140,120
global_load_lds_dwordx4 v245, s[60:61] offset:1024
Mvva 92,128,100
Tv 120,240,24576
Tv 122,240,26624
Mvva 92,132,108
global_load_lds_dwordx4 v246, s[60:61] offset:2048
Mvva 92,136,116
Tv 124,240,28672
Tv 126,240,30720
Mvva 92,140,124
v_add_u32 v241, 65536, v241
Mvva 96,144,96
Tv 128,241,16384
Tv 130,241,18432
Mvva 96,148,104
global_load_lds_dwordx4 v247, s[60:61] offset:3072
Mvva 96,152,112
Tv 132,241,20480
Tv 134,241,22528
Mvva 96,156,120
s_xor_b32 s50, s89, 17
s_lshl_b32 s50, s50, 12
s_add_u32 s62, s80, s50
s_addc_u32 s63, s81, 0
global_load_dwordx4 a[160:163], v24, s[62:63] offset:0 sc1
Mvva 100,144,100
Tv 136,241,24576
Tv 138,241,26624
Mvva 100,148,108
global_load_dwordx4 a[164:167], v25, s[62:63] offset:0 sc1
Mvva 100,152,116
Tv 140,241,28672
Tv 142,241,30720
Mvva 100,156,124
s_waitcnt lgkmcnt(15)
v_add_u32 v242, 65536, v242
Mvva 104,160,96
Tv 144,242,16384
Tv 146,242,18432
Mvva 104,164,104
global_load_dwordx4 a[168:171], v24, s[62:63] offset:1024 sc1
Mvva 104,168,112
Tv 148,242,20480
Tv 150,242,22528
Mvva 104,172,120
global_load_dwordx4 a[172:175], v25, s[62:63] offset:1024 sc1
Mvva 108,160,100
Tv 152,242,24576
Tv 154,242,26624
Mvva 108,164,108
s_lshl_b32 s50, s86, 16
s_add_u32 s50, s50, 8192
s_add_u32 s64, s82, s50
s_addc_u32 s65, s83, 0
global_load_dwordx4 v[192:195], v24, s[64:65] offset:2048 sc1
Mvva 108,168,116
Tv 156,242,28672
Tv 158,242,30720
Mvva 108,172,124
global_load_dwordx4 v[196:199], v24, s[64:65] offset:3072 sc1
s_waitcnt vmcnt(26)
s_waitcnt lgkmcnt(15)
Mvva 48,112,96
v_add_u32 v243, 65536, v243
Tv 160,243,16384
Tv 162,243,18432
Mvva 48,116,104
ds_read_b32 v5, v3 offset:0
s_add_u32 s44, s44, 1
Mvva 48,120,112
Tv 164,243,20480
Tv 166,243,22528
Mvva 48,124,120
ds_write_b32 v2, v4 offset:0
v_add_u32 v4, 1, v4
Mvva 52,112,100
Tv 168,243,24576
Mvva 52,116,108
Tv 170,243,26624
Tv 172,243,28672
Mvva 52,120,116
Tv 174,243,30720
Mvva 52,124,124
s_waitcnt lgkmcnt(15)
s_waitcnt lgkmcnt(7)
v_cmp_gt_u32 vcc, s44, v5
)ASM"
R"ASM(
s_cbranch_vccnz Lspin1713_%=
Lback1713_%=:
v_add_u32 v240, 4294901760, v240
Mvva 56,128,96
Tv 112,240,49152
Tv 114,240,51200
Mvva 56,132,104
global_load_dwordx4 a[176:179], v24, s[62:63] offset:2048 sc1
Mvva 56,136,112
Tv 116,240,53248
Tv 118,240,55296
Mvva 56,140,120
global_load_dwordx4 a[180:183], v25, s[62:63] offset:2048 sc1
Mvva 60,128,100
Tv 120,240,57344
Tv 122,240,59392
Mvva 60,132,108
global_load_dwordx4 a[184:187], v24, s[62:63] offset:3072 sc1
Mvva 60,136,116
Tv 124,240,61440
Tv 126,240,63488
Mvva 60,140,124
v_add_u32 v241, 4294901760, v241
Mvva 64,144,96
Tv 128,241,49152
Tv 130,241,51200
Mvva 64,148,104
global_load_dwordx4 a[188:191], v25, s[62:63] offset:3072 sc1
Mvva 64,152,112
Tv 132,241,53248
Tv 134,241,55296
Mvva 64,156,120
s_xor_b32 s50, s89, 17
s_lshl_b32 s50, s50, 17
s_lshl_b32 s51, s86, 6
s_xor_b32 s51, s51, 0
s_and_b32 s51, s51, 128
s_add_u32 s50, s50, s51
s_add_u32 s60, s24, s50
s_addc_u32 s61, s25, 0
s_add_u32 m0, s34, 16384
s_nop 0
global_load_lds_dwordx4 v244, s[60:61] offset:0
Mvva 68,144,100
Tv 136,241,57344
Tv 138,241,59392
Mvva 68,148,108
global_load_lds_dwordx4 v245, s[60:61] offset:1024
Mvva 68,152,116
Tv 140,241,61440
Tv 142,241,63488
Mvva 68,156,124
s_waitcnt lgkmcnt(15)
v_add_u32 v242, 4294901760, v242
Mvva 72,160,96
Tv 144,242,49152
Tv 146,242,51200
Mvva 72,164,104
global_load_lds_dwordx4 v246, s[60:61] offset:2048
Mvva 72,168,112
Tv 148,242,53248
Mvva 72,172,120
global_load_lds_dwordx4 v247, s[60:61] offset:3072
Mvva 76,160,100
Tv 150,242,55296
Mvva 76,164,108
Tv 152,242,57344
Tv 154,242,59392
Mvva 76,168,116
Tv 156,242,61440
Mvva 76,172,124
Tv 158,242,63488
s_waitcnt vmcnt(8)
s_waitcnt lgkmcnt(15)
Mvva 80,112,128
v_add_u32 v243, 4294901760, v243
Tv 160,243,49152
Tv 162,243,51200
Mvva 80,116,136
ds_read_b32 v5, v3 offset:0
s_add_u32 s44, s44, 1
v_max3_u32 v216, v176, v177, v178
v_max3_u32 v217, v182, v183, v184
Mvva 80,120,144
Tv 164,243,53248
Tv 166,243,55296
Mvva 80,124,152
ds_write_b32 v2, v4 offset:0
v_add_u32 v4, 1, v4
v_max3_u32 v218, v188, v189, v190
v_max3_u32 v219, v194, v195, v196
Mvva 84,112,132
Tv 168,243,57344
v_max3_u32 v216, v216, v179, v180
Mvva 84,116,140
Tv 170,243,59392
Tv 172,243,61440
Mvva 84,120,148
Tv 174,243,63488
v_max3_u32 v217, v217, v185, v186
Mvva 84,124,156
s_waitcnt lgkmcnt(15)
s_waitcnt lgkmcnt(7)
v_cmp_gt_u32 vcc, s44, v5
s_cbranch_vccnz Lspin1714_%=
Lback1714_%=:
v_add_u32 v240, 65536, v240
v_max3_u32 v218, v218, v191, v192
v_max3_u32 v219, v219, v197, v198
Mvva 88,128,128
Tv 112,240,32768
Tv 114,240,34816
Mvva 88,132,136
s_xor_b32 s50, s89, 18
s_lshl_b32 s50, s50, 17
s_lshl_b32 s51, s86, 6
s_xor_b32 s51, s51, 0
s_and_b32 s51, s51, 128
s_add_u32 s50, s50, s51
s_add_u32 s60, s24, s50
s_addc_u32 s61, s25, 0
s_add_u32 m0, s34, 65536
s_nop 0
global_load_lds_dwordx4 v244, s[60:61] offset:0
Mvva 88,136,144
Tv 116,240,36864
Tv 118,240,38912
Mvva 88,140,152
global_load_lds_dwordx4 v245, s[60:61] offset:1024
Mvva 92,128,132
Tv 120,240,40960
Tv 122,240,43008
Mvva 92,132,140
global_load_lds_dwordx4 v246, s[60:61] offset:2048
Mvva 92,136,148
Tv 124,240,45056
Tv 126,240,47104
Mvva 92,140,156
v_add_u32 v241, 65536, v241
v_max_u32 v216, v216, v181
v_max_u32 v217, v217, v187
Mvva 96,144,128
Tv 128,241,32768
Tv 130,241,34816
Mvva 96,148,136
global_load_lds_dwordx4 v247, s[60:61] offset:3072
Mvva 96,152,144
Tv 132,241,36864
Tv 134,241,38912
Mvva 96,156,152
s_xor_b32 s50, s89, 18
s_lshl_b32 s50, s50, 12
s_add_u32 s62, s80, s50
s_addc_u32 s63, s81, 0
global_load_dwordx4 a[192:195], v24, s[62:63] offset:0 sc1
Mvva 100,144,132
Tv 136,241,40960
Tv 138,241,43008
Mvva 100,148,140
global_load_dwordx4 a[196:199], v25, s[62:63] offset:0 sc1
Mvva 100,152,148
Tv 140,241,45056
Tv 142,241,47104
Mvva 100,156,156
s_waitcnt lgkmcnt(15)
v_add_u32 v242, 65536, v242
v_max_u32 v218, v218, v193
v_max_u32 v219, v219, v199
Mvva 104,160,128
Tv 144,242,32768
Tv 146,242,34816
Mvva 104,164,136
global_load_dwordx4 a[200:203], v24, s[62:63] offset:1024 sc1
Mvva 104,168,144
Tv 148,242,36864
v_max3_u32 v216, v216, v217, v218
Mvva 104,172,152
global_load_dwordx4 a[204:207], v25, s[62:63] offset:1024 sc1
Mvva 108,160,132
Tv 150,242,38912
v_max_u32 v216, v216, v219
Mvva 108,164,140
Tv 152,242,40960
Tv 154,242,43008
Mvva 108,168,148
Tv 156,242,45056
v_cmp_eq_u32 vcc, -1, v216
s_cbranch_vccnz Lbw7_%=
Lbok7_%=:
Mvva 108,172,156
Tv 158,242,47104
v_lshlrev_b32 v216, 16, v176
s_waitcnt lgkmcnt(15)
Mvva 48,112,128
v_add_u32 v243, 65536, v243
Tv 160,243,32768
Tv 162,243,34816
Mvva 48,116,136
ds_read_b32 v5, v3 offset:0
s_add_u32 s44, s44, 1
)ASM"
R"ASM(
v_and_b32 v217, 0xffff0000, v176
v_add_f32 v32, v32, v216
Mvva 48,120,144
Tv 164,243,36864
Tv 166,243,38912
Mvva 48,124,152
ds_write_b32 v2, v4 offset:0
v_add_u32 v4, 1, v4
v_add_f32 v33, v33, v217
v_lshlrev_b32 v218, 16, v177
Mvva 52,112,132
Tv 168,243,40960
v_and_b32 v219, 0xffff0000, v177
Mvva 52,116,140
Tv 170,243,43008
Tv 172,243,45056
Mvva 52,120,148
Tv 174,243,47104
v_add_f32 v34, v34, v218
Mvva 52,124,156
s_waitcnt lgkmcnt(15)
s_waitcnt lgkmcnt(7)
v_cmp_gt_u32 vcc, s44, v5
s_cbranch_vccnz Lspin1715_%=
Lback1715_%=:
v_add_f32 v35, v35, v219
v_lshlrev_b32 v216, 16, v178
Mvva 56,128,128
Tv 112,240,49152
Tv 114,240,51200
Mvva 56,132,136
global_load_dwordx4 a[208:211], v24, s[62:63] offset:2048 sc1
Mvva 56,136,144
Tv 116,240,53248
Tv 118,240,55296
Mvva 56,140,152
global_load_dwordx4 a[212:215], v25, s[62:63] offset:2048 sc1
Mvva 60,128,132
Tv 120,240,57344
Tv 122,240,59392
Mvva 60,132,140
global_load_dwordx4 a[216:219], v24, s[62:63] offset:3072 sc1
Mvva 60,136,148
Tv 124,240,61440
Tv 126,240,63488
Mvva 60,140,156
v_and_b32 v217, 0xffff0000, v178
v_add_f32 v36, v36, v216
Mvva 64,144,128
Tv 128,241,49152
Tv 130,241,51200
Mvva 64,148,136
global_load_dwordx4 a[220:223], v25, s[62:63] offset:3072 sc1
Mvva 64,152,144
Tv 132,241,53248
v_add_f32 v37, v37, v217
Mvva 64,156,152
Tv 134,241,55296
v_lshlrev_b32 v218, 16, v179
Mvva 68,144,132
Tv 136,241,57344
Tv 138,241,59392
Mvva 68,148,140
Tv 140,241,61440
v_and_b32 v219, 0xffff0000, v179
Mvva 68,152,148
Tv 142,241,63488
v_add_f32 v38, v38, v218
Mvva 68,156,156
s_waitcnt lgkmcnt(15)
v_add_f32 v39, v39, v219
v_lshlrev_b32 v216, 16, v180
Mvva 72,160,128
Tv 144,242,49152
v_and_b32 v217, 0xffff0000, v180
Mvva 72,164,136
Tv 146,242,51200
v_add_f32 v40, v40, v216
Mvva 72,168,144
Tv 148,242,53248
v_add_f32 v41, v41, v217
Mvva 72,172,152
Tv 150,242,55296
v_lshlrev_b32 v218, 16, v181
Mvva 76,160,132
Tv 152,242,57344
v_and_b32 v219, 0xffff0000, v181
Mvva 76,164,140
Tv 154,242,59392
v_add_f32 v42, v42, v218
Mvva 76,168,148
Tv 156,242,61440
v_add_f32 v43, v43, v219
Mvva 76,172,156
Tv 158,242,63488
v_lshlrev_b32 v216, 16, v182
s_waitcnt vmcnt(8)
s_waitcnt lgkmcnt(15)
Mvva 80,112,160
Tv 160,243,49152
Tv 162,243,51200
Mvva 80,116,168
ds_read_b32 v5, v3 offset:0
s_add_u32 s44, s44, 1
v_and_b32 v217, 0xffff0000, v182
v_add_f32 v44, v44, v216
Mvva 80,120,176
Tv 164,243,53248
Tv 166,243,55296
Mvva 80,124,184
ds_write_b32 v2, v4 offset:0
v_add_u32 v4, 1, v4
v_add_f32 v45, v45, v217
v_lshlrev_b32 v218, 16, v183
Mvva 84,112,164
Tv 168,243,57344
v_and_b32 v219, 0xffff0000, v183
Mvva 84,116,172
Tv 170,243,59392
Tv 172,243,61440
Mvva 84,120,180
Tv 174,243,63488
v_add_f32 v46, v46, v218
Mvva 84,124,188
s_waitcnt lgkmcnt(15)
s_waitcnt lgkmcnt(7)
v_cmp_gt_u32 vcc, s44, v5
s_cbranch_vccnz Lspin1716_%=
Lback1716_%=:
v_add_u32 v240, 4294901760, v240
v_add_f32 v47, v47, v219
v_lshlrev_b32 v216, 16, v184
Mvva 88,128,160
Tv 112,240,0
Tv 114,240,2048
Mvva 88,132,168
global_load_dwordx2 v[224:225], v249, s[12:13] offset:0
Mvva 88,136,176
Tv 116,240,4096
Tv 118,240,6144
Mvva 88,140,184
global_load_dwordx2 v[226:227], v250, s[12:13] offset:0
Mvva 92,128,164
Tv 120,240,8192
Tv 122,240,10240
Mvva 92,132,172
global_load_dwordx2 v[228:229], v249, s[12:13] offset:32
Mvva 92,136,180
Tv 124,240,12288
Tv 126,240,14336
Mvva 92,140,188
v_add_u32 v241, 4294901760, v241
v_and_b32 v217, 0xffff0000, v184
v_add_f32 v32, v32, v216
Mvva 96,144,160
Tv 128,241,0
Tv 130,241,2048
Mvva 96,148,168
global_load_dwordx2 v[230:231], v250, s[12:13] offset:32
Mvva 96,152,176
Tv 132,241,4096
v_add_f32 v33, v33, v217
Mvva 96,156,184
Tv 134,241,6144
v_lshlrev_b32 v218, 16, v185
Mvva 100,144,164
Tv 136,241,8192
Tv 138,241,10240
Mvva 100,148,172
Tv 140,241,12288
v_and_b32 v219, 0xffff0000, v185
Mvva 100,152,180
Tv 142,241,14336
v_add_f32 v34, v34, v218
Mvva 100,156,188
s_waitcnt lgkmcnt(15)
v_add_u32 v242, 4294901760, v242
v_add_f32 v35, v35, v219
v_lshlrev_b32 v216, 16, v186
Mvva 104,160,160
Tv 144,242,0
v_and_b32 v217, 0xffff0000, v186
Mvva 104,164,168
Tv 146,242,2048
v_add_f32 v36, v36, v216
Mvva 104,168,176
Tv 148,242,4096
v_add_f32 v37, v37, v217
Mvva 104,172,184
Tv 150,242,6144
v_lshlrev_b32 v218, 16, v187
Mvva 108,160,164
Tv 152,242,8192
v_and_b32 v219, 0xffff0000, v187
Mvva 108,164,172
Tv 154,242,10240
v_add_f32 v38, v38, v218
Mvva 108,168,180
Tv 156,242,12288
v_add_f32 v39, v39, v219
)ASM"
R"ASM(
Mvva 108,172,188
Tv 158,242,14336
v_lshlrev_b32 v216, 16, v188
s_waitcnt vmcnt(4)
s_waitcnt lgkmcnt(15)
Mvva 80,112,192
v_add_u32 v243, 4294901760, v243
Tv 160,243,0
Tv 162,243,2048
Mvva 80,116,200
ds_read_b32 v5, v3 offset:0
s_add_u32 s44, s44, 1
v_and_b32 v217, 0xffff0000, v188
v_add_f32 v40, v40, v216
Mvva 80,120,208
Tv 164,243,4096
Tv 166,243,6144
Mvva 80,124,216
ds_write_b32 v2, v4 offset:0
v_add_u32 v4, 1, v4
v_add_f32 v41, v41, v217
v_lshlrev_b32 v218, 16, v189
Mvva 84,112,196
Tv 168,243,8192
v_and_b32 v219, 0xffff0000, v189
Mvva 84,116,204
Tv 170,243,10240
Tv 172,243,12288
Mvva 84,120,212
Tv 174,243,14336
v_add_f32 v42, v42, v218
Mvva 84,124,220
s_waitcnt lgkmcnt(15)
s_waitcnt lgkmcnt(7)
v_cmp_gt_u32 vcc, s44, v5
s_cbranch_vccnz Lspin1717_%=
Lback1717_%=:
v_add_f32 v43, v43, v219
v_lshlrev_b32 v216, 16, v190
Mvva 88,128,192
Tv 112,242,16384
Tv 114,242,18432
Mvva 88,132,200
Tv 116,242,20480
v_and_b32 v217, 0xffff0000, v190
Mvva 88,136,208
Tv 118,242,22528
v_add_f32 v44, v44, v216
Mvva 88,140,216
Tv 120,242,24576
v_add_f32 v45, v45, v217
Mvva 92,128,196
Tv 122,242,26624
v_lshlrev_b32 v218, 16, v191
Mvva 92,132,204
Tv 124,242,28672
v_and_b32 v219, 0xffff0000, v191
Mvva 92,136,212
Tv 126,242,30720
v_add_f32 v46, v46, v218
Mvva 92,140,220
v_add_f32 v47, v47, v219
v_lshlrev_b32 v216, 16, v192
Mvva 96,144,192
Tv 128,243,16384
Tv 130,243,18432
Mvva 96,148,200
Tv 132,243,20480
Tv 134,243,22528
Mvva 96,152,208
Tv 136,243,24576
v_and_b32 v217, 0xffff0000, v192
Mvva 96,156,216
s_xor_b32 s50, s86, 2
s_lshl_b32 s50, s50, 16
s_add_u32 s50, s50, 4096
s_add_u32 s62, s82, s50
s_addc_u32 s63, s83, 0
CV 80,80,81
CV 81,82,83
CV 82,84,85
CV 83,86,87
global_store_dwordx4 v24, v[80:83], s[62:63] offset:0 sc1
v_add_f32 v32, v32, v216
v_add_f32 v33, v33, v217
Mvva 100,144,196
Tv 138,243,26624
Tv 140,243,28672
Mvva 100,148,204
CV 88,88,89
CV 89,90,91
CV 90,92,93
CV 91,94,95
global_store_dwordx4 v24, v[88:91], s[62:63] offset:1024 sc1
v_lshlrev_b32 v218, 16, v193
v_and_b32 v219, 0xffff0000, v193
Mvva 100,152,212
Tv 142,243,30720
v_add_f32 v34, v34, v218
Mvva 100,156,220
s_waitcnt lgkmcnt(15)
v_add_u32 v242, 65536, v242
v_add_f32 v35, v35, v219
v_lshlrev_b32 v216, 16, v194
Mvva 104,160,192
Tv 144,242,0
v_and_b32 v217, 0xffff0000, v194
Mvva 104,164,200
Tv 146,242,2048
v_add_f32 v36, v36, v216
Mvva 104,168,208
Tv 148,242,4096
v_add_f32 v37, v37, v217
Mvva 104,172,216
Tv 150,242,6144
v_lshlrev_b32 v218, 16, v195
Mvva 108,160,196
Tv 152,242,8192
v_and_b32 v219, 0xffff0000, v195
Mvva 108,164,204
Tv 154,242,10240
v_add_f32 v38, v38, v218
Mvva 108,168,212
Tv 156,242,12288
v_add_f32 v39, v39, v219
Mvva 108,172,220
Tv 158,242,14336
v_lshlrev_b32 v216, 16, v196
s_waitcnt lgkmcnt(15)
Mvva 64,112,160
v_add_u32 v243, 65536, v243
Tv 160,243,0
Tv 162,243,2048
Mvva 64,116,168
Tv 164,243,4096
v_and_b32 v217, 0xffff0000, v196
Mvva 64,120,176
Tv 166,243,6144
v_add_f32 v40, v40, v216
Mvva 64,124,184
Tv 168,243,8192
v_add_f32 v41, v41, v217
Mvva 68,112,164
Tv 170,243,10240
v_lshlrev_b32 v218, 16, v197
Mvva 68,116,172
Tv 172,243,12288
v_and_b32 v219, 0xffff0000, v197
Mvva 68,120,180
Tv 174,243,14336
v_add_f32 v42, v42, v218
Mvva 68,124,188
s_waitcnt lgkmcnt(15)
v_add_f32 v43, v43, v219
v_lshlrev_b32 v216, 16, v198
Mvva 72,128,160
Tv 112,240,16384
Tv 114,240,18432
Mvva 72,132,168
s_xor_b32 s50, s86, 3
s_lshl_b32 s50, s50, 16
s_add_u32 s50, s50, 8192
s_add_u32 s62, s82, s50
s_addc_u32 s63, s83, 0
CV 96,96,97
CV 97,98,99
CV 98,100,101
CV 99,102,103
global_store_dwordx4 v24, v[96:99], s[62:63] offset:0 sc1
Mvva 72,136,176
Tv 116,240,20480
Tv 118,240,22528
Mvva 72,140,184
CV 104,104,105
CV 105,106,107
CV 106,108,109
CV 107,110,111
global_store_dwordx4 v24, v[104:107], s[62:63] offset:1024 sc1
Mvva 76,128,164
Tv 120,240,24576
v_and_b32 v217, 0xffff0000, v198
Mvva 76,132,172
Tv 122,240,26624
Tv 124,240,28672
Mvva 76,136,180
Tv 126,240,30720
v_add_f32 v44, v44, v216
Mvva 76,140,188
s_waitcnt lgkmcnt(15)
v_add_f32 v45, v45, v217
v_lshlrev_b32 v218, 16, v199
Mvva 64,144,192
Tv 128,241,16384
Tv 130,241,18432
Mvva 64,148,200
Tv 132,241,20480
v_and_b32 v219, 0xffff0000, v199
Mvva 64,152,208
Tv 134,241,22528
v_add_f32 v46, v46, v218
)ASM"
R"ASM(
Mvva 64,156,216
Tv 136,241,24576
v_add_f32 v47, v47, v219
Mvva 68,144,196
Tv 138,241,26624
Mvva 68,148,204
Tv 140,241,28672
Mvva 68,152,212
Tv 142,241,30720
Mvva 68,156,220
s_waitcnt lgkmcnt(15)
v_add_u32 v240, 65536, v240
Mvva 72,160,192
Tv 144,240,0
Mvva 72,164,200
Tv 146,240,2048
Mvva 72,168,208
Tv 148,240,4096
Mvva 72,172,216
Tv 150,240,6144
Mvva 76,160,196
Tv 152,240,8192
Mvva 76,164,204
Tv 154,240,10240
Mvva 76,168,212
Tv 156,240,12288
Mvva 76,172,220
Tv 158,240,14336
s_waitcnt lgkmcnt(15)
Mvva 48,112,160
v_add_u32 v241, 65536, v241
Tv 160,241,0
Tv 162,241,2048
Mvva 48,116,168
Tv 164,241,4096
Mvva 48,120,176
Tv 166,241,6144
Mvva 48,124,184
Tv 168,241,8192
Mvva 52,112,164
Tv 170,241,10240
Mvva 52,116,172
Tv 172,241,12288
Mvva 52,120,180
Tv 174,241,14336
Mvva 52,124,188
s_waitcnt lgkmcnt(15)
Mvva 56,128,160
Mvva 56,132,168
s_xor_b32 s50, s86, 1
s_lshl_b32 s50, s50, 16
s_add_u32 s50, s50, 0
s_add_u32 s62, s82, s50
s_addc_u32 s63, s83, 0
CV 64,64,65
CV 65,66,67
CV 66,68,69
CV 67,70,71
global_store_dwordx4 v24, v[64:67], s[62:63] offset:0 sc1
Mvva 56,136,176
Mvva 56,140,184
CV 72,72,73
CV 73,74,75
CV 74,76,77
CV 75,78,79
global_store_dwordx4 v24, v[72:75], s[62:63] offset:1024 sc1
Mvva 60,128,164
Mvva 60,132,172
Mvva 60,136,180
Mvva 60,140,188
s_waitcnt lgkmcnt(8)
Mvva 48,144,192
Mvva 48,148,200
Mvva 48,152,208
Mvva 48,156,216
Mvva 52,144,196
Mvva 52,148,204
Mvva 52,152,212
Mvva 52,156,220
s_waitcnt lgkmcnt(0)
Mvva 56,160,192
Mvva 56,164,200
Mvva 56,168,208
Mvva 56,172,216
Mvva 60,160,196
Mvva 60,164,204
Mvva 60,168,212
Mvva 60,172,220
s_lshl_b32 s50, s86, 16
s_add_u32 s62, s82, s50
s_addc_u32 s63, s83, 0
v_add_u32 v240, 4096, v24
v_add_u32 v241, 8192, v24
s_mov_b32 s45, 262144
global_load_dwordx4 v[128:131], v241, s[62:63] offset:0 sc1
global_load_dwordx4 v[132:135], v241, s[62:63] offset:1024 sc1
global_load_dwordx4 v[120:123], v240, s[62:63] offset:0 sc1
global_load_dwordx4 v[124:127], v240, s[62:63] offset:1024 sc1
global_load_dwordx4 v[112:115], v24, s[62:63] offset:0 sc1
global_load_dwordx4 v[116:119], v24, s[62:63] offset:1024 sc1
v_mul_f32 v176, 0xbfb8aa3b, v32
v_mul_f32 v177, 0xbfb8aa3b, v33
v_mul_f32 v178, 0xbfb8aa3b, v34
v_mul_f32 v179, 0xbfb8aa3b, v35
v_mul_f32 v180, 0xbfb8aa3b, v36
v_mul_f32 v181, 0xbfb8aa3b, v37
v_mul_f32 v182, 0xbfb8aa3b, v38
v_mul_f32 v183, 0xbfb8aa3b, v39
v_mul_f32 v184, 0xbfb8aa3b, v40
v_mul_f32 v185, 0xbfb8aa3b, v41
v_mul_f32 v186, 0xbfb8aa3b, v42
v_mul_f32 v187, 0xbfb8aa3b, v43
v_mul_f32 v188, 0xbfb8aa3b, v44
v_mul_f32 v189, 0xbfb8aa3b, v45
v_mul_f32 v190, 0xbfb8aa3b, v46
v_mul_f32 v191, 0xbfb8aa3b, v47
EX 176
EX 177
EX 178
EX 179
EX 180
EX 181
EX 182
EX 183
EX 184
EX 185
EX 186
EX 187
EX 188
EX 189
EX 190
EX 191
v_add_f32 v176, 1.0, v176
v_add_f32 v177, 1.0, v177
v_add_f32 v178, 1.0, v178
v_add_f32 v179, 1.0, v179
v_add_f32 v180, 1.0, v180
v_add_f32 v181, 1.0, v181
v_add_f32 v182, 1.0, v182
v_add_f32 v183, 1.0, v183
v_add_f32 v184, 1.0, v184
v_add_f32 v185, 1.0, v185
v_add_f32 v186, 1.0, v186
v_add_f32 v187, 1.0, v187
v_add_f32 v188, 1.0, v188
v_add_f32 v189, 1.0, v189
v_add_f32 v190, 1.0, v190
v_add_f32 v191, 1.0, v191
v_rcp_f32 v176, v176
v_rcp_f32 v177, v177
v_rcp_f32 v178, v178
v_rcp_f32 v179, v179
v_rcp_f32 v180, v180
v_rcp_f32 v181, v181
v_rcp_f32 v182, v182
v_rcp_f32 v183, v183
v_rcp_f32 v184, v184
v_rcp_f32 v185, v185
v_rcp_f32 v186, v186
v_rcp_f32 v187, v187
v_rcp_f32 v188, v188
v_rcp_f32 v189, v189
v_rcp_f32 v190, v190
v_rcp_f32 v191, v191
v_mul_f32 v176, v176, v32
v_mul_f32 v177, v177, v33
v_mul_f32 v178, v178, v34
v_mul_f32 v179, v179, v35
v_mul_f32 v180, v180, v36
v_mul_f32 v181, v181, v37
v_mul_f32 v182, v182, v38
v_mul_f32 v183, v183, v39
v_mul_f32 v184, v184, v40
v_mul_f32 v185, v185, v41
v_mul_f32 v186, v186, v42
v_mul_f32 v187, v187, v43
v_mul_f32 v188, v188, v44
v_mul_f32 v189, v189, v45
v_mul_f32 v190, v190, v46
v_mul_f32 v191, v191, v47
s_waitcnt vmcnt(12)
v_lshlrev_b32 v192, 16, v224
v_and_b32 v193, 0xffff0000, v224
v_lshlrev_b32 v194, 16, v225
v_and_b32 v195, 0xffff0000, v225
v_lshlrev_b32 v196, 16, v226
v_and_b32 v197, 0xffff0000, v226
v_lshlrev_b32 v198, 16, v227
v_and_b32 v199, 0xffff0000, v227
v_lshlrev_b32 v200, 16, v228
v_and_b32 v201, 0xffff0000, v228
v_lshlrev_b32 v202, 16, v229
v_and_b32 v203, 0xffff0000, v229
v_lshlrev_b32 v204, 16, v230
v_and_b32 v205, 0xffff0000, v230
v_lshlrev_b32 v206, 16, v231
v_and_b32 v207, 0xffff0000, v231
s_branch Lxw7_%=
Lxch7_%=:
global_load_dwordx4 v[128:131], v241, s[62:63] offset:0 sc1
)ASM"
R"ASM(
global_load_dwordx4 v[132:135], v241, s[62:63] offset:1024 sc1
global_load_dwordx4 v[120:123], v240, s[62:63] offset:0 sc1
global_load_dwordx4 v[124:127], v240, s[62:63] offset:1024 sc1
global_load_dwordx4 v[112:115], v24, s[62:63] offset:0 sc1
global_load_dwordx4 v[116:119], v24, s[62:63] offset:1024 sc1
Lxw7_%=:
s_waitcnt vmcnt(0)
v_max3_u32 v160, v112, v113, v114
v_max3_u32 v161, v118, v119, v120
v_max3_u32 v162, v124, v125, v126
v_max3_u32 v163, v130, v131, v132
v_max3_u32 v160, v160, v115, v116
v_max3_u32 v161, v161, v121, v122
v_max3_u32 v162, v162, v127, v128
v_max3_u32 v163, v163, v133, v134
v_max_u32 v160, v160, v117
v_max_u32 v161, v161, v123
v_max_u32 v162, v162, v129
v_max_u32 v163, v163, v135
v_max3_u32 v160, v160, v161, v162
v_max_u32 v160, v160, v163
v_cmp_eq_u32 vcc, -1, v160
s_sub_u32 s45, s45, 1
s_cbranch_scc1 Lxgot7_%=
s_cbranch_vccnz Lxch7_%=
Lxgot7_%=:
v_lshlrev_b32 v136, 16, v112
v_and_b32 v137, 0xffff0000, v112
v_lshlrev_b32 v138, 16, v113
v_and_b32 v139, 0xffff0000, v113
v_lshlrev_b32 v140, 16, v114
v_and_b32 v141, 0xffff0000, v114
v_lshlrev_b32 v142, 16, v115
v_and_b32 v143, 0xffff0000, v115
v_lshlrev_b32 v144, 16, v116
v_and_b32 v145, 0xffff0000, v116
v_lshlrev_b32 v146, 16, v117
v_and_b32 v147, 0xffff0000, v117
v_lshlrev_b32 v148, 16, v118
v_and_b32 v149, 0xffff0000, v118
v_lshlrev_b32 v150, 16, v119
v_and_b32 v151, 0xffff0000, v119
v_pk_add_f32 v[48:49], v[48:49], v[136:137]
v_pk_add_f32 v[50:51], v[50:51], v[138:139]
v_pk_add_f32 v[52:53], v[52:53], v[140:141]
v_pk_add_f32 v[54:55], v[54:55], v[142:143]
v_pk_add_f32 v[56:57], v[56:57], v[144:145]
v_pk_add_f32 v[58:59], v[58:59], v[146:147]
v_pk_add_f32 v[60:61], v[60:61], v[148:149]
v_pk_add_f32 v[62:63], v[62:63], v[150:151]
v_lshlrev_b32 v136, 16, v120
v_and_b32 v137, 0xffff0000, v120
v_lshlrev_b32 v138, 16, v121
v_and_b32 v139, 0xffff0000, v121
v_lshlrev_b32 v140, 16, v122
v_and_b32 v141, 0xffff0000, v122
v_lshlrev_b32 v142, 16, v123
v_and_b32 v143, 0xffff0000, v123
v_lshlrev_b32 v144, 16, v124
v_and_b32 v145, 0xffff0000, v124
v_lshlrev_b32 v146, 16, v125
v_and_b32 v147, 0xffff0000, v125
v_lshlrev_b32 v148, 16, v126
v_and_b32 v149, 0xffff0000, v126
v_lshlrev_b32 v150, 16, v127
v_and_b32 v151, 0xffff0000, v127
v_pk_add_f32 v[48:49], v[48:49], v[136:137]
v_pk_add_f32 v[50:51], v[50:51], v[138:139]
v_pk_add_f32 v[52:53], v[52:53], v[140:141]
v_pk_add_f32 v[54:55], v[54:55], v[142:143]
v_pk_add_f32 v[56:57], v[56:57], v[144:145]
v_pk_add_f32 v[58:59], v[58:59], v[146:147]
v_pk_add_f32 v[60:61], v[60:61], v[148:149]
v_pk_add_f32 v[62:63], v[62:63], v[150:151]
v_lshlrev_b32 v136, 16, v128
v_and_b32 v137, 0xffff0000, v128
v_lshlrev_b32 v138, 16, v129
v_and_b32 v139, 0xffff0000, v129
v_lshlrev_b32 v140, 16, v130
v_and_b32 v141, 0xffff0000, v130
v_lshlrev_b32 v142, 16, v131
v_and_b32 v143, 0xffff0000, v131
v_lshlrev_b32 v144, 16, v132
v_and_b32 v145, 0xffff0000, v132
v_lshlrev_b32 v146, 16, v133
v_and_b32 v147, 0xffff0000, v133
v_lshlrev_b32 v148, 16, v134
v_and_b32 v149, 0xffff0000, v134
v_lshlrev_b32 v150, 16, v135
v_and_b32 v151, 0xffff0000, v135
v_pk_add_f32 v[48:49], v[48:49], v[136:137]
v_pk_add_f32 v[50:51], v[50:51], v[138:139]
v_pk_add_f32 v[52:53], v[52:53], v[140:141]
v_pk_add_f32 v[54:55], v[54:55], v[142:143]
v_pk_add_f32 v[56:57], v[56:57], v[144:145]
v_pk_add_f32 v[58:59], v[58:59], v[146:147]
v_pk_add_f32 v[60:61], v[60:61], v[148:149]
v_pk_add_f32 v[62:63], v[62:63], v[150:151]
v_pk_fma_f32 v[192:193], v[48:49], v[176:177], v[192:193]
v_pk_fma_f32 v[194:195], v[50:51], v[178:179], v[194:195]
v_pk_fma_f32 v[196:197], v[52:53], v[180:181], v[196:197]
v_pk_fma_f32 v[198:199], v[54:55], v[182:183], v[198:199]
v_pk_fma_f32 v[200:201], v[56:57], v[184:185], v[200:201]
v_pk_fma_f32 v[202:203], v[58:59], v[186:187], v[202:203]
v_pk_fma_f32 v[204:205], v[60:61], v[188:189], v[204:205]
v_pk_fma_f32 v[206:207], v[62:63], v[190:191], v[206:207]
CV 160,192,193
CV 161,194,195
CV 162,196,197
CV 163,198,199
CV 164,200,201
CV 165,202,203
CV 166,204,205
CV 167,206,207
global_store_dwordx2 v249, v[160:161], s[30:31] offset:0
global_store_dwordx2 v250, v[162:163], s[30:31] offset:0
global_store_dwordx2 v249, v[164:165], s[30:31] offset:32
global_store_dwordx2 v250, v[166:167], s[30:31] offset:32
s_or_b32 s50, s40, s35
s_or_b32 s50, s50, s41
s_cbranch_scc1 Ldone_%=
s_lshl_b64 exec, 1, s40
v_mov_b32 v223, s47
global_store_dword v220, v223, s[28:29] offset:128 sc1
s_mov_b64 exec, -1
s_branch Ldone_%=
Lwait_%=:
s_waitcnt vmcnt(0)
Lnotify_%=:
s_cmp_lg_u32 s73, 0
s_cbranch_scc1 Lwspin0_%=
s_mov_b32 s73, 1
v_mov_b32 v222, 150528
v_mov_b32 v223, 1
s_mov_b64 exec, 1
ds_add_rtn_u32 v222, v222, v223
s_waitcnt lgkmcnt(0)
v_readfirstlane_b32 s50, v222
s_mov_b64 exec, -1
s_nop 2
s_cmp_lg_u32 s50, 3
s_cbranch_scc1 Lwspin0_%=
s_lshl_b64 exec, 1, s40
v_mov_b32 v223, s47
global_store_dword v220, v223, s[28:29] offset:0 sc1
s_mov_b64 exec, -1
Lwspin0_%=:
s_mov_b32 s45, 262144
Lwspin_%=:
s_andn2_b32 s50, s72, s70
s_cbranch_scc0 Lwret_%=
s_mov_b32 exec_lo, s72
s_mov_b32 exec_hi, 0
global_load_dword v222, v220, s[28:29] sc1
s_mov_b64 exec, -1
s_waitcnt vmcnt(0)
v_cmp_eq_u32 s[70:71], s47, v222
s_nop 2
s_sub_u32 s45, s45, 1
s_cbranch_scc0 Lwspin_%=
Lwret_%=:
s_setpc_b64 s[76:77]
Lstat_%=:
s_mov_b32 s45, 262144
Lstat1_%=:
global_load_dwordx4 v[240:243], v30, s[28:29] offset:0 sc1
global_load_dwordx4 v[244:247], v30, s[28:29] offset:256 sc1
global_load_dwordx4 v[248:251], v30, s[28:29] offset:512 sc1
global_load_dwordx4 v[252:255], v30, s[28:29] offset:768 sc1
s_waitcnt vmcnt(0)
v_xor_b32 v31, s47, v241
v_xor_b32 v243, s47, v243
v_xor_b32 v245, s47, v245
v_xor_b32 v247, s47, v247
v_xor_b32 v249, s47, v249
v_xor_b32 v251, s47, v251
v_xor_b32 v253, s47, v253
v_xor_b32 v255, s47, v255
v_or3_b32 v31, v31, v243, v245
v_or3_b32 v31, v31, v247, v249
v_or3_b32 v31, v31, v251, v253
v_or_b32 v31, v31, v255
v_cmp_ne_u32 vcc, 0, v31
s_sub_u32 s45, s45, 1
s_cbranch_scc1 Lstatok_%=
s_cbranch_vccnz Lstat1_%=
s_branch Lstatok_%=
Lcsw_%=:
s_mov_b32 s45, 262144
Lcsw1_%=:
global_load_dwordx2 v[240:241], v20, s[28:29] offset:0 sc1
global_load_dwordx2 v[242:243], v20, s[28:29] offset:2048 sc1
global_load_dwordx2 v[244:245], v21, s[28:29] offset:0 sc1
global_load_dwordx2 v[246:247], v21, s[28:29] offset:2048 sc1
global_load_dwordx2 v[248:249], v22, s[28:29] offset:0 sc1
global_load_dwordx2 v[250:251], v22, s[28:29] offset:2048 sc1
global_load_dwordx2 v[252:253], v23, s[28:29] offset:0 sc1
global_load_dwordx2 v[254:255], v23, s[28:29] offset:2048 sc1
s_waitcnt vmcnt(0)
v_xor_b32 v31, s47, v241
)ASM"
R"ASM(
v_xor_b32 v243, s47, v243
v_xor_b32 v245, s47, v245
v_xor_b32 v247, s47, v247
v_xor_b32 v249, s47, v249
v_xor_b32 v251, s47, v251
v_xor_b32 v253, s47, v253
v_xor_b32 v255, s47, v255
v_or3_b32 v31, v31, v243, v245
v_or3_b32 v31, v31, v247, v249
v_or3_b32 v31, v31, v251, v253
v_or_b32 v31, v31, v255
v_cmp_ne_u32 vcc, 0, v31
s_sub_u32 s45, s45, 1
s_cbranch_scc1 Lcsok_%=
s_cbranch_vccnz Lcsw1_%=
s_branch Lcsok_%=
Lbw7_%=:
s_mov_b32 s45, 262144
Lbw17_%=:
s_lshl_b32 s50, s86, 16
s_add_u32 s50, s50, 0
s_add_u32 s64, s82, s50
s_addc_u32 s65, s83, 0
global_load_dwordx4 v[176:179], v24, s[64:65] offset:2048 sc1
global_load_dwordx4 v[180:183], v24, s[64:65] offset:3072 sc1
s_lshl_b32 s50, s86, 16
s_add_u32 s50, s50, 4096
s_add_u32 s64, s82, s50
s_addc_u32 s65, s83, 0
global_load_dwordx4 v[184:187], v24, s[64:65] offset:2048 sc1
global_load_dwordx4 v[188:191], v24, s[64:65] offset:3072 sc1
s_lshl_b32 s50, s86, 16
s_add_u32 s50, s50, 8192
s_add_u32 s64, s82, s50
s_addc_u32 s65, s83, 0
global_load_dwordx4 v[192:195], v24, s[64:65] offset:2048 sc1
global_load_dwordx4 v[196:199], v24, s[64:65] offset:3072 sc1
s_waitcnt vmcnt(0)
v_max3_u32 v216, v176, v177, v178
v_max3_u32 v217, v182, v183, v184
v_max3_u32 v218, v188, v189, v190
v_max3_u32 v219, v194, v195, v196
v_max3_u32 v216, v216, v179, v180
v_max3_u32 v217, v217, v185, v186
v_max3_u32 v218, v218, v191, v192
v_max3_u32 v219, v219, v197, v198
v_max_u32 v216, v216, v181
v_max_u32 v217, v217, v187
v_max_u32 v218, v218, v193
v_max_u32 v219, v219, v199
v_max3_u32 v216, v216, v217, v218
v_max_u32 v216, v216, v219
v_cmp_eq_u32 vcc, -1, v216
s_sub_u32 s45, s45, 1
s_cbranch_scc1 Lbok7_%=
s_cbranch_vccnz Lbw17_%=
s_branch Lbok7_%=
Lspec10_%=:
s_cmp_lg_u32 s45, 6
s_cbranch_scc1 Lback10_%=
s_cmp_lg_u32 s35, 0
s_cbranch_scc1 Lnohf_%=
s_lshl_b64 exec, 1, s40
v_mov_b32 v223, s47
global_store_dword v220, v223, s[28:29] offset:2048 sc1
s_mov_b64 exec, -1
Lnohf_%=:
s_branch Lback10_%=
Lspec6_%=:
s_cmp_lg_u32 s45, 1
s_cbranch_scc1 Lback6_%=
global_load_dword v221, v220, s[28:29] sc1
s_branch Lback6_%=
Lspec14_%=:
s_cmp_lg_u32 s45, 0
s_cbranch_scc1 Lback14_%=
v_cmp_eq_u32 s[70:71], s47, v221
s_mov_b32 s72, 0x40000
s_nop 1
Lhw_%=:
s_cmp_eq_u32 s71, -1
s_cbranch_scc1 Lhok_%=
global_load_dword v221, v220, s[28:29] sc1
s_waitcnt vmcnt(0)
v_cmp_eq_u32 s[70:71], s47, v221
s_nop 2
s_sub_u32 s72, s72, 1
s_cbranch_scc0 Lhw_%=
Lhok_%=:
s_add_u32 s62, s84, 0
s_addc_u32 s63, s85, 0
global_load_dwordx4 a[212:215], v24, s[62:63] offset:0 sc1
global_load_dwordx4 a[216:219], v248, s[62:63] offset:0 sc1
global_load_dwordx4 a[220:223], v24, s[62:63] offset:1024 sc1
global_load_dwordx4 a[224:227], v248, s[62:63] offset:1024 sc1
global_load_dwordx4 a[228:231], v24, s[62:63] offset:2048 sc1
global_load_dwordx4 a[232:235], v248, s[62:63] offset:2048 sc1
global_load_dwordx4 a[236:239], v24, s[62:63] offset:3072 sc1
global_load_dwordx4 a[240:243], v248, s[62:63] offset:3072 sc1
s_branch Lback14_%=
Lspec2_%=:
s_cmp_lg_u32 s45, 0
s_cbranch_scc1 Lback2_%=
s_add_u32 s62, s84, 4096
s_addc_u32 s63, s85, 0
global_load_dwordx4 a[244:247], v24, s[62:63] offset:0 sc1
global_load_dwordx4 a[248:251], v248, s[62:63] offset:0 sc1
global_load_dwordx4 a[192:195], v24, s[62:63] offset:1024 sc1
global_load_dwordx4 a[196:199], v248, s[62:63] offset:1024 sc1
s_mov_b32 s90, m0
s_mul_i32 s50, s86, 352
s_add_u32 s50, s50, 0
s_min_u32 s50, s50, s87
s_lshl_b32 s50, s50, 10
s_lshl_b32 s51, s86, 6
s_xor_b32 s51, s51, 128
s_and_b32 s51, s51, 128
s_add_u32 s50, s50, s51
s_add_u32 s60, s26, s50
s_addc_u32 s61, s27, 0
s_add_u32 m0, s34, 131072
s_nop 0
global_load_lds_dwordx4 v244, s[60:61] offset:0
global_load_lds_dwordx4 v245, s[60:61] offset:1024
global_load_lds_dwordx4 v246, s[60:61] offset:2048
global_load_lds_dwordx4 v247, s[60:61] offset:3072
s_mov_b32 m0, s90
s_branch Lback2_%=
Lredo_%=:
s_cmp_lg_u32 s46, 0
s_cbranch_scc1 Lnoredo_%=
s_waitcnt vmcnt(0) lgkmcnt(0)
s_nop 7
s_mov_b32 s46, 1
s_mov_b64 s[36:37], s[14:15]
s_mov_b32 s45, 32
v_mov_b32 v208, 0xff800000
v_mov_b32 v209, 0xff800000
v_mov_b32 v210, 0xff800000
v_mov_b32 v211, 0xff800000
Lmax_%=:
s_barrier
s_add_u32 m0, s34, 0
s_nop 0
global_load_lds_dwordx4 v6, s[36:37] offset:0
global_load_lds_dwordx4 v7, s[36:37] offset:1024
global_load_lds_dwordx4 v8, s[36:37] offset:2048
global_load_lds_dwordx4 v9, s[36:37] offset:3072
s_add_u32 s36, s36, 16384
s_addc_u32 s37, s37, 0
s_waitcnt vmcnt(0)
s_barrier
Ba 128,12,0
Ba 132,13,0
Ba 136,14,0
Ba 140,15,0
s_waitcnt lgkmcnt(0)
Zvav 112,128,48
Mvav 112,132,52
Mvav 112,136,56
Mvav 112,140,60
Zvav 116,128,64
Mvav 116,132,68
Mvav 116,136,72
Mvav 116,140,76
Zvav 120,128,80
Mvav 120,132,84
Mvav 120,136,88
Mvav 120,140,92
Zvav 124,128,96
Mvav 124,132,100
Mvav 124,136,104
Mvav 124,140,108
s_nop 7
v_max3_f32 v208, v208, v112, v113
v_max3_f32 v208, v208, v114, v115
v_max3_f32 v209, v209, v116, v117
v_max3_f32 v209, v209, v118, v119
v_max3_f32 v210, v210, v120, v121
v_max3_f32 v210, v210, v122, v123
v_max3_f32 v211, v211, v124, v125
v_max3_f32 v211, v211, v126, v127
Ba 144,12,4096
Ba 148,13,4096
Ba 152,14,4096
Ba 156,15,4096
s_waitcnt lgkmcnt(0)
Zvav 112,144,48
Mvav 112,148,52
Mvav 112,152,56
Mvav 112,156,60
Zvav 116,144,64
Mvav 116,148,68
Mvav 116,152,72
Mvav 116,156,76
Zvav 120,144,80
Mvav 120,148,84
Mvav 120,152,88
Mvav 120,156,92
Zvav 124,144,96
)ASM"
R"ASM(
Mvav 124,148,100
Mvav 124,152,104
Mvav 124,156,108
s_nop 7
v_max3_f32 v208, v208, v112, v113
v_max3_f32 v208, v208, v114, v115
v_max3_f32 v209, v209, v116, v117
v_max3_f32 v209, v209, v118, v119
v_max3_f32 v210, v210, v120, v121
v_max3_f32 v210, v210, v122, v123
v_max3_f32 v211, v211, v124, v125
v_max3_f32 v211, v211, v126, v127
Ba 128,12,8192
Ba 132,13,8192
Ba 136,14,8192
Ba 140,15,8192
s_waitcnt lgkmcnt(0)
Zvav 112,128,48
Mvav 112,132,52
Mvav 112,136,56
Mvav 112,140,60
Zvav 116,128,64
Mvav 116,132,68
Mvav 116,136,72
Mvav 116,140,76
Zvav 120,128,80
Mvav 120,132,84
Mvav 120,136,88
Mvav 120,140,92
Zvav 124,128,96
Mvav 124,132,100
Mvav 124,136,104
Mvav 124,140,108
s_nop 7
v_max3_f32 v208, v208, v112, v113
v_max3_f32 v208, v208, v114, v115
v_max3_f32 v209, v209, v116, v117
v_max3_f32 v209, v209, v118, v119
v_max3_f32 v210, v210, v120, v121
v_max3_f32 v210, v210, v122, v123
v_max3_f32 v211, v211, v124, v125
v_max3_f32 v211, v211, v126, v127
Ba 144,12,12288
Ba 148,13,12288
Ba 152,14,12288
Ba 156,15,12288
s_waitcnt lgkmcnt(0)
Zvav 112,144,48
Mvav 112,148,52
Mvav 112,152,56
Mvav 112,156,60
Zvav 116,144,64
Mvav 116,148,68
Mvav 116,152,72
Mvav 116,156,76
Zvav 120,144,80
Mvav 120,148,84
Mvav 120,152,88
Mvav 120,156,92
Zvav 124,144,96
Mvav 124,148,100
Mvav 124,152,104
Mvav 124,156,108
s_nop 7
v_max3_f32 v208, v208, v112, v113
v_max3_f32 v208, v208, v114, v115
v_max3_f32 v209, v209, v116, v117
v_max3_f32 v209, v209, v118, v119
v_max3_f32 v210, v210, v120, v121
v_max3_f32 v210, v210, v122, v123
v_max3_f32 v211, v211, v124, v125
v_max3_f32 v211, v211, v126, v127
s_sub_u32 s45, s45, 1
s_cmp_lg_u32 s45, 0
s_cbranch_scc1 Lmax_%=
v_mov_b32 v212, v208
v_mov_b32 v213, v209
v_mov_b32 v214, v210
v_mov_b32 v215, v211
v_permlane32_swap_b32 v208, v212
v_permlane32_swap_b32 v209, v213
v_permlane32_swap_b32 v210, v214
v_permlane32_swap_b32 v211, v215
v_max_f32 v208, v208, v212
v_max_f32 v209, v209, v213
v_max_f32 v210, v210, v214
v_max_f32 v211, v211, v215
v_mov_b32 v212, v208
v_mov_b32 v213, v209
v_mov_b32 v214, v210
v_mov_b32 v215, v211
v_permlane16_swap_b32 v208, v212
v_permlane16_swap_b32 v209, v213
v_permlane16_swap_b32 v210, v214
v_permlane16_swap_b32 v211, v215
v_max_f32 v208, v208, v212
v_max_f32 v209, v209, v213
v_max_f32 v210, v210, v214
v_max_f32 v211, v211, v215
v_sub_f32 v32, 0, v208
v_sub_f32 v33, 0, v208
v_sub_f32 v34, 0, v208
v_sub_f32 v35, 0, v208
v_sub_f32 v36, 0, v209
v_sub_f32 v37, 0, v209
v_sub_f32 v38, 0, v209
v_sub_f32 v39, 0, v209
v_sub_f32 v40, 0, v210
v_sub_f32 v41, 0, v210
v_sub_f32 v42, 0, v210
v_sub_f32 v43, 0, v210
v_sub_f32 v44, 0, v211
v_sub_f32 v45, 0, v211
v_sub_f32 v46, 0, v211
v_sub_f32 v47, 0, v211
s_barrier
s_mov_b64 s[36:37], s[14:15]
s_mov_b64 s[38:39], s[16:17]
s_add_u32 m0, s34, 0
s_nop 0
global_load_lds_dwordx4 v6, s[36:37] offset:0
global_load_lds_dwordx4 v7, s[36:37] offset:1024
global_load_lds_dwordx4 v8, s[36:37] offset:2048
global_load_lds_dwordx4 v9, s[36:37] offset:3072
s_add_u32 s36, s36, 16384
s_addc_u32 s37, s37, 0
s_add_u32 m0, s34, 16384
s_nop 0
global_load_lds_dwordx4 v6, s[36:37] offset:0
global_load_lds_dwordx4 v7, s[36:37] offset:1024
global_load_lds_dwordx4 v8, s[36:37] offset:2048
global_load_lds_dwordx4 v9, s[36:37] offset:3072
s_add_u32 s36, s36, 16384
s_addc_u32 s37, s37, 0
s_add_u32 m0, s42, 0
s_nop 0
global_load_lds_dwordx4 v10, s[38:39] offset:0
global_load_lds_dwordx4 v11, s[38:39] offset:1024
global_load_lds_dwordx4 v10, s[38:39] offset:2048
global_load_lds_dwordx4 v11, s[38:39] offset:3072
s_add_u32 s38, s38, 16384
s_addc_u32 s39, s39, 0
s_add_u32 m0, s34, 32768
s_nop 0
global_load_lds_dwordx4 v6, s[36:37] offset:0
global_load_lds_dwordx4 v7, s[36:37] offset:1024
global_load_lds_dwordx4 v8, s[36:37] offset:2048
global_load_lds_dwordx4 v9, s[36:37] offset:3072
s_add_u32 s36, s36, 16384
s_addc_u32 s37, s37, 0
s_add_u32 m0, s42, 16384
s_nop 0
global_load_lds_dwordx4 v10, s[38:39] offset:0
global_load_lds_dwordx4 v11, s[38:39] offset:1024
global_load_lds_dwordx4 v10, s[38:39] offset:2048
global_load_lds_dwordx4 v11, s[38:39] offset:3072
s_add_u32 s38, s38, 16384
s_addc_u32 s39, s39, 0
s_waitcnt vmcnt(0)
s_barrier
s_branch Lhead_%=
Lspin300_%=:
ds_read_b32 v5, v3
s_waitcnt lgkmcnt(0)
v_cmp_gt_u32 vcc, s44, v5
s_cbranch_vccnz Lspin300_%=
s_branch Lback300_%=
Lspin301_%=:
ds_read_b32 v5, v3
s_waitcnt lgkmcnt(0)
v_cmp_gt_u32 vcc, s44, v5
s_cbranch_vccnz Lspin301_%=
s_branch Lback301_%=
Lspin302_%=:
ds_read_b32 v5, v3
s_waitcnt lgkmcnt(0)
v_cmp_gt_u32 vcc, s44, v5
s_cbranch_vccnz Lspin302_%=
s_branch Lback302_%=
Lspin303_%=:
ds_read_b32 v5, v3
s_waitcnt lgkmcnt(0)
v_cmp_gt_u32 vcc, s44, v5
s_cbranch_vccnz Lspin303_%=
s_branch Lback303_%=
Lspin304_%=:
ds_read_b32 v5, v3
s_waitcnt lgkmcnt(0)
v_cmp_gt_u32 vcc, s44, v5
s_cbranch_vccnz Lspin304_%=
s_branch Lback304_%=
Lspin305_%=:
ds_read_b32 v5, v3
s_waitcnt lgkmcnt(0)
v_cmp_gt_u32 vcc, s44, v5
s_cbranch_vccnz Lspin305_%=
s_branch Lback305_%=
Lspin306_%=:
ds_read_b32 v5, v3
s_waitcnt lgkmcnt(0)
)ASM"
R"ASM(
v_cmp_gt_u32 vcc, s44, v5
s_cbranch_vccnz Lspin306_%=
s_branch Lback306_%=
Lspin307_%=:
ds_read_b32 v5, v3
s_waitcnt lgkmcnt(0)
v_cmp_gt_u32 vcc, s44, v5
s_cbranch_vccnz Lspin307_%=
s_branch Lback307_%=
Lspin308_%=:
ds_read_b32 v5, v3
s_waitcnt lgkmcnt(0)
v_cmp_gt_u32 vcc, s44, v5
s_cbranch_vccnz Lspin308_%=
s_branch Lback308_%=
Lspin309_%=:
ds_read_b32 v5, v3
s_waitcnt lgkmcnt(0)
v_cmp_gt_u32 vcc, s44, v5
s_cbranch_vccnz Lspin309_%=
s_branch Lback309_%=
Lspin310_%=:
ds_read_b32 v5, v3
s_waitcnt lgkmcnt(0)
v_cmp_gt_u32 vcc, s44, v5
s_cbranch_vccnz Lspin310_%=
s_branch Lback310_%=
Lspin311_%=:
ds_read_b32 v5, v3
s_waitcnt lgkmcnt(0)
v_cmp_gt_u32 vcc, s44, v5
s_cbranch_vccnz Lspin311_%=
s_branch Lback311_%=
Lspin312_%=:
ds_read_b32 v5, v3
s_waitcnt lgkmcnt(0)
v_cmp_gt_u32 vcc, s44, v5
s_cbranch_vccnz Lspin312_%=
s_branch Lback312_%=
Lspin313_%=:
ds_read_b32 v5, v3
s_waitcnt lgkmcnt(0)
v_cmp_gt_u32 vcc, s44, v5
s_cbranch_vccnz Lspin313_%=
s_branch Lback313_%=
Lspin314_%=:
ds_read_b32 v5, v3
s_waitcnt lgkmcnt(0)
v_cmp_gt_u32 vcc, s44, v5
s_cbranch_vccnz Lspin314_%=
s_branch Lback314_%=
Lspin315_%=:
ds_read_b32 v5, v3
s_waitcnt lgkmcnt(0)
v_cmp_gt_u32 vcc, s44, v5
s_cbranch_vccnz Lspin315_%=
s_branch Lback315_%=
Lspin102_%=:
ds_read_b32 v5, v3
s_waitcnt lgkmcnt(0)
v_cmp_gt_u32 vcc, s44, v5
s_cbranch_vccnz Lspin102_%=
s_branch Lback102_%=
Lspin106_%=:
ds_read_b32 v5, v3
s_waitcnt lgkmcnt(0)
v_cmp_gt_u32 vcc, s44, v5
s_cbranch_vccnz Lspin106_%=
s_branch Lback106_%=
Lspin10_%=:
ds_read_b32 v5, v3
s_waitcnt lgkmcnt(0)
v_cmp_gt_u32 vcc, s44, v5
s_cbranch_vccnz Lspin10_%=
s_branch Lspec10_%=
Lspin14_%=:
ds_read_b32 v5, v3
s_waitcnt lgkmcnt(0)
v_cmp_gt_u32 vcc, s44, v5
s_cbranch_vccnz Lspin14_%=
s_branch Lspec14_%=
Lspin2_%=:
ds_read_b32 v5, v3
s_waitcnt lgkmcnt(0)
v_cmp_gt_u32 vcc, s44, v5
s_cbranch_vccnz Lspin2_%=
s_branch Lspec2_%=
Lspin6_%=:
ds_read_b32 v5, v3
s_waitcnt lgkmcnt(0)
v_cmp_gt_u32 vcc, s44, v5
s_cbranch_vccnz Lspin6_%=
s_branch Lspec6_%=
Lspin222_%=:
ds_read_b32 v5, v3
s_waitcnt lgkmcnt(0)
v_cmp_gt_u32 vcc, s44, v5
s_cbranch_vccnz Lspin222_%=
s_branch Lback222_%=
Lspin226_%=:
ds_read_b32 v5, v3
s_waitcnt lgkmcnt(0)
v_cmp_gt_u32 vcc, s44, v5
s_cbranch_vccnz Lspin226_%=
s_branch Lback226_%=
Lspin400_%=:
ds_read_b32 v5, v3
s_waitcnt lgkmcnt(0)
v_cmp_gt_u32 vcc, s44, v5
s_cbranch_vccnz Lspin400_%=
s_branch Lback400_%=
Leven_%=:
s_cmp_lg_u32 s86, 3
s_cbranch_scc1 Lnz9_%=
v_accvgpr_write_b32 a152, 0
v_accvgpr_write_b32 a153, 0
v_accvgpr_write_b32 a154, 0
v_accvgpr_write_b32 a155, 0
v_accvgpr_write_b32 a156, 0
v_accvgpr_write_b32 a157, 0
v_accvgpr_write_b32 a158, 0
v_accvgpr_write_b32 a159, 0
Lnz9_%=:
v_add_u32 v240, 131072, v240
Tv 112,240,0
Tv 114,240,2048
Tv 116,240,4096
Tv 118,240,6144
Tv 120,240,8192
Tv 122,240,10240
Tv 124,240,12288
Tv 126,240,14336
v_add_u32 v241, 131072, v241
Tv 128,241,0
Tv 130,241,2048
Tv 132,241,4096
Tv 134,241,6144
Tv 136,241,8192
Tv 138,241,10240
Tv 140,241,12288
Tv 142,241,14336
v_add_u32 v242, 131072, v242
Tv 144,242,0
Tv 146,242,2048
Tv 148,242,4096
Tv 150,242,6144
Tv 152,242,8192
Tv 154,242,10240
Tv 156,242,12288
Tv 158,242,14336
s_waitcnt lgkmcnt(15)
Zvva 80,112,212
v_add_u32 v243, 131072, v243
Tv 160,243,0
Tv 162,243,2048
Mvva 80,116,220
Mvva 80,120,228
Tv 164,243,4096
Tv 166,243,6144
Mvva 80,124,236
ds_write_b32 v2, v4 offset:0
v_add_u32 v4, 1, v4
Zvva 84,112,216
Tv 168,243,8192
Mvva 84,116,224
Tv 170,243,10240
Tv 172,243,12288
Mvva 84,120,232
Tv 174,243,14336
Mvva 84,124,240
s_waitcnt lgkmcnt(15)
v_add_u32 v240, 4294836224, v240
Zvva 88,128,212
Tv 112,240,0
Tv 114,240,2048
Mvva 88,132,220
Tv 116,240,4096
Mvva 88,136,228
Tv 118,240,6144
Mvva 88,140,236
Tv 120,240,8192
Zvva 92,128,216
Tv 122,240,10240
Mvva 92,132,224
Tv 124,240,12288
Mvva 92,136,232
Tv 126,240,14336
Mvva 92,140,240
s_waitcnt lgkmcnt(15)
v_add_u32 v241, 4294836224, v241
Zvva 96,144,212
Tv 128,241,0
Tv 130,241,2048
Mvva 96,148,220
Tv 132,241,4096
Mvva 96,152,228
Tv 134,241,6144
Mvva 96,156,236
Tv 136,241,8192
Zvva 100,144,216
)ASM"
R"ASM(
Tv 138,241,10240
Mvva 100,148,224
Tv 140,241,12288
Mvva 100,152,232
Tv 142,241,14336
Mvva 100,156,240
s_waitcnt lgkmcnt(15)
v_add_u32 v242, 4294836224, v242
Zvva 104,160,212
Tv 144,242,0
Mvva 104,164,220
Tv 146,242,2048
Mvva 104,168,228
Tv 148,242,4096
Mvva 104,172,236
Tv 150,242,6144
Zvva 108,160,216
Tv 152,242,8192
Mvva 108,164,224
Tv 154,242,10240
Mvva 108,168,232
Tv 156,242,12288
Mvva 108,172,240
Tv 158,242,14336
s_waitcnt lgkmcnt(15)
Mvva 80,112,244
v_add_u32 v243, 4294836224, v243
Tv 160,243,0
Tv 162,243,2048
Mvva 80,116,192
ds_read_b32 v5, v3 offset:0
s_add_u32 s44, s44, 1
Mvvv 80,120,252
Tv 164,243,4096
Tv 166,243,6144
Mvva 80,124,128
ds_write_b32 v2, v4 offset:0
v_add_u32 v4, 1, v4
Mvva 84,112,248
Tv 168,243,8192
Mvva 84,116,196
Tv 170,243,10240
Tv 172,243,12288
Mvvv 84,120,236
Tv 174,243,14336
Mvva 84,124,132
s_waitcnt lgkmcnt(15)
s_waitcnt lgkmcnt(7)
v_cmp_gt_u32 vcc, s44, v5
s_cbranch_vccnz Lspin1901_%=
Lback1901_%=:
Mvva 88,128,244
Tv 112,240,16384
Mvva 88,132,192
Tv 114,240,18432
Mvvv 88,136,252
Tv 116,240,20480
Mvva 88,140,128
Tv 118,240,22528
Mvva 92,128,248
Tv 120,240,24576
Mvva 92,132,196
Tv 122,240,26624
Mvvv 92,136,236
Mvva 92,140,132
Mvva 96,144,244
Tv 128,241,16384
Mvva 96,148,192
Tv 130,241,18432
Mvvv 96,152,252
Tv 132,241,20480
Mvva 96,156,128
Tv 134,241,22528
Mvva 100,144,248
Tv 136,241,24576
Mvva 100,148,196
Tv 138,241,26624
Mvvv 100,152,236
Mvva 100,156,132
s_waitcnt lgkmcnt(12)
Mvva 104,160,244
Tv 144,242,16384
Mvva 104,164,192
Tv 146,242,18432
Mvvv 104,168,252
Tv 148,242,20480
Mvva 104,172,128
Mvva 108,160,248
Tv 150,242,22528
Mvva 108,164,196
Tv 152,242,24576
Mvvv 108,168,236
Tv 154,242,26624
Mvva 108,172,132
s_waitcnt vmcnt(0)
s_mov_b32 s72, 0
s_call_b64 s[76:77], Lnotify_%=
s_waitcnt lgkmcnt(12)
Mvva 80,112,136
Tv 160,243,16384
Tv 162,243,18432
Mvva 80,116,144
ds_read_b32 v5, v3 offset:0
s_add_u32 s44, s44, 1
Mvva 80,120,152
Tv 164,243,20480
Tv 166,243,22528
Mvva 84,112,140
ds_write_b32 v2, v4 offset:0
v_add_u32 v4, 1, v4
Mvva 84,116,148
Tv 168,243,24576
Tv 170,243,26624
Mvva 84,120,156
s_waitcnt lgkmcnt(14)
s_waitcnt lgkmcnt(5)
v_cmp_gt_u32 vcc, s44, v5
s_cbranch_vccnz Lspin1902_%=
Lback1902_%=:
v_add_u32 v242, 65536, v242
Mvva 88,128,136
Tv 112,242,0
Tv 114,242,2048
Tv 116,242,4096
Mvva 88,132,144
s_xor_b32 s50, s89, 0
s_lshl_b32 s50, s50, 17
s_lshl_b32 s51, s86, 6
s_xor_b32 s51, s51, 128
s_and_b32 s51, s51, 128
s_add_u32 s50, s50, s51
s_add_u32 s60, s24, s50
s_addc_u32 s61, s25, 0
s_add_u32 m0, s34, 49152
s_nop 0
global_load_lds_dwordx4 v244, s[60:61] offset:0
Mvva 88,136,152
Tv 118,242,6144
Tv 120,242,8192
Tv 122,242,10240
Mvva 92,128,140
global_load_lds_dwordx4 v245, s[60:61] offset:1024
Mvva 92,132,148
Tv 124,242,12288
Tv 126,242,14336
Mvva 92,136,156
global_load_lds_dwordx4 v246, s[60:61] offset:2048
v_add_u32 v243, 65536, v243
Mvva 96,144,136
Tv 128,243,0
Tv 130,243,2048
Tv 132,243,4096
Mvva 96,148,144
global_load_lds_dwordx4 v247, s[60:61] offset:3072
Mvva 96,152,152
Tv 134,243,6144
Tv 136,243,8192
Tv 138,243,10240
Mvva 100,144,140
s_xor_b32 s50, s89, 0
s_lshl_b32 s50, s50, 17
s_lshl_b32 s51, s86, 6
s_xor_b32 s51, s51, 0
s_and_b32 s51, s51, 128
s_add_u32 s50, s50, s51
s_add_u32 s60, s24, s50
s_addc_u32 s61, s25, 0
s_add_u32 m0, s34, 98304
s_nop 0
global_load_lds_dwordx4 v244, s[60:61] offset:0
Mvva 100,148,148
Tv 140,243,12288
Tv 142,243,14336
Mvva 100,152,156
s_waitcnt lgkmcnt(15)
v_add_u32 v242, 4294901760, v242
Mvva 104,160,136
Tv 144,242,32768
Tv 146,242,34816
Tv 148,242,36864
Mvva 104,164,144
global_load_lds_dwordx4 v245, s[60:61] offset:1024
Mvva 104,168,152
Tv 150,242,38912
Tv 152,242,40960
Tv 154,242,43008
Mvva 108,160,140
global_load_lds_dwordx4 v246, s[60:61] offset:2048
Mvva 108,164,148
Tv 156,242,45056
Tv 158,242,47104
Mvva 108,168,156
global_load_lds_dwordx4 v247, s[60:61] offset:3072
s_waitcnt lgkmcnt(15)
Zvva 64,112,212
v_add_u32 v243, 4294901760, v243
Tv 160,243,32768
Tv 162,243,34816
Mvva 64,116,220
ds_read_b32 v5, v3 offset:0
)ASM"
R"ASM(
s_add_u32 s44, s44, 1
Mvva 64,120,228
Tv 164,243,36864
Tv 166,243,38912
Mvva 64,124,236
ds_write_b32 v2, v4 offset:0
v_add_u32 v4, 1, v4
Zvva 68,112,216
Tv 168,243,40960
Mvva 68,116,224
Tv 170,243,43008
Tv 172,243,45056
Mvva 68,120,232
Tv 174,243,47104
Mvva 68,124,240
s_waitcnt lgkmcnt(15)
s_waitcnt lgkmcnt(7)
v_cmp_gt_u32 vcc, s44, v5
s_cbranch_vccnz Lspin1903_%=
Lback1903_%=:
v_add_u32 v242, 65536, v242
Zvva 72,128,212
Tv 112,242,16384
Tv 114,242,18432
Mvva 72,132,220
s_xor_b32 s50, s86, 2
s_lshl_b32 s50, s50, 16
s_add_u32 s50, s50, 4096
s_add_u32 s62, s82, s50
s_addc_u32 s63, s83, 0
CV 80,80,81
CV 81,82,83
CV 82,84,85
CV 83,86,87
global_store_dwordx4 v24, v[80:83], s[62:63] offset:2048 sc1
Mvva 72,136,228
Tv 116,242,20480
Mvva 72,140,236
CV 88,88,89
CV 89,90,91
CV 90,92,93
CV 91,94,95
global_store_dwordx4 v24, v[88:91], s[62:63] offset:3072 sc1
Zvva 76,128,216
Tv 118,242,22528
Tv 120,242,24576
Mvva 76,132,224
s_xor_b32 s50, s86, 3
s_lshl_b32 s50, s50, 16
s_add_u32 s50, s50, 8192
s_add_u32 s62, s82, s50
s_addc_u32 s63, s83, 0
CV 96,96,97
CV 97,98,99
CV 98,100,101
CV 99,102,103
global_store_dwordx4 v24, v[96:99], s[62:63] offset:2048 sc1
Mvva 76,136,232
Tv 122,242,26624
Mvva 76,140,240
CV 104,104,105
CV 105,106,107
CV 106,108,109
CV 107,110,111
global_store_dwordx4 v24, v[104:107], s[62:63] offset:3072 sc1
v_add_u32 v243, 65536, v243
Mvva 64,144,244
Tv 128,243,16384
Tv 130,243,18432
Mvva 64,148,192
s_xor_b32 s50, s89, 1
s_lshl_b32 s50, s50, 17
s_lshl_b32 s51, s86, 6
s_xor_b32 s51, s51, 128
s_and_b32 s51, s51, 128
s_add_u32 s50, s50, s51
s_add_u32 s60, s24, s50
s_addc_u32 s61, s25, 0
s_add_u32 m0, s34, 114688
s_nop 0
global_load_lds_dwordx4 v244, s[60:61] offset:0
Mvvv 64,152,252
Tv 132,243,20480
Mvva 64,156,128
global_load_lds_dwordx4 v245, s[60:61] offset:1024
Mvva 68,144,248
Tv 134,243,22528
Tv 136,243,24576
Mvva 68,148,196
global_load_lds_dwordx4 v246, s[60:61] offset:2048
Mvvv 68,152,236
Tv 138,243,26624
Mvva 68,156,132
s_waitcnt lgkmcnt(12)
global_load_lds_dwordx4 v247, s[60:61] offset:3072
v_add_u32 v240, 65536, v240
Mvva 72,160,244
Tv 144,240,0
Tv 146,240,2048
Mvva 72,164,192
s_xor_b32 s50, s89, 1
s_lshl_b32 s50, s50, 17
s_lshl_b32 s51, s86, 6
s_xor_b32 s51, s51, 0
s_and_b32 s51, s51, 128
s_add_u32 s50, s50, s51
s_add_u32 s60, s24, s50
s_addc_u32 s61, s25, 0
s_add_u32 m0, s34, 0
s_nop 0
global_load_lds_dwordx4 v244, s[60:61] offset:0
Mvvv 72,168,252
Tv 148,240,4096
Tv 150,240,6144
Mvva 72,172,128
global_load_lds_dwordx4 v245, s[60:61] offset:1024
Mvva 76,160,248
Tv 152,240,8192
Tv 154,240,10240
Mvva 76,164,196
global_load_lds_dwordx4 v246, s[60:61] offset:2048
Mvvv 76,168,236
Tv 156,240,12288
Tv 158,240,14336
Mvva 76,172,132
global_load_lds_dwordx4 v247, s[60:61] offset:3072
s_waitcnt vmcnt(16)
s_waitcnt lgkmcnt(14)
Mvva 64,112,136
v_add_u32 v241, 65536, v241
Tv 160,241,0
Tv 162,241,2048
Tv 164,241,4096
Mvva 64,116,144
ds_read_b32 v5, v3 offset:0
s_add_u32 s44, s44, 1
Mvva 64,120,152
Tv 166,241,6144
Tv 168,241,8192
Tv 170,241,10240
Mvva 68,112,140
ds_write_b32 v2, v4 offset:0
v_add_u32 v4, 1, v4
Mvva 68,116,148
Tv 172,241,12288
Tv 174,241,14336
Mvva 68,120,156
s_waitcnt lgkmcnt(15)
s_waitcnt lgkmcnt(6)
v_cmp_gt_u32 vcc, s44, v5
s_cbranch_vccnz Lspin1904_%=
Lback1904_%=:
v_add_u32 v240, 4294901760, v240
Mvva 72,128,136
Tv 112,240,32768
Tv 114,240,34816
Mvva 72,132,144
Tv 116,240,36864
Tv 118,240,38912
Mvva 72,136,152
Tv 120,240,40960
Mvva 76,128,140
Tv 122,240,43008
Tv 124,240,45056
Mvva 76,132,148
Tv 126,240,47104
Mvva 76,136,156
v_add_u32 v241, 4294901760, v241
Zvva 48,144,212
Tv 128,241,32768
Tv 130,241,34816
Mvva 48,148,220
Tv 132,241,36864
Mvva 48,152,228
Tv 134,241,38912
Mvva 48,156,236
Tv 136,241,40960
Tv 138,241,43008
Zvva 52,144,216
Tv 140,241,45056
Mvva 52,148,224
s_xor_b32 s50, s86, 1
s_lshl_b32 s50, s50, 16
s_add_u32 s50, s50, 0
s_add_u32 s62, s82, s50
s_addc_u32 s63, s83, 0
CV 64,64,65
CV 65,66,67
CV 66,68,69
CV 67,70,71
global_store_dwordx4 v24, v[64:67], s[62:63] offset:2048 sc1
Mvva 52,152,232
Tv 142,241,47104
Mvva 52,156,240
s_waitcnt lgkmcnt(15)
CV 72,72,73
CV 73,74,75
CV 74,76,77
CV 75,78,79
global_store_dwordx4 v24, v[72:75], s[62:63] offset:3072 sc1
)ASM"
R"ASM(
v_add_u32 v240, 65536, v240
Zvva 56,160,212
Tv 144,240,16384
Mvva 56,164,220
Tv 146,240,18432
Mvva 56,168,228
Tv 148,240,20480
Mvva 56,172,236
Zvva 60,160,216
Tv 150,240,22528
Mvva 60,164,224
Tv 152,240,24576
Mvva 60,168,232
Tv 154,240,26624
Mvva 60,172,240
s_mov_b32 s70, 0
s_and_b32 s50, s40, 28
s_lshl_b32 s72, 15, s50
s_andn2_b32 s50, s72, s70
s_cbranch_scc0 Lrdya9_%=
s_call_b64 s[76:77], Lwspin0_%=
Lrdya9_%=:
s_xor_b32 s50, s89, 0
s_lshl_b32 s50, s50, 12
s_add_u32 s62, s80, s50
s_addc_u32 s63, s81, 0
global_load_dwordx4 a[0:3], v24, s[62:63] offset:0 sc1
global_load_dwordx4 a[4:7], v25, s[62:63] offset:0 sc1
global_load_dwordx4 a[8:11], v24, s[62:63] offset:1024 sc1
global_load_dwordx4 a[12:15], v25, s[62:63] offset:1024 sc1
global_load_dwordx4 a[16:19], v24, s[62:63] offset:2048 sc1
global_load_dwordx4 a[20:23], v25, s[62:63] offset:2048 sc1
global_load_dwordx4 a[24:27], v24, s[62:63] offset:3072 sc1
global_load_dwordx4 a[28:31], v25, s[62:63] offset:3072 sc1
s_xor_b32 s50, s89, 1
s_lshl_b32 s50, s50, 12
s_add_u32 s62, s80, s50
s_addc_u32 s63, s81, 0
global_load_dwordx4 a[32:35], v24, s[62:63] offset:0 sc1
global_load_dwordx4 a[36:39], v25, s[62:63] offset:0 sc1
global_load_dwordx4 a[40:43], v24, s[62:63] offset:1024 sc1
global_load_dwordx4 a[44:47], v25, s[62:63] offset:1024 sc1
global_load_dwordx4 a[48:51], v24, s[62:63] offset:2048 sc1
global_load_dwordx4 a[52:55], v25, s[62:63] offset:2048 sc1
global_load_dwordx4 a[56:59], v24, s[62:63] offset:3072 sc1
global_load_dwordx4 a[60:63], v25, s[62:63] offset:3072 sc1
s_waitcnt vmcnt(30)
s_waitcnt lgkmcnt(14)
Mvva 48,112,244
v_add_u32 v241, 65536, v241
Tv 160,241,16384
Tv 162,241,18432
Mvva 48,116,192
ds_read_b32 v5, v3 offset:0
s_add_u32 s44, s44, 1
Mvvv 48,120,252
Tv 164,241,20480
Mvva 48,124,128
ds_write_b32 v2, v4 offset:0
v_add_u32 v4, 1, v4
Mvva 52,112,248
Tv 166,241,22528
Mvva 52,116,196
Tv 168,241,24576
Mvvv 52,120,236
Tv 170,241,26624
Mvva 52,124,132
s_waitcnt lgkmcnt(14)
s_waitcnt lgkmcnt(5)
v_cmp_gt_u32 vcc, s44, v5
s_cbranch_vccnz Lspin1905_%=
Lback1905_%=:
v_add_u32 v240, 4294901760, v240
Mvva 56,128,244
Tv 112,240,49152
Tv 114,240,51200
Mvva 56,132,192
s_xor_b32 s50, s89, 2
s_lshl_b32 s50, s50, 17
s_lshl_b32 s51, s86, 6
s_xor_b32 s51, s51, 128
s_and_b32 s51, s51, 128
s_add_u32 s50, s50, s51
s_add_u32 s60, s24, s50
s_addc_u32 s61, s25, 0
s_add_u32 m0, s34, 16384
s_nop 0
global_load_lds_dwordx4 v244, s[60:61] offset:0
Mvvv 56,136,252
Tv 116,240,53248
Tv 118,240,55296
Mvva 56,140,128
global_load_lds_dwordx4 v245, s[60:61] offset:1024
Mvva 60,128,248
Tv 120,240,57344
Tv 122,240,59392
Mvva 60,132,196
global_load_lds_dwordx4 v246, s[60:61] offset:2048
Mvvv 60,136,236
Tv 124,240,61440
Tv 126,240,63488
Mvva 60,140,132
v_add_u32 v241, 4294901760, v241
Mvva 48,144,136
Tv 128,241,49152
Tv 130,241,51200
Mvva 48,148,144
global_load_lds_dwordx4 v247, s[60:61] offset:3072
Mvva 48,152,152
Tv 132,241,53248
Tv 134,241,55296
Mvva 52,144,140
Tv 136,241,57344
Tv 138,241,59392
Mvva 52,148,148
Tv 140,241,61440
Tv 142,241,63488
Mvva 52,152,156
s_waitcnt lgkmcnt(15)
v_add_u32 v242, 4294901760, v242
Mvva 56,160,136
Tv 144,242,49152
Tv 146,242,51200
Mvva 56,164,144
Tv 148,242,53248
Mvva 56,168,152
Tv 150,242,55296
Mvva 60,160,140
Tv 152,242,57344
Tv 154,242,59392
Mvva 60,164,148
Tv 156,242,61440
Mvva 60,168,156
Tv 158,242,63488
s_waitcnt vmcnt(12)
s_waitcnt lgkmcnt(15)
Zvva 80,112,0
v_add_u32 v243, 4294901760, v243
Tv 160,243,49152
Tv 162,243,51200
Mvva 80,116,8
ds_read_b32 v5, v3 offset:0
s_add_u32 s44, s44, 1
Mvva 80,120,16
Tv 164,243,53248
Tv 166,243,55296
Mvva 80,124,24
ds_write_b32 v2, v4 offset:0
v_add_u32 v4, 1, v4
Zvva 84,112,4
Tv 168,243,57344
v_mov_b32 v32, v48
v_mov_b32 v33, v49
Mvva 84,116,12
Tv 170,243,59392
Tv 172,243,61440
Mvva 84,120,20
Tv 174,243,63488
v_mov_b32 v34, v50
v_mov_b32 v35, v51
Mvva 84,124,28
s_waitcnt lgkmcnt(15)
s_waitcnt lgkmcnt(7)
v_cmp_gt_u32 vcc, s44, v5
s_cbranch_vccnz Lspin1906_%=
Lback1906_%=:
v_add_u32 v240, 65536, v240
v_mov_b32 v36, v52
v_mov_b32 v37, v53
Zvva 88,128,0
Tv 112,240,32768
Tv 114,240,34816
Mvva 88,132,8
s_xor_b32 s50, s89, 2
s_lshl_b32 s50, s50, 17
s_lshl_b32 s51, s86, 6
s_xor_b32 s51, s51, 0
s_and_b32 s51, s51, 128
s_add_u32 s50, s50, s51
s_add_u32 s60, s24, s50
s_addc_u32 s61, s25, 0
s_add_u32 m0, s34, 65536
s_nop 0
global_load_lds_dwordx4 v244, s[60:61] offset:0
Mvva 88,136,16
Tv 116,240,36864
Tv 118,240,38912
Mvva 88,140,24
global_load_lds_dwordx4 v245, s[60:61] offset:1024
Zvva 92,128,4
Tv 120,240,40960
Tv 122,240,43008
Mvva 92,132,12
global_load_lds_dwordx4 v246, s[60:61] offset:2048
Mvva 92,136,20
Tv 124,240,45056
Tv 126,240,47104
Mvva 92,140,28
v_add_u32 v241, 65536, v241
v_mov_b32 v38, v54
)ASM"
R"ASM(
v_mov_b32 v39, v55
Zvva 96,144,0
Tv 128,241,32768
Tv 130,241,34816
Mvva 96,148,8
global_load_lds_dwordx4 v247, s[60:61] offset:3072
Mvva 96,152,16
Tv 132,241,36864
Tv 134,241,38912
Mvva 96,156,24
s_xor_b32 s50, s89, 2
s_lshl_b32 s50, s50, 12
s_add_u32 s62, s80, s50
s_addc_u32 s63, s81, 0
global_load_dwordx4 a[64:67], v24, s[62:63] offset:0 sc1
Zvva 100,144,4
Tv 136,241,40960
Tv 138,241,43008
Mvva 100,148,12
global_load_dwordx4 a[68:71], v25, s[62:63] offset:0 sc1
Mvva 100,152,20
Tv 140,241,45056
Tv 142,241,47104
Mvva 100,156,28
s_waitcnt lgkmcnt(15)
v_add_u32 v242, 65536, v242
v_mov_b32 v40, v56
v_mov_b32 v41, v57
Zvva 104,160,0
Tv 144,242,32768
Tv 146,242,34816
Mvva 104,164,8
global_load_dwordx4 a[72:75], v24, s[62:63] offset:1024 sc1
Mvva 104,168,16
Tv 148,242,36864
v_mov_b32 v42, v58
v_mov_b32 v43, v59
Mvva 104,172,24
global_load_dwordx4 a[76:79], v25, s[62:63] offset:1024 sc1
Zvva 108,160,4
Tv 150,242,38912
v_mov_b32 v44, v60
v_mov_b32 v45, v61
Mvva 108,164,12
Tv 152,242,40960
Tv 154,242,43008
Mvva 108,168,20
Tv 156,242,45056
v_mov_b32 v46, v62
v_mov_b32 v47, v63
Mvva 108,172,28
Tv 158,242,47104
s_waitcnt lgkmcnt(15)
Zvva 48,112,0
v_add_u32 v243, 65536, v243
Tv 160,243,32768
Tv 162,243,34816
Mvva 48,116,8
ds_read_b32 v5, v3 offset:0
s_add_u32 s44, s44, 1
Mvva 48,120,16
Tv 164,243,36864
Tv 166,243,38912
Mvva 48,124,24
ds_write_b32 v2, v4 offset:0
v_add_u32 v4, 1, v4
Zvva 52,112,4
Tv 168,243,40960
Mvva 52,116,12
Tv 170,243,43008
Tv 172,243,45056
Mvva 52,120,20
Tv 174,243,47104
Mvva 52,124,28
s_waitcnt lgkmcnt(15)
s_waitcnt lgkmcnt(7)
v_cmp_gt_u32 vcc, s44, v5
s_cbranch_vccnz Lspin1907_%=
Lback1907_%=:
Zvva 56,128,0
Tv 112,240,49152
Tv 114,240,51200
Mvva 56,132,8
global_load_dwordx4 a[80:83], v24, s[62:63] offset:2048 sc1
Mvva 56,136,16
Tv 116,240,53248
Tv 118,240,55296
Mvva 56,140,24
global_load_dwordx4 a[84:87], v25, s[62:63] offset:2048 sc1
Zvva 60,128,4
Tv 120,240,57344
Tv 122,240,59392
Mvva 60,132,12
global_load_dwordx4 a[88:91], v24, s[62:63] offset:3072 sc1
Mvva 60,136,20
Tv 124,240,61440
Tv 126,240,63488
Mvva 60,140,28
Zvva 64,144,0
Tv 128,241,49152
Tv 130,241,51200
Mvva 64,148,8
global_load_dwordx4 a[92:95], v25, s[62:63] offset:3072 sc1
Mvva 64,152,16
Tv 132,241,53248
Tv 134,241,55296
Mvva 64,156,24
s_xor_b32 s50, s89, 3
s_lshl_b32 s50, s50, 17
s_lshl_b32 s51, s86, 6
s_xor_b32 s51, s51, 128
s_and_b32 s51, s51, 128
s_add_u32 s50, s50, s51
s_add_u32 s60, s24, s50
s_addc_u32 s61, s25, 0
s_add_u32 m0, s34, 32768
s_nop 0
global_load_lds_dwordx4 v244, s[60:61] offset:0
Zvva 68,144,4
Tv 136,241,57344
Tv 138,241,59392
Mvva 68,148,12
global_load_lds_dwordx4 v245, s[60:61] offset:1024
Mvva 68,152,20
Tv 140,241,61440
Tv 142,241,63488
Mvva 68,156,28
s_waitcnt lgkmcnt(15)
Zvva 72,160,0
Tv 144,242,49152
Tv 146,242,51200
Mvva 72,164,8
global_load_lds_dwordx4 v246, s[60:61] offset:2048
Mvva 72,168,16
Tv 148,242,53248
Mvva 72,172,24
global_load_lds_dwordx4 v247, s[60:61] offset:3072
Zvva 76,160,4
Tv 150,242,55296
Mvva 76,164,12
Tv 152,242,57344
Tv 154,242,59392
Mvva 76,168,20
Tv 156,242,61440
Mvva 76,172,28
Tv 158,242,63488
s_waitcnt vmcnt(16)
s_waitcnt lgkmcnt(15)
Mvva 80,112,32
Tv 160,243,49152
Tv 162,243,51200
Mvva 80,116,40
ds_read_b32 v5, v3 offset:0
s_add_u32 s44, s44, 1
Mvva 80,120,48
Tv 164,243,53248
Tv 166,243,55296
Mvva 80,124,56
ds_write_b32 v2, v4 offset:0
v_add_u32 v4, 1, v4
Mvva 84,112,36
Tv 168,243,57344
Mvva 84,116,44
Tv 170,243,59392
Tv 172,243,61440
Mvva 84,120,52
Tv 174,243,63488
Mvva 84,124,60
s_waitcnt lgkmcnt(15)
s_waitcnt lgkmcnt(7)
v_cmp_gt_u32 vcc, s44, v5
s_cbranch_vccnz Lspin1908_%=
Lback1908_%=:
v_add_u32 v240, 4294901760, v240
Mvva 88,128,32
Tv 112,240,0
Tv 114,240,2048
Mvva 88,132,40
s_and_b32 s50, s40, 28
s_xor_b32 s50, s50, 16
s_lshl_b64 exec, 15, s50
global_load_dword v221, v220, s[28:29] sc1
s_mov_b64 exec, -1
Mvva 88,136,48
Tv 116,240,4096
Tv 118,240,6144
Mvva 88,140,56
s_xor_b32 s50, s89, 3
s_lshl_b32 s50, s50, 17
s_lshl_b32 s51, s86, 6
s_xor_b32 s51, s51, 0
s_and_b32 s51, s51, 128
s_add_u32 s50, s50, s51
s_add_u32 s60, s24, s50
s_addc_u32 s61, s25, 0
s_add_u32 m0, s34, 81920
s_nop 0
global_load_lds_dwordx4 v244, s[60:61] offset:0
Mvva 92,128,36
Tv 120,240,8192
)ASM"
R"ASM(
Tv 122,240,10240
Mvva 92,132,44
global_load_lds_dwordx4 v245, s[60:61] offset:1024
Mvva 92,136,52
Tv 124,240,12288
Tv 126,240,14336
Mvva 92,140,60
v_add_u32 v241, 4294901760, v241
Mvva 96,144,32
Tv 128,241,0
Tv 130,241,2048
Mvva 96,148,40
global_load_lds_dwordx4 v246, s[60:61] offset:2048
Mvva 96,152,48
Tv 132,241,4096
Tv 134,241,6144
Mvva 96,156,56
global_load_lds_dwordx4 v247, s[60:61] offset:3072
Mvva 100,144,36
Tv 136,241,8192
Tv 138,241,10240
Mvva 100,148,44
s_xor_b32 s50, s89, 3
s_lshl_b32 s50, s50, 12
s_add_u32 s62, s80, s50
s_addc_u32 s63, s81, 0
global_load_dwordx4 a[96:99], v24, s[62:63] offset:0 sc1
Mvva 100,152,52
Tv 140,241,12288
Tv 142,241,14336
Mvva 100,156,60
s_waitcnt lgkmcnt(15)
v_add_u32 v242, 4294901760, v242
Mvva 104,160,32
Tv 144,242,0
Tv 146,242,2048
Mvva 104,164,40
global_load_dwordx4 a[100:103], v25, s[62:63] offset:0 sc1
Mvva 104,168,48
Tv 148,242,4096
Tv 150,242,6144
Mvva 104,172,56
global_load_dwordx4 a[104:107], v24, s[62:63] offset:1024 sc1
Mvva 108,160,36
Tv 152,242,8192
Mvva 108,164,44
global_load_dwordx4 a[108:111], v25, s[62:63] offset:1024 sc1
Mvva 108,168,52
Tv 154,242,10240
Tv 156,242,12288
Mvva 108,172,60
Tv 158,242,14336
s_waitcnt vmcnt(21)
s_waitcnt lgkmcnt(15)
Mvva 48,112,32
v_add_u32 v243, 4294901760, v243
Tv 160,243,0
Tv 162,243,2048
Mvva 48,116,40
ds_read_b32 v5, v3 offset:0
s_add_u32 s44, s44, 1
Mvva 48,120,48
Tv 164,243,4096
Tv 166,243,6144
Mvva 48,124,56
ds_write_b32 v2, v4 offset:0
v_add_u32 v4, 1, v4
Mvva 52,112,36
Tv 168,243,8192
Mvva 52,116,44
Tv 170,243,10240
Tv 172,243,12288
Mvva 52,120,52
Tv 174,243,14336
Mvva 52,124,60
s_waitcnt lgkmcnt(15)
s_waitcnt lgkmcnt(7)
v_cmp_gt_u32 vcc, s44, v5
s_cbranch_vccnz Lspin1909_%=
Lback1909_%=:
Mvva 56,128,32
Tv 112,240,16384
Tv 114,240,18432
Mvva 56,132,40
global_load_dwordx4 a[112:115], v24, s[62:63] offset:2048 sc1
Mvva 56,136,48
Tv 116,240,20480
Tv 118,240,22528
Mvva 56,140,56
global_load_dwordx4 a[116:119], v25, s[62:63] offset:2048 sc1
Mvva 60,128,36
Tv 120,240,24576
Tv 122,240,26624
Mvva 60,132,44
global_load_dwordx4 a[120:123], v24, s[62:63] offset:3072 sc1
Mvva 60,136,52
Tv 124,240,28672
Tv 126,240,30720
Mvva 60,140,60
Mvva 64,144,32
Tv 128,241,16384
Tv 130,241,18432
Mvva 64,148,40
global_load_dwordx4 a[124:127], v25, s[62:63] offset:3072 sc1
Mvva 64,152,48
Tv 132,241,20480
Tv 134,241,22528
Mvva 64,156,56
s_xor_b32 s50, s89, 16
s_lshl_b32 s50, s50, 17
s_lshl_b32 s51, s86, 6
s_xor_b32 s51, s51, 128
s_and_b32 s51, s51, 128
s_add_u32 s50, s50, s51
s_add_u32 s60, s24, s50
s_addc_u32 s61, s25, 0
s_add_u32 m0, s34, 49152
s_nop 0
global_load_lds_dwordx4 v244, s[60:61] offset:0
Mvva 68,144,36
Tv 136,241,24576
Tv 138,241,26624
Mvva 68,148,44
global_load_lds_dwordx4 v245, s[60:61] offset:1024
Mvva 68,152,52
Tv 140,241,28672
Tv 142,241,30720
Mvva 68,156,60
s_waitcnt lgkmcnt(15)
Mvva 72,160,32
Tv 144,242,16384
Tv 146,242,18432
Mvva 72,164,40
global_load_lds_dwordx4 v246, s[60:61] offset:2048
Mvva 72,168,48
Tv 148,242,20480
Mvva 72,172,56
global_load_lds_dwordx4 v247, s[60:61] offset:3072
Mvva 76,160,36
Tv 150,242,22528
Mvva 76,164,44
Tv 152,242,24576
Tv 154,242,26624
Mvva 76,168,52
Tv 156,242,28672
Mvva 76,172,60
Tv 158,242,30720
s_waitcnt vmcnt(16)
s_waitcnt lgkmcnt(15)
Mvva 80,112,64
Tv 160,243,16384
Tv 162,243,18432
Mvva 80,116,72
ds_read_b32 v5, v3 offset:0
s_add_u32 s44, s44, 1
Mvva 80,120,80
Tv 164,243,20480
Tv 166,243,22528
Mvva 80,124,88
ds_write_b32 v2, v4 offset:0
v_add_u32 v4, 1, v4
Mvva 84,112,68
Tv 168,243,24576
Tv 170,243,26624
Mvva 84,116,76
v_cmp_eq_u32 s[70:71], s47, v221
s_and_b32 s50, s40, 28
s_xor_b32 s50, s50, 16
s_lshl_b32 s72, 15, s50
s_andn2_b32 s50, s72, s70
s_cbranch_scc0 Lrdyb9_10_%=
s_call_b64 s[76:77], Lwait_%=
Lrdyb9_10_%=:
Mvva 84,120,84
Tv 172,243,28672
Tv 174,243,30720
Mvva 84,124,92
s_waitcnt lgkmcnt(15)
s_waitcnt lgkmcnt(7)
v_cmp_gt_u32 vcc, s44, v5
s_cbranch_vccnz Lspin1910_%=
Lback1910_%=:
v_add_u32 v240, 65536, v240
Mvva 88,128,64
Tv 112,240,0
Tv 114,240,2048
Mvva 88,132,72
s_xor_b32 s50, s89, 16
s_lshl_b32 s50, s50, 17
s_lshl_b32 s51, s86, 6
s_xor_b32 s51, s51, 0
s_and_b32 s51, s51, 128
s_add_u32 s50, s50, s51
s_add_u32 s60, s24, s50
s_addc_u32 s61, s25, 0
s_add_u32 m0, s34, 98304
s_nop 0
global_load_lds_dwordx4 v244, s[60:61] offset:0
Mvva 88,136,80
Tv 116,240,4096
)ASM"
R"ASM(
Tv 118,240,6144
Mvva 88,140,88
global_load_lds_dwordx4 v245, s[60:61] offset:1024
Mvva 92,128,68
Tv 120,240,8192
Tv 122,240,10240
Mvva 92,132,76
global_load_lds_dwordx4 v246, s[60:61] offset:2048
Mvva 92,136,84
Tv 124,240,12288
Tv 126,240,14336
Mvva 92,140,92
v_add_u32 v241, 65536, v241
Mvva 96,144,64
Tv 128,241,0
Tv 130,241,2048
Mvva 96,148,72
global_load_lds_dwordx4 v247, s[60:61] offset:3072
Mvva 96,152,80
Tv 132,241,4096
Tv 134,241,6144
Mvva 96,156,88
s_xor_b32 s50, s89, 16
s_lshl_b32 s50, s50, 12
s_add_u32 s62, s80, s50
s_addc_u32 s63, s81, 0
global_load_dwordx4 a[128:131], v24, s[62:63] offset:0 sc1
Mvva 100,144,68
Tv 136,241,8192
Tv 138,241,10240
Mvva 100,148,76
global_load_dwordx4 a[132:135], v25, s[62:63] offset:0 sc1
Mvva 100,152,84
Tv 140,241,12288
Tv 142,241,14336
Mvva 100,156,92
s_waitcnt lgkmcnt(15)
v_add_u32 v242, 65536, v242
Mvva 104,160,64
Tv 144,242,0
Tv 146,242,2048
Mvva 104,164,72
global_load_dwordx4 a[136:139], v24, s[62:63] offset:1024 sc1
Mvva 104,168,80
Tv 148,242,4096
Mvva 104,172,88
global_load_dwordx4 a[140:143], v25, s[62:63] offset:1024 sc1
Mvva 108,160,68
Tv 150,242,6144
Mvva 108,164,76
Tv 152,242,8192
Tv 154,242,10240
Mvva 108,168,84
Tv 156,242,12288
Mvva 108,172,92
Tv 158,242,14336
s_waitcnt vmcnt(20)
s_waitcnt lgkmcnt(15)
Mvva 48,112,64
v_add_u32 v243, 65536, v243
Tv 160,243,0
Tv 162,243,2048
Mvva 48,116,72
ds_read_b32 v5, v3 offset:0
s_add_u32 s44, s44, 1
Mvva 48,120,80
Tv 164,243,4096
Tv 166,243,6144
Mvva 48,124,88
ds_write_b32 v2, v4 offset:0
v_add_u32 v4, 1, v4
Mvva 52,112,68
Tv 168,243,8192
Mvva 52,116,76
Tv 170,243,10240
Tv 172,243,12288
Mvva 52,120,84
Tv 174,243,14336
Mvva 52,124,92
s_waitcnt lgkmcnt(15)
s_waitcnt lgkmcnt(7)
v_cmp_gt_u32 vcc, s44, v5
s_cbranch_vccnz Lspin1911_%=
Lback1911_%=:
v_add_u32 v240, 4294901760, v240
Mvva 56,128,64
Tv 112,240,32768
Tv 114,240,34816
Mvva 56,132,72
global_load_dwordx4 a[144:147], v24, s[62:63] offset:2048 sc1
Mvva 56,136,80
Tv 116,240,36864
Tv 118,240,38912
Mvva 56,140,88
global_load_dwordx4 a[148:151], v25, s[62:63] offset:2048 sc1
Mvva 60,128,68
Tv 120,240,40960
Tv 122,240,43008
Mvva 60,132,76
global_load_dwordx4 a[152:155], v24, s[62:63] offset:3072 sc1
Mvva 60,136,84
Tv 124,240,45056
Tv 126,240,47104
Mvva 60,140,92
v_add_u32 v241, 4294901760, v241
Mvva 64,144,64
Tv 128,241,32768
Tv 130,241,34816
Mvva 64,148,72
global_load_dwordx4 a[156:159], v25, s[62:63] offset:3072 sc1
Mvva 64,152,80
Tv 132,241,36864
Tv 134,241,38912
Mvva 64,156,88
s_xor_b32 s50, s89, 17
s_lshl_b32 s50, s50, 17
s_lshl_b32 s51, s86, 6
s_xor_b32 s51, s51, 128
s_and_b32 s51, s51, 128
s_add_u32 s50, s50, s51
s_add_u32 s60, s24, s50
s_addc_u32 s61, s25, 0
s_add_u32 m0, s34, 114688
s_nop 0
global_load_lds_dwordx4 v244, s[60:61] offset:0
Mvva 68,144,68
Tv 136,241,40960
Tv 138,241,43008
Mvva 68,148,76
global_load_lds_dwordx4 v245, s[60:61] offset:1024
Mvva 68,152,84
Tv 140,241,45056
Tv 142,241,47104
Mvva 68,156,92
s_waitcnt lgkmcnt(15)
v_add_u32 v242, 4294901760, v242
Mvva 72,160,64
Tv 144,242,32768
Tv 146,242,34816
Mvva 72,164,72
global_load_lds_dwordx4 v246, s[60:61] offset:2048
Mvva 72,168,80
Tv 148,242,36864
Mvva 72,172,88
global_load_lds_dwordx4 v247, s[60:61] offset:3072
Mvva 76,160,68
Tv 150,242,38912
Mvva 76,164,76
Tv 152,242,40960
Tv 154,242,43008
Mvva 76,168,84
Tv 156,242,45056
Mvva 76,172,92
Tv 158,242,47104
s_waitcnt vmcnt(16)
s_waitcnt lgkmcnt(15)
Mvva 80,112,96
v_add_u32 v243, 4294901760, v243
Tv 160,243,32768
Tv 162,243,34816
Mvva 80,116,104
ds_read_b32 v5, v3 offset:0
s_add_u32 s44, s44, 1
Mvva 80,120,112
Tv 164,243,36864
Tv 166,243,38912
Mvva 80,124,120
ds_write_b32 v2, v4 offset:0
v_add_u32 v4, 1, v4
Mvva 84,112,100
Tv 168,243,40960
Mvva 84,116,108
Tv 170,243,43008
Tv 172,243,45056
Mvva 84,120,116
Tv 174,243,47104
Mvva 84,124,124
s_waitcnt lgkmcnt(15)
s_waitcnt lgkmcnt(7)
v_cmp_gt_u32 vcc, s44, v5
s_cbranch_vccnz Lspin1912_%=
Lback1912_%=:
v_add_u32 v240, 65536, v240
Mvva 88,128,96
Tv 112,240,16384
Tv 114,240,18432
Mvva 88,132,104
s_xor_b32 s50, s89, 17
s_lshl_b32 s50, s50, 17
s_lshl_b32 s51, s86, 6
s_xor_b32 s51, s51, 0
s_and_b32 s51, s51, 128
s_add_u32 s50, s50, s51
s_add_u32 s60, s24, s50
s_addc_u32 s61, s25, 0
s_add_u32 m0, s34, 0
s_nop 0
global_load_lds_dwordx4 v244, s[60:61] offset:0
Mvva 88,136,112
Tv 116,240,20480
)ASM"
R"ASM(
Tv 118,240,22528
Mvva 88,140,120
global_load_lds_dwordx4 v245, s[60:61] offset:1024
Mvva 92,128,100
Tv 120,240,24576
Tv 122,240,26624
Mvva 92,132,108
global_load_lds_dwordx4 v246, s[60:61] offset:2048
Mvva 92,136,116
Tv 124,240,28672
Tv 126,240,30720
Mvva 92,140,124
v_add_u32 v241, 65536, v241
Mvva 96,144,96
Tv 128,241,16384
Tv 130,241,18432
Mvva 96,148,104
global_load_lds_dwordx4 v247, s[60:61] offset:3072
Mvva 96,152,112
Tv 132,241,20480
Tv 134,241,22528
Mvva 96,156,120
s_xor_b32 s50, s89, 17
s_lshl_b32 s50, s50, 12
s_add_u32 s62, s80, s50
s_addc_u32 s63, s81, 0
global_load_dwordx4 a[160:163], v24, s[62:63] offset:0 sc1
Mvva 100,144,100
Tv 136,241,24576
Tv 138,241,26624
Mvva 100,148,108
global_load_dwordx4 a[164:167], v25, s[62:63] offset:0 sc1
Mvva 100,152,116
Tv 140,241,28672
Tv 142,241,30720
Mvva 100,156,124
s_waitcnt lgkmcnt(15)
v_add_u32 v242, 65536, v242
Mvva 104,160,96
Tv 144,242,16384
Tv 146,242,18432
Mvva 104,164,104
global_load_dwordx4 a[168:171], v24, s[62:63] offset:1024 sc1
Mvva 104,168,112
Tv 148,242,20480
Mvva 104,172,120
global_load_dwordx4 a[172:175], v25, s[62:63] offset:1024 sc1
Mvva 108,160,100
Tv 150,242,22528
Mvva 108,164,108
Tv 152,242,24576
Tv 154,242,26624
Mvva 108,168,116
Tv 156,242,28672
Mvva 108,172,124
Tv 158,242,30720
s_waitcnt vmcnt(20)
s_waitcnt lgkmcnt(15)
Mvva 48,112,96
v_add_u32 v243, 65536, v243
Tv 160,243,16384
Tv 162,243,18432
Mvva 48,116,104
ds_read_b32 v5, v3 offset:0
s_add_u32 s44, s44, 1
Mvva 48,120,112
Tv 164,243,20480
Tv 166,243,22528
Mvva 48,124,120
ds_write_b32 v2, v4 offset:0
v_add_u32 v4, 1, v4
Mvva 52,112,100
Tv 168,243,24576
Mvva 52,116,108
Tv 170,243,26624
Tv 172,243,28672
Mvva 52,120,116
Tv 174,243,30720
Mvva 52,124,124
s_waitcnt lgkmcnt(15)
s_waitcnt lgkmcnt(7)
v_cmp_gt_u32 vcc, s44, v5
s_cbranch_vccnz Lspin1913_%=
Lback1913_%=:
v_add_u32 v240, 4294901760, v240
Mvva 56,128,96
Tv 112,240,49152
Tv 114,240,51200
Mvva 56,132,104
global_load_dwordx4 a[176:179], v24, s[62:63] offset:2048 sc1
Mvva 56,136,112
Tv 116,240,53248
Tv 118,240,55296
Mvva 56,140,120
global_load_dwordx4 a[180:183], v25, s[62:63] offset:2048 sc1
Mvva 60,128,100
Tv 120,240,57344
Tv 122,240,59392
Mvva 60,132,108
global_load_dwordx4 a[184:187], v24, s[62:63] offset:3072 sc1
Mvva 60,136,116
Tv 124,240,61440
Tv 126,240,63488
Mvva 60,140,124
v_add_u32 v241, 4294901760, v241
Mvva 64,144,96
Tv 128,241,49152
Tv 130,241,51200
Mvva 64,148,104
global_load_dwordx4 a[188:191], v25, s[62:63] offset:3072 sc1
Mvva 64,152,112
Tv 132,241,53248
Tv 134,241,55296
Mvva 64,156,120
s_xor_b32 s50, s89, 18
s_lshl_b32 s50, s50, 17
s_lshl_b32 s51, s86, 6
s_xor_b32 s51, s51, 128
s_and_b32 s51, s51, 128
s_add_u32 s50, s50, s51
s_add_u32 s60, s24, s50
s_addc_u32 s61, s25, 0
s_add_u32 m0, s34, 16384
s_nop 0
global_load_lds_dwordx4 v244, s[60:61] offset:0
Mvva 68,144,100
Tv 136,241,57344
Tv 138,241,59392
Mvva 68,148,108
global_load_lds_dwordx4 v245, s[60:61] offset:1024
Mvva 68,152,116
Tv 140,241,61440
Tv 142,241,63488
Mvva 68,156,124
s_waitcnt lgkmcnt(15)
v_add_u32 v242, 4294901760, v242
Mvva 72,160,96
Tv 144,242,49152
Tv 146,242,51200
Mvva 72,164,104
global_load_lds_dwordx4 v246, s[60:61] offset:2048
Mvva 72,168,112
Tv 148,242,53248
Mvva 72,172,120
global_load_lds_dwordx4 v247, s[60:61] offset:3072
Mvva 76,160,100
Tv 150,242,55296
Mvva 76,164,108
Tv 152,242,57344
Tv 154,242,59392
Mvva 76,168,116
Tv 156,242,61440
Mvva 76,172,124
Tv 158,242,63488
s_waitcnt vmcnt(16)
s_waitcnt lgkmcnt(15)
Mvva 80,112,128
v_add_u32 v243, 4294901760, v243
Tv 160,243,49152
Tv 162,243,51200
Mvva 80,116,136
ds_read_b32 v5, v3 offset:0
s_add_u32 s44, s44, 1
Mvva 80,120,144
Tv 164,243,53248
Tv 166,243,55296
Mvva 80,124,152
ds_write_b32 v2, v4 offset:0
v_add_u32 v4, 1, v4
Mvva 84,112,132
Tv 168,243,57344
Mvva 84,116,140
Tv 170,243,59392
Tv 172,243,61440
Mvva 84,120,148
Tv 174,243,63488
Mvva 84,124,156
s_waitcnt lgkmcnt(15)
s_waitcnt lgkmcnt(7)
v_cmp_gt_u32 vcc, s44, v5
s_cbranch_vccnz Lspin1914_%=
Lback1914_%=:
v_add_u32 v240, 65536, v240
Mvva 88,128,128
Tv 112,240,32768
Tv 114,240,34816
Mvva 88,132,136
s_xor_b32 s50, s89, 18
s_lshl_b32 s50, s50, 17
s_lshl_b32 s51, s86, 6
s_xor_b32 s51, s51, 0
s_and_b32 s51, s51, 128
s_add_u32 s50, s50, s51
s_add_u32 s60, s24, s50
s_addc_u32 s61, s25, 0
s_add_u32 m0, s34, 65536
s_nop 0
global_load_lds_dwordx4 v244, s[60:61] offset:0
Mvva 88,136,144
Tv 116,240,36864
)ASM"
R"ASM(
Tv 118,240,38912
Mvva 88,140,152
global_load_lds_dwordx4 v245, s[60:61] offset:1024
Mvva 92,128,132
Tv 120,240,40960
Tv 122,240,43008
Mvva 92,132,140
global_load_lds_dwordx4 v246, s[60:61] offset:2048
Mvva 92,136,148
Tv 124,240,45056
Tv 126,240,47104
Mvva 92,140,156
v_add_u32 v241, 65536, v241
Mvva 96,144,128
Tv 128,241,32768
Tv 130,241,34816
Mvva 96,148,136
global_load_lds_dwordx4 v247, s[60:61] offset:3072
Mvva 96,152,144
Tv 132,241,36864
Tv 134,241,38912
Mvva 96,156,152
s_xor_b32 s50, s89, 18
s_lshl_b32 s50, s50, 12
s_add_u32 s62, s80, s50
s_addc_u32 s63, s81, 0
global_load_dwordx4 a[192:195], v24, s[62:63] offset:0 sc1
Mvva 100,144,132
Tv 136,241,40960
Tv 138,241,43008
Mvva 100,148,140
global_load_dwordx4 a[196:199], v25, s[62:63] offset:0 sc1
Mvva 100,152,148
Tv 140,241,45056
Tv 142,241,47104
Mvva 100,156,156
s_waitcnt lgkmcnt(15)
v_add_u32 v242, 65536, v242
Mvva 104,160,128
Tv 144,242,32768
Tv 146,242,34816
Mvva 104,164,136
global_load_dwordx4 a[200:203], v24, s[62:63] offset:1024 sc1
Mvva 104,168,144
Tv 148,242,36864
Mvva 104,172,152
global_load_dwordx4 a[204:207], v25, s[62:63] offset:1024 sc1
Mvva 108,160,132
Tv 150,242,38912
Mvva 108,164,140
Tv 152,242,40960
Tv 154,242,43008
Mvva 108,168,148
Tv 156,242,45056
Mvva 108,172,156
Tv 158,242,47104
s_waitcnt vmcnt(20)
s_waitcnt lgkmcnt(15)
Mvva 48,112,128
v_add_u32 v243, 65536, v243
Tv 160,243,32768
Tv 162,243,34816
Mvva 48,116,136
ds_read_b32 v5, v3 offset:0
s_add_u32 s44, s44, 1
Mvva 48,120,144
Tv 164,243,36864
Tv 166,243,38912
Mvva 48,124,152
ds_write_b32 v2, v4 offset:0
v_add_u32 v4, 1, v4
Mvva 52,112,132
Tv 168,243,40960
Mvva 52,116,140
Tv 170,243,43008
Tv 172,243,45056
Mvva 52,120,148
Tv 174,243,47104
Mvva 52,124,156
s_waitcnt lgkmcnt(15)
s_waitcnt lgkmcnt(7)
v_cmp_gt_u32 vcc, s44, v5
s_cbranch_vccnz Lspin1915_%=
Lback1915_%=:
Mvva 56,128,128
Tv 112,240,49152
Tv 114,240,51200
Mvva 56,132,136
global_load_dwordx4 a[208:211], v24, s[62:63] offset:2048 sc1
Mvva 56,136,144
Tv 116,240,53248
Tv 118,240,55296
Mvva 56,140,152
global_load_dwordx4 a[212:215], v25, s[62:63] offset:2048 sc1
Mvva 60,128,132
Tv 120,240,57344
Tv 122,240,59392
Mvva 60,132,140
global_load_dwordx4 a[216:219], v24, s[62:63] offset:3072 sc1
Mvva 60,136,148
Tv 124,240,61440
Tv 126,240,63488
Mvva 60,140,156
global_load_dwordx4 a[220:223], v25, s[62:63] offset:3072 sc1
Mvva 64,144,128
Tv 128,241,49152
Tv 130,241,51200
Mvva 64,148,136
s_xor_b32 s50, s89, 19
s_lshl_b32 s50, s50, 17
s_lshl_b32 s51, s86, 6
s_xor_b32 s51, s51, 128
s_and_b32 s51, s51, 128
s_add_u32 s50, s50, s51
s_add_u32 s60, s24, s50
s_addc_u32 s61, s25, 0
s_add_u32 m0, s34, 32768
s_nop 0
global_load_lds_dwordx4 v244, s[60:61] offset:0
Mvva 64,152,144
Tv 132,241,53248
Tv 134,241,55296
Mvva 64,156,152
global_load_lds_dwordx4 v245, s[60:61] offset:1024
Mvva 68,144,132
Tv 136,241,57344
Tv 138,241,59392
Mvva 68,148,140
global_load_lds_dwordx4 v246, s[60:61] offset:2048
Mvva 68,152,148
Tv 140,241,61440
Tv 142,241,63488
Mvva 68,156,156
s_waitcnt lgkmcnt(15)
global_load_lds_dwordx4 v247, s[60:61] offset:3072
Mvva 72,160,128
Tv 144,242,49152
Tv 146,242,51200
Mvva 72,164,136
s_lshl_b32 s50, s86, 16
s_add_u32 s50, s50, 0
s_add_u32 s64, s82, s50
s_addc_u32 s65, s83, 0
global_load_dwordx4 v[176:179], v24, s[64:65] offset:2048 sc1
Mvva 72,168,144
Tv 148,242,53248
Tv 150,242,55296
Mvva 72,172,152
global_load_dwordx4 v[180:183], v24, s[64:65] offset:3072 sc1
Mvva 76,160,132
Tv 152,242,57344
Tv 154,242,59392
Mvva 76,164,140
s_lshl_b32 s50, s86, 16
s_add_u32 s50, s50, 4096
s_add_u32 s64, s82, s50
s_addc_u32 s65, s83, 0
global_load_dwordx4 v[184:187], v24, s[64:65] offset:2048 sc1
Mvva 76,168,148
Tv 156,242,61440
Tv 158,242,63488
Mvva 76,172,156
global_load_dwordx4 v[188:191], v24, s[64:65] offset:3072 sc1
s_waitcnt vmcnt(20)
s_waitcnt lgkmcnt(15)
Mvva 80,112,160
Tv 160,243,49152
Tv 162,243,51200
Mvva 80,116,168
ds_read_b32 v5, v3 offset:0
s_add_u32 s44, s44, 1
Mvva 80,120,176
Tv 164,243,53248
Tv 166,243,55296
Mvva 80,124,184
ds_write_b32 v2, v4 offset:0
v_add_u32 v4, 1, v4
Mvva 84,112,164
Tv 168,243,57344
Mvva 84,116,172
Tv 170,243,59392
Tv 172,243,61440
Mvva 84,120,180
Tv 174,243,63488
Mvva 84,124,188
s_waitcnt lgkmcnt(15)
s_waitcnt lgkmcnt(7)
v_cmp_gt_u32 vcc, s44, v5
s_cbranch_vccnz Lspin1916_%=
Lback1916_%=:
v_add_u32 v240, 4294901760, v240
Mvva 88,128,160
Tv 112,240,0
Tv 114,240,2048
Mvva 88,132,168
s_and_b32 s50, s40, 28
s_xor_b32 s50, s50, 20
s_lshl_b64 exec, 15, s50
global_load_dword v221, v220, s[28:29] sc1
s_mov_b64 exec, -1
)ASM"
R"ASM(
Mvva 88,136,176
Tv 116,240,4096
Tv 118,240,6144
Mvva 88,140,184
s_xor_b32 s50, s89, 20
s_lshl_b32 s50, s50, 17
s_lshl_b32 s51, s86, 6
s_xor_b32 s51, s51, 128
s_and_b32 s51, s51, 128
s_add_u32 s50, s50, s51
s_add_u32 s60, s24, s50
s_addc_u32 s61, s25, 0
s_add_u32 m0, s34, 81920
s_nop 0
global_load_lds_dwordx4 v244, s[60:61] offset:0
Mvva 92,128,164
Tv 120,240,8192
Tv 122,240,10240
Mvva 92,132,172
global_load_lds_dwordx4 v245, s[60:61] offset:1024
Mvva 92,136,180
Tv 124,240,12288
Tv 126,240,14336
Mvva 92,140,188
global_load_lds_dwordx4 v246, s[60:61] offset:2048
v_add_u32 v241, 4294901760, v241
Mvva 96,144,160
Tv 128,241,0
Tv 130,241,2048
Mvva 96,148,168
global_load_lds_dwordx4 v247, s[60:61] offset:3072
Mvva 96,152,176
Tv 132,241,4096
Tv 134,241,6144
Mvva 96,156,184
s_xor_b32 s50, s89, 19
s_lshl_b32 s50, s50, 12
s_add_u32 s62, s80, s50
s_addc_u32 s63, s81, 0
global_load_dwordx4 a[224:227], v24, s[62:63] offset:0 sc1
Mvva 100,144,164
Tv 136,241,8192
Tv 138,241,10240
Mvva 100,148,172
global_load_dwordx4 a[228:231], v25, s[62:63] offset:0 sc1
Mvva 100,152,180
Tv 140,241,12288
Tv 142,241,14336
Mvva 100,156,188
s_waitcnt lgkmcnt(15)
v_add_u32 v242, 4294901760, v242
Mvva 104,160,160
Tv 144,242,0
Tv 146,242,2048
Mvva 104,164,168
global_load_dwordx4 a[232:235], v24, s[62:63] offset:1024 sc1
Mvva 104,168,176
Tv 148,242,4096
Tv 150,242,6144
Mvva 104,172,184
global_load_dwordx4 a[236:239], v25, s[62:63] offset:1024 sc1
Mvva 108,160,164
Tv 152,242,8192
Tv 154,242,10240
Mvva 108,164,172
s_lshl_b32 s50, s86, 16
s_add_u32 s50, s50, 8192
s_add_u32 s64, s82, s50
s_addc_u32 s65, s83, 0
global_load_dwordx4 v[192:195], v24, s[64:65] offset:2048 sc1
Mvva 108,168,180
Tv 156,242,12288
Tv 158,242,14336
Mvva 108,172,188
global_load_dwordx4 v[196:199], v24, s[64:65] offset:3072 sc1
s_waitcnt vmcnt(27)
s_waitcnt lgkmcnt(15)
Mvva 48,112,160
v_add_u32 v243, 4294901760, v243
Tv 160,243,0
Tv 162,243,2048
Mvva 48,116,168
ds_read_b32 v5, v3 offset:0
s_add_u32 s44, s44, 1
Mvva 48,120,176
Tv 164,243,4096
Tv 166,243,6144
Mvva 48,124,184
ds_write_b32 v2, v4 offset:0
v_add_u32 v4, 1, v4
Mvva 52,112,164
Tv 168,243,8192
Mvva 52,116,172
Tv 170,243,10240
Tv 172,243,12288
Mvva 52,120,180
Tv 174,243,14336
Mvva 52,124,188
s_waitcnt lgkmcnt(15)
s_waitcnt lgkmcnt(7)
v_cmp_gt_u32 vcc, s44, v5
s_cbranch_vccnz Lspin1917_%=
Lback1917_%=:
Mvva 56,128,160
Tv 112,240,16384
Tv 114,240,18432
Mvva 56,132,168
global_load_dwordx4 a[240:243], v24, s[62:63] offset:2048 sc1
Mvva 56,136,176
Tv 116,240,20480
Tv 118,240,22528
Mvva 56,140,184
global_load_dwordx4 a[244:247], v25, s[62:63] offset:2048 sc1
Mvva 60,128,164
Tv 120,240,24576
Tv 122,240,26624
Mvva 60,132,172
global_load_dwordx4 v[208:211], v24, s[62:63] offset:3072 sc1
Mvva 60,136,180
Tv 124,240,28672
Tv 126,240,30720
Mvva 60,140,188
Mvva 64,144,160
Tv 128,241,16384
Tv 130,241,18432
Mvva 64,148,168
global_load_dwordx4 v[212:215], v25, s[62:63] offset:3072 sc1
Mvva 64,152,176
Tv 132,241,20480
Tv 134,241,22528
Mvva 64,156,184
s_xor_b32 s50, s89, 19
s_lshl_b32 s50, s50, 17
s_lshl_b32 s51, s86, 6
s_xor_b32 s51, s51, 0
s_and_b32 s51, s51, 128
s_add_u32 s50, s50, s51
s_add_u32 s60, s24, s50
s_addc_u32 s61, s25, 0
s_add_u32 m0, s34, 49152
s_nop 0
global_load_lds_dwordx4 v244, s[60:61] offset:0
Mvva 68,144,164
Tv 136,241,24576
Tv 138,241,26624
Mvva 68,148,172
global_load_lds_dwordx4 v245, s[60:61] offset:1024
Mvva 68,152,180
Tv 140,241,28672
Tv 142,241,30720
Mvva 68,156,188
s_waitcnt lgkmcnt(15)
Mvva 72,160,160
Tv 144,242,16384
Tv 146,242,18432
Mvva 72,164,168
global_load_lds_dwordx4 v246, s[60:61] offset:2048
Mvva 72,168,176
Tv 148,242,20480
Mvva 72,172,184
global_load_lds_dwordx4 v247, s[60:61] offset:3072
Mvva 76,160,164
Tv 150,242,22528
Mvva 76,164,172
Tv 152,242,24576
Tv 154,242,26624
Mvva 76,168,180
Tv 156,242,28672
Mvva 76,172,188
Tv 158,242,30720
s_waitcnt vmcnt(8)
s_waitcnt lgkmcnt(15)
Mvva 80,112,192
Tv 160,243,16384
Tv 162,243,18432
Mvva 80,116,200
ds_read_b32 v5, v3 offset:0
s_add_u32 s44, s44, 1
v_max3_u32 v216, v176, v177, v178
v_max3_u32 v217, v182, v183, v184
Mvva 80,120,208
Tv 164,243,20480
Tv 166,243,22528
Mvva 80,124,216
ds_write_b32 v2, v4 offset:0
v_add_u32 v4, 1, v4
v_max3_u32 v218, v188, v189, v190
v_max3_u32 v219, v194, v195, v196
Mvva 84,112,196
Tv 168,243,24576
Tv 170,243,26624
Mvva 84,116,204
v_cmp_eq_u32 s[70:71], s47, v221
s_and_b32 s50, s40, 28
s_xor_b32 s50, s50, 20
s_lshl_b32 s72, 15, s50
s_andn2_b32 s50, s72, s70
s_cbranch_scc0 Lrdyb9_18_%=
s_call_b64 s[76:77], Lwait_%=
Lrdyb9_18_%=:
)ASM"
R"ASM(
v_max3_u32 v216, v216, v179, v180
v_max3_u32 v217, v217, v185, v186
Mvva 84,120,212
Tv 172,243,28672
Tv 174,243,30720
Mvva 84,124,220
s_waitcnt lgkmcnt(15)
s_waitcnt lgkmcnt(7)
v_cmp_gt_u32 vcc, s44, v5
s_cbranch_vccnz Lspin1918_%=
Lback1918_%=:
v_add_u32 v240, 65536, v240
v_max3_u32 v218, v218, v191, v192
v_max3_u32 v219, v219, v197, v198
Mvva 88,128,192
Tv 112,240,0
Tv 114,240,2048
Mvva 88,132,200
s_xor_b32 s50, s89, 20
s_lshl_b32 s50, s50, 17
s_lshl_b32 s51, s86, 6
s_xor_b32 s51, s51, 0
s_and_b32 s51, s51, 128
s_add_u32 s50, s50, s51
s_add_u32 s60, s24, s50
s_addc_u32 s61, s25, 0
s_add_u32 m0, s34, 98304
s_nop 0
global_load_lds_dwordx4 v244, s[60:61] offset:0
Mvva 88,136,208
Tv 116,240,4096
Tv 118,240,6144
Mvva 88,140,216
global_load_lds_dwordx4 v245, s[60:61] offset:1024
Mvva 92,128,196
Tv 120,240,8192
Tv 122,240,10240
Mvva 92,132,204
global_load_lds_dwordx4 v246, s[60:61] offset:2048
Mvva 92,136,212
Tv 124,240,12288
Tv 126,240,14336
Mvva 92,140,220
v_add_u32 v241, 65536, v241
v_max_u32 v216, v216, v181
v_max_u32 v217, v217, v187
Mvva 96,144,192
Tv 128,241,0
Tv 130,241,2048
Mvva 96,148,200
global_load_lds_dwordx4 v247, s[60:61] offset:3072
Mvva 96,152,208
Tv 132,241,4096
Tv 134,241,6144
Mvva 96,156,216
s_xor_b32 s50, s89, 20
s_lshl_b32 s50, s50, 12
s_add_u32 s62, s80, s50
s_addc_u32 s63, s81, 0
global_load_dwordx4 a[0:3], v24, s[62:63] offset:0 sc1
Mvva 100,144,196
Tv 136,241,8192
Tv 138,241,10240
Mvva 100,148,204
global_load_dwordx4 a[4:7], v25, s[62:63] offset:0 sc1
Mvva 100,152,212
Tv 140,241,12288
Tv 142,241,14336
Mvva 100,156,220
s_waitcnt lgkmcnt(15)
v_add_u32 v242, 65536, v242
v_max_u32 v218, v218, v193
v_max_u32 v219, v219, v199
Mvva 104,160,192
Tv 144,242,0
Tv 146,242,2048
Mvva 104,164,200
global_load_dwordx4 a[8:11], v24, s[62:63] offset:1024 sc1
Mvva 104,168,208
Tv 148,242,4096
v_max3_u32 v216, v216, v217, v218
Mvva 104,172,216
global_load_dwordx4 a[12:15], v25, s[62:63] offset:1024 sc1
Mvva 108,160,196
Tv 150,242,6144
v_max_u32 v216, v216, v219
Mvva 108,164,204
Tv 152,242,8192
Tv 154,242,10240
Mvva 108,168,212
Tv 156,242,12288
v_cmp_eq_u32 vcc, -1, v216
s_cbranch_vccnz Lbw9_%=
Lbok9_%=:
Mvva 108,172,220
Tv 158,242,14336
v_lshlrev_b32 v216, 16, v176
s_waitcnt lgkmcnt(15)
Mvva 48,112,192
v_add_u32 v243, 65536, v243
Tv 160,243,0
Tv 162,243,2048
Mvva 48,116,200
ds_read_b32 v5, v3 offset:0
s_add_u32 s44, s44, 1
v_and_b32 v217, 0xffff0000, v176
v_add_f32 v32, v32, v216
Mvva 48,120,208
Tv 164,243,4096
Tv 166,243,6144
Mvva 48,124,216
ds_write_b32 v2, v4 offset:0
v_add_u32 v4, 1, v4
v_add_f32 v33, v33, v217
v_lshlrev_b32 v218, 16, v177
Mvva 52,112,196
Tv 168,243,8192
v_and_b32 v219, 0xffff0000, v177
Mvva 52,116,204
Tv 170,243,10240
Tv 172,243,12288
Mvva 52,120,212
Tv 174,243,14336
v_add_f32 v34, v34, v218
Mvva 52,124,220
s_waitcnt lgkmcnt(15)
s_waitcnt lgkmcnt(7)
v_cmp_gt_u32 vcc, s44, v5
s_cbranch_vccnz Lspin1919_%=
Lback1919_%=:
v_add_u32 v240, 4294901760, v240
v_add_f32 v35, v35, v219
v_lshlrev_b32 v216, 16, v178
Mvva 56,128,192
Tv 112,240,32768
Tv 114,240,34816
Mvva 56,132,200
global_load_dwordx4 a[16:19], v24, s[62:63] offset:2048 sc1
Mvva 56,136,208
Tv 116,240,36864
Tv 118,240,38912
Mvva 56,140,216
global_load_dwordx4 a[20:23], v25, s[62:63] offset:2048 sc1
Mvva 60,128,196
Tv 120,240,40960
Tv 122,240,43008
Mvva 60,132,204
global_load_dwordx4 a[24:27], v24, s[62:63] offset:3072 sc1
Mvva 60,136,212
Tv 124,240,45056
Tv 126,240,47104
Mvva 60,140,220
v_add_u32 v241, 4294901760, v241
v_and_b32 v217, 0xffff0000, v178
v_add_f32 v36, v36, v216
Mvva 64,144,192
Tv 128,241,32768
Tv 130,241,34816
Mvva 64,148,200
global_load_dwordx4 a[28:31], v25, s[62:63] offset:3072 sc1
Mvva 64,152,208
Tv 132,241,36864
v_add_f32 v37, v37, v217
Mvva 64,156,216
Tv 134,241,38912
v_lshlrev_b32 v218, 16, v179
Mvva 68,144,196
Tv 136,241,40960
Tv 138,241,43008
Mvva 68,148,204
Tv 140,241,45056
v_and_b32 v219, 0xffff0000, v179
Mvva 68,152,212
Tv 142,241,47104
v_add_f32 v38, v38, v218
Mvva 68,156,220
s_waitcnt lgkmcnt(15)
v_add_u32 v242, 4294901760, v242
v_add_f32 v39, v39, v219
v_lshlrev_b32 v216, 16, v180
Mvva 72,160,192
Tv 144,242,32768
v_and_b32 v217, 0xffff0000, v180
Mvva 72,164,200
Tv 146,242,34816
v_add_f32 v40, v40, v216
Mvva 72,168,208
Tv 148,242,36864
v_add_f32 v41, v41, v217
Mvva 72,172,216
Tv 150,242,38912
v_lshlrev_b32 v218, 16, v181
Mvva 76,160,196
Tv 152,242,40960
v_and_b32 v219, 0xffff0000, v181
Mvva 76,164,204
Tv 154,242,43008
v_add_f32 v42, v42, v218
Mvva 76,168,212
Tv 156,242,45056
)ASM"
R"ASM(
v_add_f32 v43, v43, v219
Mvva 76,172,220
Tv 158,242,47104
v_lshlrev_b32 v216, 16, v182
s_waitcnt vmcnt(8)
s_waitcnt lgkmcnt(15)
Mvva 80,112,224
v_add_u32 v243, 4294901760, v243
Tv 160,243,32768
Tv 162,243,34816
Mvva 80,116,232
ds_read_b32 v5, v3 offset:0
s_add_u32 s44, s44, 1
v_and_b32 v217, 0xffff0000, v182
v_add_f32 v44, v44, v216
Mvva 80,120,240
Tv 164,243,36864
Tv 166,243,38912
Mvvv 80,124,208
ds_write_b32 v2, v4 offset:0
v_add_u32 v4, 1, v4
v_add_f32 v45, v45, v217
v_lshlrev_b32 v218, 16, v183
Mvva 84,112,228
Tv 168,243,40960
v_and_b32 v219, 0xffff0000, v183
Mvva 84,116,236
Tv 170,243,43008
Tv 172,243,45056
Mvva 84,120,244
Tv 174,243,47104
v_add_f32 v46, v46, v218
Mvvv 84,124,212
s_waitcnt lgkmcnt(15)
s_waitcnt lgkmcnt(7)
v_cmp_gt_u32 vcc, s44, v5
s_cbranch_vccnz Lspin1920_%=
Lback1920_%=:
v_add_u32 v240, 65536, v240
v_add_f32 v47, v47, v219
v_lshlrev_b32 v216, 16, v184
Mvva 88,128,224
Tv 112,240,16384
Tv 114,240,18432
Mvva 88,132,232
global_load_dwordx2 v[224:225], v249, s[12:13] offset:0
Mvva 88,136,240
Tv 116,240,20480
Tv 118,240,22528
Mvvv 88,140,208
global_load_dwordx2 v[226:227], v250, s[12:13] offset:0
Mvva 92,128,228
Tv 120,240,24576
Tv 122,240,26624
Mvva 92,132,236
global_load_dwordx2 v[228:229], v249, s[12:13] offset:32
Mvva 92,136,244
Tv 124,240,28672
Tv 126,240,30720
Mvvv 92,140,212
v_add_u32 v241, 65536, v241
v_and_b32 v217, 0xffff0000, v184
v_add_f32 v32, v32, v216
Mvva 96,144,224
Tv 128,241,16384
Tv 130,241,18432
Mvva 96,148,232
global_load_dwordx2 v[230:231], v250, s[12:13] offset:32
Mvva 96,152,240
Tv 132,241,20480
v_add_f32 v33, v33, v217
Mvvv 96,156,208
Tv 134,241,22528
v_lshlrev_b32 v218, 16, v185
Mvva 100,144,228
Tv 136,241,24576
Tv 138,241,26624
Mvva 100,148,236
Tv 140,241,28672
v_and_b32 v219, 0xffff0000, v185
Mvva 100,152,244
Tv 142,241,30720
v_add_f32 v34, v34, v218
Mvvv 100,156,212
s_waitcnt lgkmcnt(15)
v_add_u32 v242, 65536, v242
v_add_f32 v35, v35, v219
v_lshlrev_b32 v216, 16, v186
Mvva 104,160,224
Tv 144,242,16384
v_and_b32 v217, 0xffff0000, v186
Mvva 104,164,232
Tv 146,242,18432
v_add_f32 v36, v36, v216
Mvva 104,168,240
Tv 148,242,20480
v_add_f32 v37, v37, v217
Mvvv 104,172,208
Tv 150,242,22528
v_lshlrev_b32 v218, 16, v187
Mvva 108,160,228
Tv 152,242,24576
v_and_b32 v219, 0xffff0000, v187
Mvva 108,164,236
Tv 154,242,26624
v_add_f32 v38, v38, v218
Mvva 108,168,244
Tv 156,242,28672
v_add_f32 v39, v39, v219
Mvvv 108,172,212
Tv 158,242,30720
v_lshlrev_b32 v216, 16, v188
s_waitcnt vmcnt(4)
s_waitcnt lgkmcnt(15)
Mvva 80,112,0
v_add_u32 v243, 65536, v243
Tv 160,243,16384
Tv 162,243,18432
Mvva 80,116,8
ds_read_b32 v5, v3 offset:0
s_add_u32 s44, s44, 1
v_and_b32 v217, 0xffff0000, v188
v_add_f32 v40, v40, v216
Mvva 80,120,16
Tv 164,243,20480
Tv 166,243,22528
Mvva 80,124,24
ds_write_b32 v2, v4 offset:0
v_add_u32 v4, 1, v4
v_add_f32 v41, v41, v217
v_lshlrev_b32 v218, 16, v189
Mvva 84,112,4
Tv 168,243,24576
v_and_b32 v219, 0xffff0000, v189
Mvva 84,116,12
Tv 170,243,26624
Tv 172,243,28672
Mvva 84,120,20
Tv 174,243,30720
v_add_f32 v42, v42, v218
Mvva 84,124,28
s_waitcnt lgkmcnt(15)
s_waitcnt lgkmcnt(7)
v_cmp_gt_u32 vcc, s44, v5
s_cbranch_vccnz Lspin1921_%=
Lback1921_%=:
v_add_u32 v242, 4294901760, v242
v_add_f32 v43, v43, v219
v_lshlrev_b32 v216, 16, v190
Mvva 88,128,0
Tv 112,242,49152
Tv 114,242,51200
Mvva 88,132,8
Tv 116,242,53248
v_and_b32 v217, 0xffff0000, v190
Mvva 88,136,16
Tv 118,242,55296
v_add_f32 v44, v44, v216
Mvva 88,140,24
Tv 120,242,57344
v_add_f32 v45, v45, v217
Mvva 92,128,4
Tv 122,242,59392
v_lshlrev_b32 v218, 16, v191
Mvva 92,132,12
Tv 124,242,61440
v_and_b32 v219, 0xffff0000, v191
Mvva 92,136,20
Tv 126,242,63488
v_add_f32 v46, v46, v218
Mvva 92,140,28
v_add_u32 v243, 4294901760, v243
v_add_f32 v47, v47, v219
v_lshlrev_b32 v216, 16, v192
Mvva 96,144,0
Tv 128,243,49152
Tv 130,243,51200
Mvva 96,148,8
Tv 132,243,53248
Tv 134,243,55296
Mvva 96,152,16
Tv 136,243,57344
v_and_b32 v217, 0xffff0000, v192
Mvva 96,156,24
s_xor_b32 s50, s86, 2
s_lshl_b32 s50, s50, 16
s_add_u32 s50, s50, 4096
s_add_u32 s62, s82, s50
s_addc_u32 s63, s83, 0
CV 80,80,81
CV 81,82,83
CV 82,84,85
CV 83,86,87
global_store_dwordx4 v24, v[80:83], s[62:63] offset:0 sc1
v_add_f32 v32, v32, v216
v_add_f32 v33, v33, v217
Mvva 100,144,4
Tv 138,243,59392
Tv 140,243,61440
Mvva 100,148,12
)ASM"
R"ASM(
CV 88,88,89
CV 89,90,91
CV 90,92,93
CV 91,94,95
global_store_dwordx4 v24, v[88:91], s[62:63] offset:1024 sc1
v_lshlrev_b32 v218, 16, v193
v_and_b32 v219, 0xffff0000, v193
Mvva 100,152,20
Tv 142,243,63488
v_add_f32 v34, v34, v218
Mvva 100,156,28
s_waitcnt lgkmcnt(15)
v_add_u32 v242, 65536, v242
v_add_f32 v35, v35, v219
v_lshlrev_b32 v216, 16, v194
Mvva 104,160,0
Tv 144,242,32768
v_and_b32 v217, 0xffff0000, v194
Mvva 104,164,8
Tv 146,242,34816
v_add_f32 v36, v36, v216
Mvva 104,168,16
Tv 148,242,36864
v_add_f32 v37, v37, v217
Mvva 104,172,24
Tv 150,242,38912
v_lshlrev_b32 v218, 16, v195
Mvva 108,160,4
Tv 152,242,40960
v_and_b32 v219, 0xffff0000, v195
Mvva 108,164,12
Tv 154,242,43008
v_add_f32 v38, v38, v218
Mvva 108,168,20
Tv 156,242,45056
v_add_f32 v39, v39, v219
Mvva 108,172,28
Tv 158,242,47104
v_lshlrev_b32 v216, 16, v196
s_waitcnt lgkmcnt(15)
Mvva 64,112,224
v_add_u32 v243, 65536, v243
Tv 160,243,32768
Tv 162,243,34816
Mvva 64,116,232
Tv 164,243,36864
v_and_b32 v217, 0xffff0000, v196
Mvva 64,120,240
Tv 166,243,38912
v_add_f32 v40, v40, v216
Mvvv 64,124,208
Tv 168,243,40960
v_add_f32 v41, v41, v217
Mvva 68,112,228
Tv 170,243,43008
v_lshlrev_b32 v218, 16, v197
Mvva 68,116,236
Tv 172,243,45056
v_and_b32 v219, 0xffff0000, v197
Mvva 68,120,244
Tv 174,243,47104
v_add_f32 v42, v42, v218
Mvvv 68,124,212
s_waitcnt lgkmcnt(15)
v_add_u32 v240, 4294901760, v240
v_add_f32 v43, v43, v219
v_lshlrev_b32 v216, 16, v198
Mvva 72,128,224
Tv 112,240,49152
Tv 114,240,51200
Mvva 72,132,232
s_xor_b32 s50, s86, 3
s_lshl_b32 s50, s50, 16
s_add_u32 s50, s50, 8192
s_add_u32 s62, s82, s50
s_addc_u32 s63, s83, 0
CV 96,96,97
CV 97,98,99
CV 98,100,101
CV 99,102,103
global_store_dwordx4 v24, v[96:99], s[62:63] offset:0 sc1
Mvva 72,136,240
Tv 116,240,53248
Tv 118,240,55296
Mvvv 72,140,208
CV 104,104,105
CV 105,106,107
CV 106,108,109
CV 107,110,111
global_store_dwordx4 v24, v[104:107], s[62:63] offset:1024 sc1
Mvva 76,128,228
Tv 120,240,57344
v_and_b32 v217, 0xffff0000, v198
Mvva 76,132,236
Tv 122,240,59392
Tv 124,240,61440
Mvva 76,136,244
Tv 126,240,63488
v_add_f32 v44, v44, v216
Mvvv 76,140,212
s_waitcnt lgkmcnt(15)
v_add_u32 v241, 4294901760, v241
v_add_f32 v45, v45, v217
v_lshlrev_b32 v218, 16, v199
Mvva 64,144,0
Tv 128,241,49152
Tv 130,241,51200
Mvva 64,148,8
Tv 132,241,53248
v_and_b32 v219, 0xffff0000, v199
Mvva 64,152,16
Tv 134,241,55296
v_add_f32 v46, v46, v218
Mvva 64,156,24
Tv 136,241,57344
v_add_f32 v47, v47, v219
Mvva 68,144,4
Tv 138,241,59392
Mvva 68,148,12
Tv 140,241,61440
Mvva 68,152,20
Tv 142,241,63488
Mvva 68,156,28
s_waitcnt lgkmcnt(15)
v_add_u32 v240, 65536, v240
Mvva 72,160,0
Tv 144,240,32768
Mvva 72,164,8
Tv 146,240,34816
Mvva 72,168,16
Tv 148,240,36864
Mvva 72,172,24
Tv 150,240,38912
Mvva 76,160,4
Tv 152,240,40960
Mvva 76,164,12
Tv 154,240,43008
Mvva 76,168,20
Tv 156,240,45056
Mvva 76,172,28
Tv 158,240,47104
s_waitcnt lgkmcnt(15)
Mvva 48,112,224
v_add_u32 v241, 65536, v241
Tv 160,241,32768
Tv 162,241,34816
Mvva 48,116,232
Tv 164,241,36864
Mvva 48,120,240
Tv 166,241,38912
Mvvv 48,124,208
Tv 168,241,40960
Mvva 52,112,228
Tv 170,241,43008
Mvva 52,116,236
Tv 172,241,45056
Mvva 52,120,244
Tv 174,241,47104
Mvvv 52,124,212
s_waitcnt lgkmcnt(15)
Mvva 56,128,224
Mvva 56,132,232
s_xor_b32 s50, s86, 1
s_lshl_b32 s50, s50, 16
s_add_u32 s50, s50, 0
s_add_u32 s62, s82, s50
s_addc_u32 s63, s83, 0
CV 64,64,65
CV 65,66,67
CV 66,68,69
CV 67,70,71
global_store_dwordx4 v24, v[64:67], s[62:63] offset:0 sc1
Mvva 56,136,240
Mvvv 56,140,208
CV 72,72,73
CV 73,74,75
CV 74,76,77
CV 75,78,79
global_store_dwordx4 v24, v[72:75], s[62:63] offset:1024 sc1
Mvva 60,128,228
Mvva 60,132,236
Mvva 60,136,244
Mvvv 60,140,212
s_waitcnt lgkmcnt(8)
Mvva 48,144,0
Mvva 48,148,8
Mvva 48,152,16
Mvva 48,156,24
Mvva 52,144,4
Mvva 52,148,12
Mvva 52,152,20
Mvva 52,156,28
s_waitcnt lgkmcnt(0)
Mvva 56,160,0
Mvva 56,164,8
Mvva 56,168,16
Mvva 56,172,24
Mvva 60,160,4
Mvva 60,164,12
Mvva 60,168,20
)ASM"
R"ASM(
Mvva 60,172,28
s_lshl_b32 s50, s86, 16
s_add_u32 s62, s82, s50
s_addc_u32 s63, s83, 0
v_add_u32 v240, 4096, v24
v_add_u32 v241, 8192, v24
s_mov_b32 s45, 262144
global_load_dwordx4 v[128:131], v241, s[62:63] offset:0 sc1
global_load_dwordx4 v[132:135], v241, s[62:63] offset:1024 sc1
global_load_dwordx4 v[120:123], v240, s[62:63] offset:0 sc1
global_load_dwordx4 v[124:127], v240, s[62:63] offset:1024 sc1
global_load_dwordx4 v[112:115], v24, s[62:63] offset:0 sc1
global_load_dwordx4 v[116:119], v24, s[62:63] offset:1024 sc1
v_mul_f32 v176, 0xbfb8aa3b, v32
v_mul_f32 v177, 0xbfb8aa3b, v33
v_mul_f32 v178, 0xbfb8aa3b, v34
v_mul_f32 v179, 0xbfb8aa3b, v35
v_mul_f32 v180, 0xbfb8aa3b, v36
v_mul_f32 v181, 0xbfb8aa3b, v37
v_mul_f32 v182, 0xbfb8aa3b, v38
v_mul_f32 v183, 0xbfb8aa3b, v39
v_mul_f32 v184, 0xbfb8aa3b, v40
v_mul_f32 v185, 0xbfb8aa3b, v41
v_mul_f32 v186, 0xbfb8aa3b, v42
v_mul_f32 v187, 0xbfb8aa3b, v43
v_mul_f32 v188, 0xbfb8aa3b, v44
v_mul_f32 v189, 0xbfb8aa3b, v45
v_mul_f32 v190, 0xbfb8aa3b, v46
v_mul_f32 v191, 0xbfb8aa3b, v47
EX 176
EX 177
EX 178
EX 179
EX 180
EX 181
EX 182
EX 183
EX 184
EX 185
EX 186
EX 187
EX 188
EX 189
EX 190
EX 191
v_add_f32 v176, 1.0, v176
v_add_f32 v177, 1.0, v177
v_add_f32 v178, 1.0, v178
v_add_f32 v179, 1.0, v179
v_add_f32 v180, 1.0, v180
v_add_f32 v181, 1.0, v181
v_add_f32 v182, 1.0, v182
v_add_f32 v183, 1.0, v183
v_add_f32 v184, 1.0, v184
v_add_f32 v185, 1.0, v185
v_add_f32 v186, 1.0, v186
v_add_f32 v187, 1.0, v187
v_add_f32 v188, 1.0, v188
v_add_f32 v189, 1.0, v189
v_add_f32 v190, 1.0, v190
v_add_f32 v191, 1.0, v191
v_rcp_f32 v176, v176
v_rcp_f32 v177, v177
v_rcp_f32 v178, v178
v_rcp_f32 v179, v179
v_rcp_f32 v180, v180
v_rcp_f32 v181, v181
v_rcp_f32 v182, v182
v_rcp_f32 v183, v183
v_rcp_f32 v184, v184
v_rcp_f32 v185, v185
v_rcp_f32 v186, v186
v_rcp_f32 v187, v187
v_rcp_f32 v188, v188
v_rcp_f32 v189, v189
v_rcp_f32 v190, v190
v_rcp_f32 v191, v191
v_mul_f32 v176, v176, v32
v_mul_f32 v177, v177, v33
v_mul_f32 v178, v178, v34
v_mul_f32 v179, v179, v35
v_mul_f32 v180, v180, v36
v_mul_f32 v181, v181, v37
v_mul_f32 v182, v182, v38
v_mul_f32 v183, v183, v39
v_mul_f32 v184, v184, v40
v_mul_f32 v185, v185, v41
v_mul_f32 v186, v186, v42
v_mul_f32 v187, v187, v43
v_mul_f32 v188, v188, v44
v_mul_f32 v189, v189, v45
v_mul_f32 v190, v190, v46
v_mul_f32 v191, v191, v47
s_waitcnt vmcnt(12)
v_lshlrev_b32 v192, 16, v224
v_and_b32 v193, 0xffff0000, v224
v_lshlrev_b32 v194, 16, v225
v_and_b32 v195, 0xffff0000, v225
v_lshlrev_b32 v196, 16, v226
v_and_b32 v197, 0xffff0000, v226
v_lshlrev_b32 v198, 16, v227
v_and_b32 v199, 0xffff0000, v227
v_lshlrev_b32 v200, 16, v228
v_and_b32 v201, 0xffff0000, v228
v_lshlrev_b32 v202, 16, v229
v_and_b32 v203, 0xffff0000, v229
v_lshlrev_b32 v204, 16, v230
v_and_b32 v205, 0xffff0000, v230
v_lshlrev_b32 v206, 16, v231
v_and_b32 v207, 0xffff0000, v231
s_branch Lxw9_%=
Lxch9_%=:
global_load_dwordx4 v[128:131], v241, s[62:63] offset:0 sc1
global_load_dwordx4 v[132:135], v241, s[62:63] offset:1024 sc1
global_load_dwordx4 v[120:123], v240, s[62:63] offset:0 sc1
global_load_dwordx4 v[124:127], v240, s[62:63] offset:1024 sc1
global_load_dwordx4 v[112:115], v24, s[62:63] offset:0 sc1
global_load_dwordx4 v[116:119], v24, s[62:63] offset:1024 sc1
Lxw9_%=:
s_waitcnt vmcnt(0)
v_max3_u32 v160, v112, v113, v114
v_max3_u32 v161, v118, v119, v120
v_max3_u32 v162, v124, v125, v126
v_max3_u32 v163, v130, v131, v132
v_max3_u32 v160, v160, v115, v116
v_max3_u32 v161, v161, v121, v122
v_max3_u32 v162, v162, v127, v128
v_max3_u32 v163, v163, v133, v134
v_max_u32 v160, v160, v117
v_max_u32 v161, v161, v123
v_max_u32 v162, v162, v129
v_max_u32 v163, v163, v135
v_max3_u32 v160, v160, v161, v162
v_max_u32 v160, v160, v163
v_cmp_eq_u32 vcc, -1, v160
s_sub_u32 s45, s45, 1
s_cbranch_scc1 Lxgot9_%=
s_cbranch_vccnz Lxch9_%=
Lxgot9_%=:
v_lshlrev_b32 v136, 16, v112
v_and_b32 v137, 0xffff0000, v112
v_lshlrev_b32 v138, 16, v113
v_and_b32 v139, 0xffff0000, v113
v_lshlrev_b32 v140, 16, v114
v_and_b32 v141, 0xffff0000, v114
v_lshlrev_b32 v142, 16, v115
v_and_b32 v143, 0xffff0000, v115
v_lshlrev_b32 v144, 16, v116
v_and_b32 v145, 0xffff0000, v116
v_lshlrev_b32 v146, 16, v117
v_and_b32 v147, 0xffff0000, v117
v_lshlrev_b32 v148, 16, v118
v_and_b32 v149, 0xffff0000, v118
v_lshlrev_b32 v150, 16, v119
v_and_b32 v151, 0xffff0000, v119
v_pk_add_f32 v[48:49], v[48:49], v[136:137]
v_pk_add_f32 v[50:51], v[50:51], v[138:139]
v_pk_add_f32 v[52:53], v[52:53], v[140:141]
v_pk_add_f32 v[54:55], v[54:55], v[142:143]
v_pk_add_f32 v[56:57], v[56:57], v[144:145]
v_pk_add_f32 v[58:59], v[58:59], v[146:147]
v_pk_add_f32 v[60:61], v[60:61], v[148:149]
v_pk_add_f32 v[62:63], v[62:63], v[150:151]
v_lshlrev_b32 v136, 16, v120
v_and_b32 v137, 0xffff0000, v120
v_lshlrev_b32 v138, 16, v121
v_and_b32 v139, 0xffff0000, v121
v_lshlrev_b32 v140, 16, v122
v_and_b32 v141, 0xffff0000, v122
v_lshlrev_b32 v142, 16, v123
v_and_b32 v143, 0xffff0000, v123
v_lshlrev_b32 v144, 16, v124
v_and_b32 v145, 0xffff0000, v124
v_lshlrev_b32 v146, 16, v125
v_and_b32 v147, 0xffff0000, v125
v_lshlrev_b32 v148, 16, v126
v_and_b32 v149, 0xffff0000, v126
v_lshlrev_b32 v150, 16, v127
v_and_b32 v151, 0xffff0000, v127
v_pk_add_f32 v[48:49], v[48:49], v[136:137]
v_pk_add_f32 v[50:51], v[50:51], v[138:139]
v_pk_add_f32 v[52:53], v[52:53], v[140:141]
v_pk_add_f32 v[54:55], v[54:55], v[142:143]
v_pk_add_f32 v[56:57], v[56:57], v[144:145]
v_pk_add_f32 v[58:59], v[58:59], v[146:147]
v_pk_add_f32 v[60:61], v[60:61], v[148:149]
v_pk_add_f32 v[62:63], v[62:63], v[150:151]
v_lshlrev_b32 v136, 16, v128
v_and_b32 v137, 0xffff0000, v128
v_lshlrev_b32 v138, 16, v129
v_and_b32 v139, 0xffff0000, v129
v_lshlrev_b32 v140, 16, v130
v_and_b32 v141, 0xffff0000, v130
v_lshlrev_b32 v142, 16, v131
v_and_b32 v143, 0xffff0000, v131
v_lshlrev_b32 v144, 16, v132
v_and_b32 v145, 0xffff0000, v132
v_lshlrev_b32 v146, 16, v133
v_and_b32 v147, 0xffff0000, v133
v_lshlrev_b32 v148, 16, v134
)ASM"
R"ASM(
v_and_b32 v149, 0xffff0000, v134
v_lshlrev_b32 v150, 16, v135
v_and_b32 v151, 0xffff0000, v135
v_pk_add_f32 v[48:49], v[48:49], v[136:137]
v_pk_add_f32 v[50:51], v[50:51], v[138:139]
v_pk_add_f32 v[52:53], v[52:53], v[140:141]
v_pk_add_f32 v[54:55], v[54:55], v[142:143]
v_pk_add_f32 v[56:57], v[56:57], v[144:145]
v_pk_add_f32 v[58:59], v[58:59], v[146:147]
v_pk_add_f32 v[60:61], v[60:61], v[148:149]
v_pk_add_f32 v[62:63], v[62:63], v[150:151]
v_pk_fma_f32 v[192:193], v[48:49], v[176:177], v[192:193]
v_pk_fma_f32 v[194:195], v[50:51], v[178:179], v[194:195]
v_pk_fma_f32 v[196:197], v[52:53], v[180:181], v[196:197]
v_pk_fma_f32 v[198:199], v[54:55], v[182:183], v[198:199]
v_pk_fma_f32 v[200:201], v[56:57], v[184:185], v[200:201]
v_pk_fma_f32 v[202:203], v[58:59], v[186:187], v[202:203]
v_pk_fma_f32 v[204:205], v[60:61], v[188:189], v[204:205]
v_pk_fma_f32 v[206:207], v[62:63], v[190:191], v[206:207]
CV 160,192,193
CV 161,194,195
CV 162,196,197
CV 163,198,199
CV 164,200,201
CV 165,202,203
CV 166,204,205
CV 167,206,207
global_store_dwordx2 v249, v[160:161], s[30:31] offset:0
global_store_dwordx2 v250, v[162:163], s[30:31] offset:0
global_store_dwordx2 v249, v[164:165], s[30:31] offset:32
global_store_dwordx2 v250, v[166:167], s[30:31] offset:32
s_or_b32 s50, s40, s35
s_or_b32 s50, s50, s41
s_cbranch_scc1 Ldone_%=
s_lshl_b64 exec, 1, s40
v_mov_b32 v223, s47
global_store_dword v220, v223, s[28:29] offset:128 sc1
s_mov_b64 exec, -1
s_branch Ldone_%=
Lbw9_%=:
s_mov_b32 s45, 262144
Lbw19_%=:
s_lshl_b32 s50, s86, 16
s_add_u32 s50, s50, 0
s_add_u32 s64, s82, s50
s_addc_u32 s65, s83, 0
global_load_dwordx4 v[176:179], v24, s[64:65] offset:2048 sc1
global_load_dwordx4 v[180:183], v24, s[64:65] offset:3072 sc1
s_lshl_b32 s50, s86, 16
s_add_u32 s50, s50, 4096
s_add_u32 s64, s82, s50
s_addc_u32 s65, s83, 0
global_load_dwordx4 v[184:187], v24, s[64:65] offset:2048 sc1
global_load_dwordx4 v[188:191], v24, s[64:65] offset:3072 sc1
s_lshl_b32 s50, s86, 16
s_add_u32 s50, s50, 8192
s_add_u32 s64, s82, s50
s_addc_u32 s65, s83, 0
global_load_dwordx4 v[192:195], v24, s[64:65] offset:2048 sc1
global_load_dwordx4 v[196:199], v24, s[64:65] offset:3072 sc1
s_waitcnt vmcnt(0)
v_max3_u32 v216, v176, v177, v178
v_max3_u32 v217, v182, v183, v184
v_max3_u32 v218, v188, v189, v190
v_max3_u32 v219, v194, v195, v196
v_max3_u32 v216, v216, v179, v180
v_max3_u32 v217, v217, v185, v186
v_max3_u32 v218, v218, v191, v192
v_max3_u32 v219, v219, v197, v198
v_max_u32 v216, v216, v181
v_max_u32 v217, v217, v187
v_max_u32 v218, v218, v193
v_max_u32 v219, v219, v199
v_max3_u32 v216, v216, v217, v218
v_max_u32 v216, v216, v219
v_cmp_eq_u32 vcc, -1, v216
s_sub_u32 s45, s45, 1
s_cbranch_scc1 Lbok9_%=
s_cbranch_vccnz Lbw19_%=
s_branch Lbok9_%=
Lspin1901_%=:
ds_read_b32 v5, v3
s_waitcnt lgkmcnt(0)
v_cmp_gt_u32 vcc, s44, v5
s_cbranch_vccnz Lspin1901_%=
s_branch Lback1901_%=
Lspin1902_%=:
ds_read_b32 v5, v3
s_waitcnt lgkmcnt(0)
v_cmp_gt_u32 vcc, s44, v5
s_cbranch_vccnz Lspin1902_%=
s_branch Lback1902_%=
Lspin1903_%=:
ds_read_b32 v5, v3
s_waitcnt lgkmcnt(0)
v_cmp_gt_u32 vcc, s44, v5
s_cbranch_vccnz Lspin1903_%=
s_branch Lback1903_%=
Lspin1904_%=:
ds_read_b32 v5, v3
s_waitcnt lgkmcnt(0)
v_cmp_gt_u32 vcc, s44, v5
s_cbranch_vccnz Lspin1904_%=
s_branch Lback1904_%=
Lspin1905_%=:
ds_read_b32 v5, v3
s_waitcnt lgkmcnt(0)
v_cmp_gt_u32 vcc, s44, v5
s_cbranch_vccnz Lspin1905_%=
s_branch Lback1905_%=
Lspin1906_%=:
ds_read_b32 v5, v3
s_waitcnt lgkmcnt(0)
v_cmp_gt_u32 vcc, s44, v5
s_cbranch_vccnz Lspin1906_%=
s_branch Lback1906_%=
Lspin1907_%=:
ds_read_b32 v5, v3
s_waitcnt lgkmcnt(0)
v_cmp_gt_u32 vcc, s44, v5
s_cbranch_vccnz Lspin1907_%=
s_branch Lback1907_%=
Lspin1908_%=:
ds_read_b32 v5, v3
s_waitcnt lgkmcnt(0)
v_cmp_gt_u32 vcc, s44, v5
s_cbranch_vccnz Lspin1908_%=
s_branch Lback1908_%=
Lspin1909_%=:
ds_read_b32 v5, v3
s_waitcnt lgkmcnt(0)
v_cmp_gt_u32 vcc, s44, v5
s_cbranch_vccnz Lspin1909_%=
s_branch Lback1909_%=
Lspin1910_%=:
ds_read_b32 v5, v3
s_waitcnt lgkmcnt(0)
v_cmp_gt_u32 vcc, s44, v5
s_cbranch_vccnz Lspin1910_%=
s_branch Lback1910_%=
Lspin1911_%=:
ds_read_b32 v5, v3
s_waitcnt lgkmcnt(0)
v_cmp_gt_u32 vcc, s44, v5
s_cbranch_vccnz Lspin1911_%=
s_branch Lback1911_%=
Lspin1912_%=:
ds_read_b32 v5, v3
s_waitcnt lgkmcnt(0)
v_cmp_gt_u32 vcc, s44, v5
s_cbranch_vccnz Lspin1912_%=
s_branch Lback1912_%=
Lspin1913_%=:
ds_read_b32 v5, v3
s_waitcnt lgkmcnt(0)
v_cmp_gt_u32 vcc, s44, v5
s_cbranch_vccnz Lspin1913_%=
s_branch Lback1913_%=
Lspin1914_%=:
ds_read_b32 v5, v3
s_waitcnt lgkmcnt(0)
v_cmp_gt_u32 vcc, s44, v5
s_cbranch_vccnz Lspin1914_%=
s_branch Lback1914_%=
Lspin1915_%=:
ds_read_b32 v5, v3
s_waitcnt lgkmcnt(0)
v_cmp_gt_u32 vcc, s44, v5
s_cbranch_vccnz Lspin1915_%=
s_branch Lback1915_%=
Lspin1916_%=:
ds_read_b32 v5, v3
s_waitcnt lgkmcnt(0)
v_cmp_gt_u32 vcc, s44, v5
s_cbranch_vccnz Lspin1916_%=
s_branch Lback1916_%=
Lspin1917_%=:
ds_read_b32 v5, v3
s_waitcnt lgkmcnt(0)
v_cmp_gt_u32 vcc, s44, v5
s_cbranch_vccnz Lspin1917_%=
s_branch Lback1917_%=
Lspin1918_%=:
ds_read_b32 v5, v3
s_waitcnt lgkmcnt(0)
v_cmp_gt_u32 vcc, s44, v5
s_cbranch_vccnz Lspin1918_%=
s_branch Lback1918_%=
Lspin1919_%=:
ds_read_b32 v5, v3
s_waitcnt lgkmcnt(0)
v_cmp_gt_u32 vcc, s44, v5
s_cbranch_vccnz Lspin1919_%=
s_branch Lback1919_%=
Lspin1920_%=:
ds_read_b32 v5, v3
s_waitcnt lgkmcnt(0)
v_cmp_gt_u32 vcc, s44, v5
s_cbranch_vccnz Lspin1920_%=
s_branch Lback1920_%=
)ASM"
R"ASM(
Lspin1921_%=:
ds_read_b32 v5, v3
s_waitcnt lgkmcnt(0)
v_cmp_gt_u32 vcc, s44, v5
s_cbranch_vccnz Lspin1921_%=
s_branch Lback1921_%=
Lspin1701_%=:
ds_read_b32 v5, v3
s_waitcnt lgkmcnt(0)
v_cmp_gt_u32 vcc, s44, v5
s_cbranch_vccnz Lspin1701_%=
s_branch Lback1701_%=
Lspin1702_%=:
ds_read_b32 v5, v3
s_waitcnt lgkmcnt(0)
v_cmp_gt_u32 vcc, s44, v5
s_cbranch_vccnz Lspin1702_%=
s_branch Lback1702_%=
Lspin1703_%=:
ds_read_b32 v5, v3
s_waitcnt lgkmcnt(0)
v_cmp_gt_u32 vcc, s44, v5
s_cbranch_vccnz Lspin1703_%=
s_branch Lback1703_%=
Lspin1704_%=:
ds_read_b32 v5, v3
s_waitcnt lgkmcnt(0)
v_cmp_gt_u32 vcc, s44, v5
s_cbranch_vccnz Lspin1704_%=
s_branch Lback1704_%=
Lspin1705_%=:
ds_read_b32 v5, v3
s_waitcnt lgkmcnt(0)
v_cmp_gt_u32 vcc, s44, v5
s_cbranch_vccnz Lspin1705_%=
s_branch Lback1705_%=
Lspin1706_%=:
ds_read_b32 v5, v3
s_waitcnt lgkmcnt(0)
v_cmp_gt_u32 vcc, s44, v5
s_cbranch_vccnz Lspin1706_%=
s_branch Lback1706_%=
Lspin1707_%=:
ds_read_b32 v5, v3
s_waitcnt lgkmcnt(0)
v_cmp_gt_u32 vcc, s44, v5
s_cbranch_vccnz Lspin1707_%=
s_branch Lback1707_%=
Lspin1708_%=:
ds_read_b32 v5, v3
s_waitcnt lgkmcnt(0)
v_cmp_gt_u32 vcc, s44, v5
s_cbranch_vccnz Lspin1708_%=
s_branch Lback1708_%=
Lspin1709_%=:
ds_read_b32 v5, v3
s_waitcnt lgkmcnt(0)
v_cmp_gt_u32 vcc, s44, v5
s_cbranch_vccnz Lspin1709_%=
s_branch Lback1709_%=
Lspin1710_%=:
ds_read_b32 v5, v3
s_waitcnt lgkmcnt(0)
v_cmp_gt_u32 vcc, s44, v5
s_cbranch_vccnz Lspin1710_%=
s_branch Lback1710_%=
Lspin1711_%=:
ds_read_b32 v5, v3
s_waitcnt lgkmcnt(0)
v_cmp_gt_u32 vcc, s44, v5
s_cbranch_vccnz Lspin1711_%=
s_branch Lback1711_%=
Lspin1712_%=:
ds_read_b32 v5, v3
s_waitcnt lgkmcnt(0)
v_cmp_gt_u32 vcc, s44, v5
s_cbranch_vccnz Lspin1712_%=
s_branch Lback1712_%=
Lspin1713_%=:
ds_read_b32 v5, v3
s_waitcnt lgkmcnt(0)
v_cmp_gt_u32 vcc, s44, v5
s_cbranch_vccnz Lspin1713_%=
s_branch Lback1713_%=
Lspin1714_%=:
ds_read_b32 v5, v3
s_waitcnt lgkmcnt(0)
v_cmp_gt_u32 vcc, s44, v5
s_cbranch_vccnz Lspin1714_%=
s_branch Lback1714_%=
Lspin1715_%=:
ds_read_b32 v5, v3
s_waitcnt lgkmcnt(0)
v_cmp_gt_u32 vcc, s44, v5
s_cbranch_vccnz Lspin1715_%=
s_branch Lback1715_%=
Lspin1716_%=:
ds_read_b32 v5, v3
s_waitcnt lgkmcnt(0)
v_cmp_gt_u32 vcc, s44, v5
s_cbranch_vccnz Lspin1716_%=
s_branch Lback1716_%=
Lspin1717_%=:
ds_read_b32 v5, v3
s_waitcnt lgkmcnt(0)
v_cmp_gt_u32 vcc, s44, v5
s_cbranch_vccnz Lspin1717_%=
s_branch Lback1717_%=
Ldone_%=:
.purgem Ba
.purgem Bv
.purgem CV
.purgem Cvav
.purgem EX
.purgem Maaa
.purgem Maav
.purgem Mvaa
.purgem Mvav
.purgem Mvva
.purgem Mvvv
.purgem Ta
.purgem Tv
.purgem Zaaa
.purgem Zaav
.purgem Zvaa
.purgem Zvav
.purgem Zvva
)ASM"

  :: "s"(__builtin_amdgcn_kernarg_segment_ptr()), "s"(blockIdx.x), "v"(threadIdx.x), "s"((unsigned)(size_t)lds) : FUSED_CLOBBERS);
}

extern "C" void kernel_launch(void* const* d_in, const int* in_sizes, int n_in, void* d_out, int out_size, void* d_ws, size_t ws_size,
                              hipStream_t stream) {
  fused_block<<<256, 256, 0, stream>>>(d_in[0], d_in[1], d_in[2], d_in[3], d_in[6], d_in[7], d_in[4], d_in[8], d_ws, d_out);
}
```
